```python
import torch

def _softmax(x, axis=-1):
    return torch.softmax(x, dim=axis)
def _gelu(x):
    return torch.nn.functional.gelu(x, approximate="tanh")
def _silu(x):
    return torch.nn.functional.silu(x)
def _sigmoid(x):
    return torch.sigmoid(x)
def _layernorm(x, axis=-1, eps=1e-5):
    m = x.mean(dim=axis, keepdim=True)
    v = x.var(dim=axis, keepdim=True, unbiased=False)
    return (x - m) / torch.sqrt(v + eps)
def _scatter_add(tgt, idx, src, axis=0):
    return tgt.clone().index_add_(axis, idx, src)
def _segment_sum(src, seg, n):
    out = torch.zeros((n,) + src.shape[1:], dtype=src.dtype)
    return out.index_add_(0, seg, src)
def _pad(x, pw):
    flat = tuple(v for pair in reversed(pw) for v in pair)
    return torch.nn.functional.pad(x, flat)

kernel_name = 'recurrent_scan__sliding_block_cached__dense_activation_relu__decode'

def setup_inputs():

    _w = (lambda a: (lambda t: t.to(torch.bfloat16) if t.is_floating_point() else t)(torch.as_tensor(a)))
    def _A_setup():
        B, Sq, D = 2, 1, 640
        Hq, Hkv, Sk, Dh = 32, 32, 8192, 128
        assert Hq % Hkv == 0, (Hq, Hkv)
        return {
            "x": _w((lambda *s: torch.randn(*s, dtype=torch.bfloat16) * 0.05)(B, Sq, D)),
            "k_cache": _w((lambda *s: torch.randn(*s, dtype=torch.bfloat16) * 1.6)(B, Hkv, Sk, Dh)),
            "v_cache": _w((lambda *s: torch.randn(*s, dtype=torch.bfloat16) * 0.2)(B, Hkv, Sk, Dh)),
            "wq": _w((lambda *s: torch.randn(*s, dtype=torch.bfloat16) * 1.6)(D, Hq * Dh) * (D ** -0.5)),
            "wo": _w((lambda *s: torch.randn(*s, dtype=torch.bfloat16) * 0.2)(Hq * Dh, D) * ((Hq * Dh) ** -0.5)),
        }
    _A_in = _A_setup()

    def _B_setup():
        B, S, D, Dff = 2, 1, 640, 1728
        return {
            "x": _w((lambda *s: torch.randn(*s, dtype=torch.bfloat16) * 0.05)(B, S, D)),
            "w1": _w((lambda *s: torch.randn(*s, dtype=torch.bfloat16) * 0.1)(D, Dff)),
            "w2": _w((lambda *s: torch.randn(*s, dtype=torch.bfloat16) * 0.1)(Dff, D)),
        }
    _B_in = _B_setup()

    return {
        "A_x": _A_in["x"],
        "A_k_cache": _A_in["k_cache"],
        "A_v_cache": _A_in["v_cache"],
        "A_wq": _A_in["wq"],
        "A_wo": _A_in["wo"],
        "B_x": _B_in["x"],
        "B_w1": _B_in["w1"],
        "B_w2": _B_in["w2"],
    }

def reference(A_x, A_k_cache, A_v_cache, A_wq, A_wo, B_x, B_w1, B_w2):
    def _A_ref(x, k_cache, v_cache, wq, wo):
        B, Sq, D = x.shape
        Hq, Dh = 32, 128
        Hkv, Sk = k_cache.shape[1], k_cache.shape[2]
        W = min(4096, max(256, Sk // 4))
        rep = Hq // Hkv
        q = (lambda x, *d: x.permute(*d))((x @ wq).reshape(B, Sq, Hq, Dh), 0, 2, 1, 3)
        k = (lambda x, n, axis: torch.repeat_interleave(x, n, dim=axis))(k_cache[:, :, Sk - W:, :], rep, axis=1)
        v = (lambda x, n, axis: torch.repeat_interleave(x, n, dim=axis))(v_cache[:, :, Sk - W:, :], rep, axis=1)
        scores = torch.einsum("bhqd,bhkd->bhqk", q, k) * (Dh ** -0.5)
        p = _softmax(scores, axis=-1)
        o = torch.einsum("bhqk,bhkd->bhqd", p, v)
        return (lambda x, *d: x.permute(*d))(o, 0, 2, 1, 3).reshape(B, Sq, Hq * Dh) @ wo

    def _B_ref(x, w1, w2):
        h = torch.einsum("bsd,df->bsf", x, w1)
        zero = torch.zeros_like(h)
        h = torch.maximum(h, zero)
        return torch.einsum("bsf,fd->bsd", h, w2)

    x = A_x
    xn = _layernorm(x, axis=-1)
    s = _A_ref(xn, A_k_cache, A_v_cache, A_wq, A_wo)
    g = _silu(_B_ref(xn, B_w1, B_w2))
    return x + s * g
```

```cpp
#include <hip/hip_runtime.h>
#include <hip/hip_bf16.h>
#include <stdint.h>

constexpr int B = 2, D = 640, H = 32, SK = 8192, DH = 128, W = 2048, DFF = 1728;
constexpr int NSPLIT = 4;
typedef unsigned u32x4 __attribute__((ext_vector_type(4)));
typedef __bf16 bf16x2 __attribute__((ext_vector_type(2)));

__device__ __forceinline__ float bf2f(unsigned short v) { return __uint_as_float((unsigned)v << 16); }
__device__ __forceinline__ float rbf(float f) { return __bfloat162float(__float2bfloat16(f)); }
__device__ __forceinline__ float dot2(unsigned k, unsigned q, float acc) {
  return __builtin_amdgcn_fdot2_f32_bf16(__builtin_bit_cast(bf16x2, k), __builtin_bit_cast(bf16x2, q), acc, false);
}

__device__ void layernorm_lds(const unsigned short* x, float* xn  , float* red) {
  const int tid = threadIdx.x;
  for (int b = 0; b < B; ++b) {
    float s = 0.f;
    for (int i = tid; i < D; i += blockDim.x) s += bf2f(x[b * D + i]);
    for (int o = 32; o > 0; o >>= 1) s += __shfl_xor(s, o);
    if ((tid & 63) == 0) red[tid >> 6] = s;
    __syncthreads();
    float tot = 0.f;
    for (int w = 0; w < blockDim.x / 64; ++w) tot += red[w];
    __syncthreads();
    const float meanf = tot / D;
    float v = 0.f;
    for (int i = tid; i < D; i += blockDim.x) { float d = bf2f(x[b * D + i]) - meanf; v += d * d; }
    for (int o = 32; o > 0; o >>= 1) v += __shfl_xor(v, o);
    if ((tid & 63) == 0) red[tid >> 6] = v;
    __syncthreads();
    float vt = 0.f;
    for (int w = 0; w < blockDim.x / 64; ++w) vt += red[w];
    __syncthreads();
    const float m = rbf(meanf), var = rbf(vt / D);
    const float den = rbf(sqrtf(rbf(var + 1e-5f)));
    for (int i = tid; i < D; i += blockDim.x) xn[b * D + i] = rbf(rbf(bf2f(x[b * D + i]) - m) / den);
  }
  __syncthreads();
}

__global__ void __launch_bounds__(256) k1(const unsigned short* x, const unsigned short* wq, const unsigned short* w1, float* q, float* h) {
  __shared__ float xn[B * D];
  __shared__ float red[4];
  __shared__ float part[4][2][64];
  layernorm_lds(x, xn, red);
  const int blk = blockIdx.x, tid = threadIdx.x, lane = tid & 63, w = tid >> 6;
  const bool isq = blk < 64;
  const unsigned short* wm = isq ? wq : w1;
  const int N = isq ? 4096 : DFF;
  const int col = (isq ? blk : blk - 64) * 64 + lane;
  float a0 = 0.f, a1 = 0.f;
  for (int k = w * 160; k < w * 160 + 160; ++k) {
    float wv = bf2f(wm[(size_t)k * N + col]);
    a0 += xn[k] * wv; a1 += xn[D + k] * wv;
  }
  part[w][0][lane] = a0; part[w][1][lane] = a1;
  __syncthreads();
  if (tid < 128) {
    int b = tid >> 6;
    float s = part[0][b][lane] + part[1][b][lane] + part[2][b][lane] + part[3][b][lane];
    s = rbf(s);
    if (isq) q[b * 4096 + col] = s; else h[b * DFF + col] = fmaxf(s, 0.f);
  }
}

__global__ void __launch_bounds__(256) k2(const unsigned short* kc, const unsigned short* vc, const float* q, float* pml, float* po) {
  __shared__ float sm[4][132];
  const int blk = blockIdx.x, s = blk & 3, bh = blk >> 2;
  const int tid = threadIdx.x, lane = tid & 63, w = tid >> 6, r = lane >> 4, c = lane & 15;
  const float* qp = q + bh * DH + 8 * c;
  unsigned qpk[4];
  for (int i = 0; i < 4; ++i) qpk[i] = (__float_as_uint(qp[2 * i]) >> 16) | (__float_as_uint(qp[2 * i + 1]) & 0xffff0000u);
  const size_t base = ((size_t)bh * SK + (SK - W) + s * 512 + w * 128) * DH;
  const u32x4* kp = (const u32x4*)(kc + base) + lane;
  const u32x4* vp = (const u32x4*)(vc + base) + lane;
  float m = -INFINITY, l = 0.f, acc[8] = {0, 0, 0, 0, 0, 0, 0, 0};
  for (int i = 0; i < 32; i += 4) {
    u32x4 kk[4], vv[4];
    for (int t = 0; t < 4; ++t) { kk[t] = __builtin_nontemporal_load(kp + (i + t) * 64); vv[t] = __builtin_nontemporal_load(vp + (i + t) * 64); }
    float sc[4];
    for (int t = 0; t < 4; ++t) {
      float d = 0.f;
      for (int j = 0; j < 4; ++j) d = dot2(kk[t][j], qpk[j], d);
      d += __shfl_xor(d, 1); d += __shfl_xor(d, 2); d += __shfl_xor(d, 4); d += __shfl_xor(d, 8);
      sc[t] = rbf(rbf(d) * 0.08838834764831845f);
    }
    float mn = fmaxf(fmaxf(fmaxf(sc[0], sc[1]), fmaxf(sc[2], sc[3])), m);
    float alpha = __expf(m - mn);
    l *= alpha;
    for (int j = 0; j < 8; ++j) acc[j] *= alpha;
    m = mn;
    for (int t = 0; t < 4; ++t) {
      float p = __expf(sc[t] - m);
      l += p;
      for (int j = 0; j < 4; ++j) { acc[2 * j] += p * __uint_as_float(vv[t][j] << 16); acc[2 * j + 1] += p * __uint_as_float(vv[t][j] & 0xffff0000u); }
    }
  }
  float M = fmaxf(m, __shfl_xor(m, 16)); M = fmaxf(M, __shfl_xor(M, 32));
  float sc = __expf(m - M);
  l *= sc; l += __shfl_xor(l, 16); l += __shfl_xor(l, 32);
  for (int j = 0; j < 8; ++j) { acc[j] *= sc; acc[j] += __shfl_xor(acc[j], 16); acc[j] += __shfl_xor(acc[j], 32); }
  if (r == 0) { for (int j = 0; j < 8; ++j) sm[w][4 + 8 * c + j] = acc[j]; if (c == 0) { sm[w][0] = M; sm[w][1] = l; } }
  __syncthreads();
  if (tid < 128) {
    float MM = fmaxf(fmaxf(sm[0][0], sm[1][0]), fmaxf(sm[2][0], sm[3][0]));
    float L = 0.f, o = 0.f;
    for (int ww = 0; ww < 4; ++ww) { float f = __expf(sm[ww][0] - MM); L += f * sm[ww][1]; o += f * sm[ww][4 + tid]; }
    po[blk * DH + tid] = o;
    if (tid == 0) { pml[blk * 2] = MM; pml[blk * 2 + 1] = L; }
  }
}

__global__ void __launch_bounds__(640) k3(const float* pml, const float* po, const float* h, const unsigned short* wo, const unsigned short* w2, float* part) {
  __shared__ float v[2][128];
  const int blk = blockIdx.x, tid = threadIdx.x;
  int rows; const unsigned short* wm;
  if (blk < 32) {
    rows = 128; wm = wo + (size_t)blk * 128 * D;
    if (tid < 256) {
      int b = tid >> 7, d = tid & 127, bh = b * H + blk;
      float M = -INFINITY;
      for (int s = 0; s < 4; ++s) M = fmaxf(M, pml[(bh * 4 + s) * 2]);
      float L = 0.f, o = 0.f;
      for (int s = 0; s < 4; ++s) { float f = __expf(pml[(bh * 4 + s) * 2] - M); L += f * pml[(bh * 4 + s) * 2 + 1]; o += f * po[(bh * 4 + s) * DH + d]; }
      v[b][d] = rbf(o / L);
    }
  } else {
    rows = 64; wm = w2 + (size_t)(blk - 32) * 64 * D;
    if (tid < 128) { int b = tid >> 6, d = tid & 63; v[b][d] = h[b * DFF + (blk - 32) * 64 + d]; }
  }
  __syncthreads();
  float a0 = 0.f, a1 = 0.f;
  for (int k = 0; k < rows; ++k) { float wv = bf2f(wm[(size_t)k * D + tid]); a0 += v[0][k] * wv; a1 += v[1][k] * wv; }
  part[(blk * 2 + 0) * D + tid] = a0; part[(blk * 2 + 1) * D + tid] = a1;
}

__global__ void __launch_bounds__(64) k4(const unsigned short* x, const float* part, __hip_bfloat16* out) {
  const int i = blockIdx.x * 64 + threadIdx.x;
  const int b = i / D, n = i % D;
  float s = 0.f, f = 0.f;
  for (int hh = 0; hh < 32; ++hh) s += part[(hh * 2 + b) * D + n];
  for (int j = 32; j < 59; ++j) f += part[(j * 2 + b) * D + n];
  s = rbf(s); f = rbf(f);
  float g = rbf(f / (1.f + __expf(-f)));
  out[i] = __float2bfloat16(bf2f(x[i]) + rbf(s * g));
}

extern "C" void kernel_launch(void* const* d_in, const int* in_sizes, int n_in, void* d_out, int out_size, void* d_ws, size_t ws_size, hipStream_t stream) {
  const unsigned short* x = (const unsigned short*)d_in[0];
  const unsigned short* kc = (const unsigned short*)d_in[1];
  const unsigned short* vc = (const unsigned short*)d_in[2];
  const unsigned short* wq = (const unsigned short*)d_in[3];
  const unsigned short* wo = (const unsigned short*)d_in[4];
  const unsigned short* w1 = (const unsigned short*)d_in[6];
  const unsigned short* w2 = (const unsigned short*)d_in[7];
  float* ws = (float*)d_ws;
  float* q = ws;
  float* h = ws + 8192;
  float* pml = ws + 16384;
  float* po = ws + 20480;
  float* part = ws + 65536;
  k1<<<64 + 27, 256, 0, stream>>>(x, wq, w1, q, h);
  k2<<<256, 256, 0, stream>>>(kc, vc, q, pml, po);
  k3<<<59, 640, 0, stream>>>(pml, po, h, wo, w2, part);
  k4<<<20, 64, 0, stream>>>(x, part, (__hip_bfloat16*)d_out);
}
```

```cpp
#define BENCH_STAMPS 1
#define PRIO 3
#define WK 24
#define WK2 16
#define PDELAY 24
#define WSEND 1
#define HSLEEP 2
#define H1DELAY 4
#include <cstdio>
#include <hip/hip_runtime.h>
#include <hip/hip_bf16.h>
#include <stdint.h>

#define STAMPS 1
#define NSTAMP 16
#define STAMP_OFF (256u << 20)
#define STAMP_REL (WSEND ? (long long)STAMP_OFF - (long long)((536870912ull - WS_END) & ~4095ull) : (long long)STAMP_OFF)
#define STAMP(i) do { SBAR(); stv[i] = (unsigned)__builtin_amdgcn_s_memrealtime() | 1u; SBAR(); } while (0)
#define STAMP_DECL unsigned stv[NSTAMP] = {0,0,0,0,0,0,0,0,0,0,0,0,0,0,0,0};
#define STAMP_FLUSH_OLD() do { if (lane == 0) { unsigned* sp = (unsigned*)(ws + STAMP_REL) + (blk * 10 + w) * NSTAMP; _Pragma("unroll") for (int i_ = 0; i_ < NSTAMP; ++i_) sp[i_] = stv[i_]; } } while (0)
#define STAMP_FLUSH() do { if (lane == 0 && (blk % 37 == 0) && (w == 0 || w == 1 || w == 2 || w == 9)) printf("blk %3d w%d: %u %d %d %d %d %d %d %d %d %d %d %d %d %d %d %d\n", blk, w, stv[0] % 1000000u, (int)(stv[1]-stv[0]), (int)(stv[2]-stv[0]), (int)(stv[3]-stv[0]), (int)(stv[4]-stv[0]), (int)(stv[5]-stv[0]), (int)(stv[6]-stv[0]), (int)(stv[7]-stv[0]), (int)(stv[8]-stv[0]), (int)(stv[9]-stv[0]), (int)(stv[10]-stv[0]), (int)(stv[11]-stv[0]), (int)(stv[12]-stv[0]), (int)(stv[13]-stv[0]), (int)(stv[14]-stv[0]), (int)(stv[15]-stv[0])); } while (0)

typedef unsigned u32x4 __attribute__((ext_vector_type(4)));
typedef unsigned u32x2 __attribute__((ext_vector_type(2)));
typedef float f32x4 __attribute__((ext_vector_type(4)));
typedef __bf16 bf16x8 __attribute__((ext_vector_type(8)));
typedef __bf16 bf16x2 __attribute__((ext_vector_type(2)));

#define POLLAUX 16
constexpr int AUX_NT = 2, AUX_SC1 = 16, AUX_POLL = POLLAUX;
constexpr unsigned OOB = 0x7ffffff0u;
constexpr unsigned WS_EPOCH = 0;
constexpr unsigned WS_Q1 = 4096;
constexpr unsigned WS_H1 = WS_Q1 + 32 * 8 * 1024;
constexpr unsigned WS_Q2 = WS_H1 + 32 * 8 * 1024;
constexpr unsigned WS_H2 = WS_Q2 + 32 * 8 * 128;
constexpr unsigned WS_SBOX = WS_H2 + 32 * 8 * 128;
constexpr unsigned WS_OBOX = WS_SBOX + 32 * 8 * 128;
constexpr unsigned WS_YBOX = WS_OBOX + 32 * 8 * 1024;
constexpr unsigned WS_FBOX = WS_YBOX + 8 * 32 * 1280;
constexpr unsigned WS_END = WS_FBOX + 8 * 32 * 1280;
constexpr unsigned SPIN_CAP = 1u << 16;

#define RSRC(p, n) __builtin_amdgcn_make_buffer_rsrc((void*)(p), 0, (n), 0x00020000)
#define LD128(rs, vo, so, aux) __builtin_amdgcn_raw_buffer_load_b128((rs), (vo), (so), (aux))
#define ST128(v, rs, vo, so, aux) __builtin_amdgcn_raw_buffer_store_b128((v), (rs), (vo), (so), (aux))
#define SBAR() __builtin_amdgcn_sched_barrier(0)
#define LAS __attribute__((address_space(3)))
#define LDS_LD32(p) __hip_atomic_load((LAS unsigned*)(p), __ATOMIC_RELAXED, __HIP_MEMORY_SCOPE_WORKGROUP)
#define LDS_ST32(p, v) __hip_atomic_store((LAS unsigned*)(p), (v), __ATOMIC_RELAXED, __HIP_MEMORY_SCOPE_WORKGROUP)
#define LDS_LD64(p) __hip_atomic_load((LAS unsigned long long*)(p), __ATOMIC_RELAXED, __HIP_MEMORY_SCOPE_WORKGROUP)
#define LDS_ST64(p, v) __hip_atomic_store((LAS unsigned long long*)(p), (v), __ATOMIC_RELAXED, __HIP_MEMORY_SCOPE_WORKGROUP)
#define CBAR() asm volatile("" ::: "memory")

template <int CTRL> __device__ __forceinline__ unsigned dppu(unsigned x) { return (unsigned)__builtin_amdgcn_mov_dpp((int)x, CTRL, 0xf, 0xf, true); }
template <int CTRL> __device__ __forceinline__ float dppf(float x) { return __uint_as_float(dppu<CTRL>(__float_as_uint(x))); }
constexpr int XOR1 = 0xB1, XOR2 = 0x4E, XOR7 = 0x141, ROR8 = 0x128, ROR4 = 0x124, SHL4 = 0x104, SHR4 = 0x114, BCAST = 0x150;

__device__ __forceinline__ float dot2(unsigned k, unsigned q, float acc) {
  return __builtin_amdgcn_fdot2_f32_bf16(__builtin_bit_cast(bf16x2, k), __builtin_bit_cast(bf16x2, q), acc, false);
}
__device__ __forceinline__ unsigned pk_bf16(float lo, float hi) {
  bf16x2 v; v[0] = (__bf16)lo; v[1] = (__bf16)hi; return __builtin_bit_cast(unsigned, v);
}
__device__ __forceinline__ float rbf(float f) { return __uint_as_float(pk_bf16(f, f) & 0xffff0000u); }
__device__ __forceinline__ f32x4 mfma(u32x4 a, u32x4 b, f32x4 c) {
  return __builtin_amdgcn_mfma_f32_16x16x32_bf16(__builtin_bit_cast(bf16x8, a), __builtin_bit_cast(bf16x8, b), c, 0, 0, 0);
}
__device__ __forceinline__ float x16_sum(float x) { auto s = __builtin_amdgcn_permlane16_swap(__float_as_uint(x), __float_as_uint(x), false, false); return __uint_as_float(s[0]) + __uint_as_float(s[1]); }
__device__ __forceinline__ float x32_sum(float x) { auto s = __builtin_amdgcn_permlane32_swap(__float_as_uint(x), __float_as_uint(x), false, false); return __uint_as_float(s[0]) + __uint_as_float(s[1]); }
__device__ __forceinline__ float x16_max(float x) { auto s = __builtin_amdgcn_permlane16_swap(__float_as_uint(x), __float_as_uint(x), false, false); return fmaxf(__uint_as_float(s[0]), __uint_as_float(s[1])); }
__device__ __forceinline__ float x32_max(float x) { auto s = __builtin_amdgcn_permlane32_swap(__float_as_uint(x), __float_as_uint(x), false, false); return fmaxf(__uint_as_float(s[0]), __uint_as_float(s[1])); }
__device__ __forceinline__ float row_sum(float x) { x += dppf<XOR1>(x); x += dppf<XOR2>(x); x += dppf<ROR4>(x); x += dppf<ROR8>(x); return x; }
__device__ __forceinline__ float row_max(float x) { x = fmaxf(x, dppf<XOR1>(x)); x = fmaxf(x, dppf<XOR2>(x)); x = fmaxf(x, dppf<ROR4>(x)); x = fmaxf(x, dppf<ROR8>(x)); return x; }
template <int C0, int C1> __device__ __forceinline__ float trsel(float lo, float hi, bool bit) {
  const float t0 = lo + dppf<C0>(lo), t1 = hi + dppf<C1>(hi);
  return bit ? t1 : t0;
}
__device__ __forceinline__ bool gok(u32x4 g, unsigned tag) { return (g[1] == tag) & (g[3] == tag); }
__device__ __forceinline__ u32x4 gran2(float a, float b, unsigned tag) { u32x4 g; g[0] = __float_as_uint(a); g[1] = tag; g[2] = __float_as_uint(b); g[3] = tag; return g; }
__device__ __forceinline__ void wg_barrier() { asm volatile("s_waitcnt lgkmcnt(0)\n\ts_barrier" ::: "memory"); }

constexpr float LOG2E = 1.4426950408889634f;

#define POLLDELAY() do { if (PDELAY) __builtin_amdgcn_s_sleep(PDELAY); } while (0)
#define NPACE 16
#define MAPX 0
#define KVORD 0
#define ABL_NOX0 0
#define ABL_NOB0 0
#define OSHARED 1
#define ABL 0
#define X0MODE 0
#define VMWAIT(n) asm volatile("s_waitcnt vmcnt(%0)" :: "i"(n) : "memory")

__device__ __forceinline__ float2 ld2(const float* p) { return *(const float2*)p; }

__global__ void __launch_bounds__(640, 3)
fused_decode(const unsigned short* __restrict__ x, const unsigned short* __restrict__ kc, const unsigned short* __restrict__ vc,
             const unsigned short* __restrict__ wq, const unsigned short* __restrict__ wo, const unsigned short* __restrict__ w1,
             const unsigned short* __restrict__ w2, unsigned short* __restrict__ out, unsigned char* ws) {
  extern __shared__ __attribute__((aligned(16))) unsigned char smem[];
  f32x4* red = (f32x4*)smem;
  unsigned* qs = (unsigned*)(smem + 24576);
  unsigned* hs = (unsigned*)(smem + 24576 + 256);
  float* st = (float*)(smem + 24576 + 768);
  unsigned* flg = (unsigned*)(smem + 24576 + 832);
  float* gw = (float*)(smem + 24576 + 896);
  unsigned* wpl = (unsigned*)(smem + 24576 + 848);
  unsigned* os = (unsigned*)(smem + 24576 + 4096);
  unsigned* xl = (unsigned*)(smem + 24576 + 1024);
  const int blk = blockIdx.x, tid = threadIdx.x, lane = tid & 63, w = __builtin_amdgcn_readfirstlane(tid >> 6);
  STAMP_DECL
  STAMP(0);
  const int j = (blk >> 3) & 7, h = (blk & 7) + 8 * (blk >> 6), b = j >> 2, s = j & 3;
  const int r = lane >> 4, c = lane & 15;
  const bool ffn = h < 27;
  const __amdgpu_buffer_rsrc_t rws = RSRC(ws, WS_END);
  const __amdgpu_buffer_rsrc_t rx = RSRC(x, 2560);
  u32x4 mask;
#pragma unroll
  for (int i = 0; i < 4; ++i) mask[i] = (i == ((c & 7) >> 1)) ? ((c & 1) ? 0xffff0000u : 0xffffu) : 0u;
  auto sum_publish = [&](const f32x4* rr, unsigned box, int cbase, unsigned tag) {
    const int cr = cbase + (lane >> 3), e = lane & 7;
    const float* p = (const float*)rr + ((e >> 1) * 16 + (cr & 15)) * 4 + 2 * (e & 1);
    float2 a[8];
#pragma unroll
    for (int i = 0; i < 8; ++i) a[i] = ld2(p + i * 256);
    const float v0 = ((a[0].x + a[1].x) + (a[2].x + a[3].x)) + ((a[4].x + a[5].x) + (a[6].x + a[7].x));
    const float v1 = ((a[0].y + a[1].y) + (a[2].y + a[3].y)) + ((a[4].y + a[5].y) + (a[6].y + a[7].y));
    ST128(gran2(v0, v1, tag), rws, cr < 10 ? (unsigned)((j * 32 + h) * 1280 + cr * 128 + e * 16) : OOB, box, AUX_SC1);
  };

  if (w >= 2) {
    const int sw = w - 2;
    const size_t kvbase = ((size_t)(b * 32 + h) * 8192 + 6144 + s * 512) * 128;
    const __amdgpu_buffer_rsrc_t rk = RSRC(kc + kvbase, 131072), rv = RSRC(vc + kvbase, 131072);
    const __amdgpu_buffer_rsrc_t ro = RSRC(wo + (size_t)h * 128 * 640 + j * 80, 128 * 1280 - j * 160);
    const __amdgpu_buffer_rsrc_t r2 = RSRC(w2 + (size_t)h * 64 * 640 + j * 80, ffn ? 64 * 1280 - j * 160 : 0);
    const int rot = sw * 4 + ((blk * 37) & 127);
    const int voff = lane * 16;
#define KVOFF(t) ((((((t) >> 2) * 32 + ((t) & 3) + rot) & 127) << 10))
    u32x4 K[16], V[16], WO[4], W2[2];
    unsigned pp = 0;
    auto scores = [&]() {
      CBAR();
      const u32x4 q4 = *(const u32x4*)(qs + 4 * c);
      float sb[4];
#pragma unroll
      for (int be = 0; be < 4; ++be) {
        float d[4];
#pragma unroll
        for (int tt = 0; tt < 4; ++tt) {
          const u32x4 kk = K[4 * be + tt];
          d[tt] = dot2(kk[3], q4[3], dot2(kk[2], q4[2], dot2(kk[1], q4[1], dot2(kk[0], q4[0], 0.f))));
        }
        const float e0 = trsel<XOR1, XOR1>(d[0], d[1], c & 1);
        const float e1 = trsel<XOR1, XOR1>(d[2], d[3], c & 1);
        sb[be] = trsel<XOR2, XOR2>(e0, e1, c & 2);
      }
      const float f0 = trsel<SHL4, SHR4>(sb[0], sb[1], c & 4);
      const float f1 = trsel<SHL4, SHR4>(sb[2], sb[3], c & 4);
      const float dotv = trsel<ROR8, ROR8>(f0, f1, c & 8);
      const float sc = rbf(rbf(dotv) * 0.08838834764831845f);
      const float mw = x32_max(x16_max(row_max(sc)));
      const float ew = __builtin_amdgcn_exp2f((sc - mw) * LOG2E);
      pp = pk_bf16(ew, ew);
      const float lw = x32_sum(x16_sum(row_sum(ew)));
      if (lane == 0) LDS_ST64(st + 2 * sw, (unsigned long long)__float_as_uint(mw) | ((unsigned long long)__float_as_uint(lw) << 32));
    };
    if (PRIO) __builtin_amdgcn_s_setprio(PRIO == 3 ? 3 : 2);
    wg_barrier();
#pragma unroll
    for (int i = 0; i < 24; ++i) {
      if (PRIO == 1 && i == 16) __builtin_amdgcn_s_setprio(1);
      if (PRIO == 3 && i == 16) __builtin_amdgcn_s_setprio(2);
      if (PRIO == 2 && i == 8) __builtin_amdgcn_s_setprio(1);
      if (PRIO == 2 && i == 16) __builtin_amdgcn_s_setprio(0);
      if (i >= WK) VMWAIT(WK - 1);
      if (i < 16) K[i] = LD128(rk, voff, KVOFF(i), AUX_NT); else V[i - 16] = LD128(rv, voff, KVOFF(i - 16), AUX_NT);
      SBAR();
      if (i == WK - 1) STAMP(1);
    }
    STAMP(4);
    const bool early = __builtin_amdgcn_readfirstlane(LDS_LD32(flg + 1)) != 0u;
    if (early) { scores(); STAMP(5); }
    SBAR();
    if (PRIO == 1) __builtin_amdgcn_s_setprio(0);
    if (PRIO == 3) __builtin_amdgcn_s_setprio(1);
    {
      const unsigned vo2 = c < 10 ? (unsigned)(r * 2 * 1280 + c * 16) : OOB;
#pragma unroll
      for (int i = 0; i < 2; ++i) { VMWAIT(WK2 - 1); W2[i] = LD128(r2, vo2, (8 * sw + i) * 1280, AUX_NT); SBAR(); }
    }
#pragma unroll
    for (int t = 8; t < 16; ++t) {
      VMWAIT(WK2 - 1);
      V[t] = LD128(rv, voff, KVOFF(t), AUX_NT);
      SBAR();
    }
    if (!early) {
      for (unsigned spins = 0; spins < (SPIN_CAP << 4); ++spins) {
        if (__builtin_amdgcn_readfirstlane(LDS_LD32(flg + 1)) != 0u) break;
        __builtin_amdgcn_s_sleep(1);
      }
      scores();
      STAMP(6);
    }
    SBAR();
    {
      if (PRIO == 3) __builtin_amdgcn_s_setprio(0);
      const unsigned vo4 = c < 10 ? (unsigned)(r * 4 * 1280 + c * 16) : OOB;
#pragma unroll
      for (int i = 0; i < 4; ++i) { VMWAIT(WK2 - 1); WO[i] = LD128(ro, vo4, (16 * sw + i) * 1280, AUX_NT); SBAR(); }
    }
    STAMP(7);
    const unsigned tag = __builtin_amdgcn_readfirstlane(LDS_LD32(flg));
    f32x4 oacc = {0.f, 0.f, 0.f, 0.f};
#define PVSTEP(t) oacc = mfma(mask & dppu<BCAST + (t)>(pp), V[t], oacc);
    PVSTEP(0) PVSTEP(1) PVSTEP(2) PVSTEP(3) PVSTEP(4) PVSTEP(5) PVSTEP(6) PVSTEP(7)
    PVSTEP(8) PVSTEP(9) PVSTEP(10) PVSTEP(11) PVSTEP(12) PVSTEP(13) PVSTEP(14) PVSTEP(15)
#undef PVSTEP
    red[sw * 64 + lane] = oacc;
    STAMP(8);
    wg_barrier();
    {
      const int u = lane & 7;
      const float* p = (const float*)red + (((u >> 1) & 1) * 16 + 2 * sw + (u >> 2)) * 4 + 2 * (u & 1);
      const f32x4 g0 = *(const f32x4*)gw, g1 = *(const f32x4*)(gw + 4);
      float2 a[8];
#pragma unroll
      for (int i = 0; i < 8; ++i) a[i] = ld2(p + i * 256);
      const float v0 = ((a[0].x * g0[0] + a[1].x * g0[1]) + (a[2].x * g0[2] + a[3].x * g0[3])) + ((a[4].x * g1[0] + a[5].x * g1[1]) + (a[6].x * g1[2] + a[7].x * g1[3]));
      const float v1 = ((a[0].y * g0[0] + a[1].y * g0[1]) + (a[2].y * g0[2] + a[3].y * g0[3])) + ((a[4].y * g1[0] + a[5].y * g1[1]) + (a[6].y * g1[2] + a[7].y * g1[3]));
      ST128(gran2(v0, v1, tag), rws, lane < 8 ? (unsigned)((h * 8 + j) * 1024 + sw * 128 + u * 16) : OOB, WS_OBOX, AUX_SC1);
    }
    STAMP(9);
    if (ffn) {
      const u32x2 hv = *(const u32x2*)(hs + (c >> 3) * 64 + 8 * sw + 2 * r);
      f32x4 facc = {0.f, 0.f, 0.f, 0.f};
      facc = mfma(mask & hv[0], W2[0], facc);
      facc = mfma(mask & hv[1], W2[1], facc);
      red[(8 + sw) * 64 + lane] = facc;
      asm volatile("s_waitcnt lgkmcnt(0)" ::: "memory");
      if (lane == 0) __hip_atomic_fetch_add((LAS unsigned*)(flg + 3), 1u, __ATOMIC_RELAXED, __HIP_MEMORY_SCOPE_WORKGROUP);
    }
    VMWAIT(0);
    STAMP(15);
    wg_barrier();
    STAMP(10);
    {
      const u32x4 ov = *(const u32x4*)(os + (c >> 3) * 128 + 16 * sw + 4 * r);
      f32x4 y = {0.f, 0.f, 0.f, 0.f}, y2 = {0.f, 0.f, 0.f, 0.f};
      y = mfma(mask & ov[0], WO[0], y);
      y2 = mfma(mask & ov[1], WO[1], y2);
      y = mfma(mask & ov[2], WO[2], y);
      y2 = mfma(mask & ov[3], WO[3], y2);
      red[(16 + sw) * 64 + lane] = y + y2;
    }
    STAMP(11);
    wg_barrier();
    if (sw == 0) sum_publish(red + 1024, WS_YBOX, 8, tag);
  } else {
    __builtin_amdgcn_s_setprio(3);
    const bool isq = (w == 0);
    const unsigned epoch = __builtin_amdgcn_raw_buffer_load_b32(rws, 0, WS_EPOCH, AUX_SC1);
    u32x2 xs[5];
#pragma unroll
    for (int i = 0; i < 5; ++i) xs[i] = __builtin_amdgcn_raw_buffer_load_b64(rx, (lane >> 5) * 1280 + (lane & 31) * 8, i * 256, 0);
    const int row0 = isq ? 160 * (j >> 1) : 80 * j;
    const int nrow = isq ? 160 : 80;
    unsigned short xr[5];
#pragma unroll
    for (int i = 0; i < 5; ++i) xr[i] = __builtin_amdgcn_raw_buffer_load_b16(rx, (lane >> 5) * 1280 + (lane & 31) * 2, (row0 + 32 * i) * 2, 0);
    u32x4 wa[20];
    {
      const __amdgpu_buffer_rsrc_t rw = isq ? RSRC(wq + h * 128 + (j & 1) * 64, 640 * 4096 * 2 - h * 256 - (j & 1) * 128) : RSRC(w1 + h * 64, ffn ? 640 * 1728 * 2 - h * 128 : 0);
      const int rstride = isq ? 8192 : 3456;
      const unsigned vo = (unsigned)((lane >> 3) * rstride + (lane & 7) * 16);
#pragma unroll
      for (int i = 0; i < 10; ++i) wa[i] = LD128(rw, vo, (row0 + 8 * i) * rstride, AUX_NT);
      if (isq) {
#pragma unroll
        for (int i = 10; i < 20; ++i) wa[i] = LD128(rw, vo, (row0 + 8 * i) * rstride, AUX_NT);
      }
    }
    if (isq && lane < 28) LDS_ST32(st + lane, 0u);
    STAMP(1);
    wg_barrier();
    float xf[20];
#pragma unroll
    for (int i = 0; i < 5; ++i) {
      xf[4 * i + 0] = __uint_as_float(xs[i][0] << 16); xf[4 * i + 1] = __uint_as_float(xs[i][0] & 0xffff0000u);
      xf[4 * i + 2] = __uint_as_float(xs[i][1] << 16); xf[4 * i + 3] = __uint_as_float(xs[i][1] & 0xffff0000u);
    }
    float sum = 0.f;
#pragma unroll
    for (int i = 0; i < 20; ++i) sum += xf[i];
    sum = x16_sum(row_sum(sum));
    const float mean = sum * (1.f / 640.f);
    float vs = 0.f;
#pragma unroll
    for (int i = 0; i < 20; ++i) { const float d = xf[i] - mean; vs = fmaf(d, d, vs); }
    vs = x16_sum(row_sum(vs));
    const float mb = rbf(mean), den = rbf(sqrtf(rbf(rbf(vs * (1.f / 640.f)) + 1e-5f)));
    unsigned* xw_ = xl + w * 320;
#pragma unroll
    for (int i = 0; i < 5; ++i) {
      const int row = (lane & 31) + 32 * i;
      const float xn = rbf(__uint_as_float((unsigned)xr[i] << 16) - mb) / den;
      if (row < nrow) xw_[(lane >> 5) * 160 + ((row >> 1) & 3) * 40 + (row >> 3) * 2 + (row & 1)] = pk_bf16(xn, xn);
    }
    const unsigned tag = __builtin_amdgcn_readfirstlane(epoch) + 0x9E3779B9u;
    if (isq && lane == 0) LDS_ST32(flg, tag);
    asm volatile("s_waitcnt lgkmcnt(0)" ::: "memory");
    STAMP(2);
    f32x4 acc0 = {0.f, 0.f, 0.f, 0.f}, acc1 = {0.f, 0.f, 0.f, 0.f};
    const unsigned* xrd = xw_ + (c >> 3) * 160 + r * 40;
#pragma unroll
    for (int i = 0; i < 10; i += 2) {
      const u32x4 xa = *(const u32x4*)(xrd + 2 * i);
      acc0 = mfma(mask & xa[0], wa[i], acc0); acc1 = mfma(mask & xa[1], wa[i], acc1);
      acc0 = mfma(mask & xa[2], wa[i + 1], acc0); acc1 = mfma(mask & xa[3], wa[i + 1], acc1);
    }
    if (isq) {
#pragma unroll
      for (int i = 10; i < 20; i += 2) {
        const u32x4 xa = *(const u32x4*)(xrd + 2 * i);
        acc0 = mfma(mask & xa[0], wa[i], acc0); acc1 = mfma(mask & xa[1], wa[i], acc1);
        acc0 = mfma(mask & xa[2], wa[i + 1], acc0); acc1 = mfma(mask & xa[3], wa[i + 1], acc1);
      }
    }
    if (isq || ffn) {
      f32x4 v;
#pragma unroll
      for (int i = 0; i < 4; ++i) { const float t_ = c < 8 ? acc0[i] : acc1[i]; v[i] = t_ + dppf<ROR8>(t_); }
      const u32x4 pub = gran2(c < 8 ? v[0] : v[2], c < 8 ? v[1] : v[3], tag);
      const unsigned cu = (unsigned)(h * 8 + j);
      if (isq) {
        if (X0MODE != 1 && X0MODE != 3) ST128(pub, rws, cu * 1024 + lane * 16, WS_Q1, AUX_SC1);
        STAMP(3);
        POLLDELAY();
        const unsigned goff = (unsigned)((h * 8 + (lane >> 5)) * 1024 + (b * 32 + (lane & 31)) * 16);
        u32x4 G[4];
#pragma unroll
        for (int p = 0; p < 4; ++p) G[p] = (X0MODE == 1 || X0MODE == 2) ? pub : LD128(rws, goff, WS_Q1 + p * 2048, AUX_POLL);
        for (unsigned spins = 0; X0MODE == 0 || X0MODE == 4; ++spins) {
          bool allok = true;
          CBAR();
#pragma unroll
          for (int p = 0; p < 4; ++p) if (!__all(gok(G[p], tag))) { allok = false; G[p] = LD128(rws, goff, WS_Q1 + p * 2048, AUX_POLL); }
          if (spins == 0) STAMP(14);
          if (allok || spins > SPIN_CAP) break;
        }
        const float a0 = (__uint_as_float(G[0][0]) + __uint_as_float(G[1][0])) + (__uint_as_float(G[2][0]) + __uint_as_float(G[3][0]));
        const float a1 = (__uint_as_float(G[0][2]) + __uint_as_float(G[1][2])) + (__uint_as_float(G[2][2]) + __uint_as_float(G[3][2]));
        qs[(lane >> 5) * 32 + (c & 7) * 4 + (r & 1) * 2 + (c >> 3)] = pk_bf16(a0, a1);
      } else {
        if (X0MODE == 0 || X0MODE == 2) ST128(pub, rws, (unsigned)(((h * 8 + (c & 7)) * 8 + j) * 128 + (c >> 3) * 64 + r * 16), WS_H1, AUX_SC1);
        STAMP(3);
        POLLDELAY(); POLLDELAY();
        const unsigned goff = cu * 1024 + lane * 16;
        u32x4 G0 = (X0MODE == 0 || X0MODE == 3) ? LD128(rws, goff, WS_H1, AUX_POLL) : pub;
        for (unsigned spins = 0; X0MODE == 0 && !__all(gok(G0, tag)) && spins < SPIN_CAP; ++spins) { CBAR(); G0 = LD128(rws, goff, WS_H1, AUX_POLL); }
        STAMP(14);
        float a0 = __uint_as_float(G0[0]), a1 = __uint_as_float(G0[2]);
        a0 += dppf<ROR8>(a0); a1 += dppf<ROR8>(a1);
        a0 = fmaxf(x32_sum(x16_sum(a0)), 0.f); a1 = fmaxf(x32_sum(x16_sum(a1)), 0.f);
        u32x2 g2; g2[0] = pk_bf16(a0, a1); g2[1] = tag;
        if (X0MODE == 0 || X0MODE == 2) __builtin_amdgcn_raw_buffer_store_b64(g2, rws, lane < 8 ? cu * 128 + lane * 8 : OOB, WS_H2, AUX_SC1);
        const unsigned g2off = (unsigned)((h * 8 + (lane >> 3)) * 128 + (lane & 7) * 8);
        POLLDELAY(); POLLDELAY(); POLLDELAY();
        u32x2 q2 = (X0MODE == 0 || X0MODE == 3) ? __builtin_amdgcn_raw_buffer_load_b64(rws, g2off, WS_H2, AUX_POLL) : g2;
        for (unsigned spins = 0; X0MODE == 0 && !__all(q2[1] == tag) && spins < SPIN_CAP; ++spins) { CBAR(); q2 = __builtin_amdgcn_raw_buffer_load_b64(rws, g2off, WS_H2, AUX_POLL); }
        u32x2 hv; hv[0] = (q2[0] & 0xffffu) | (q2[0] << 16); hv[1] = (q2[0] >> 16) | (q2[0] & 0xffff0000u);
        *(u32x2*)(hs + ((lane >> 1) & 1) * 64 + (lane >> 3) * 8 + (lane & 1) * 4 + ((lane >> 2) & 1) * 2) = hv;
      }
      asm volatile("s_waitcnt lgkmcnt(0)" ::: "memory");
      if (lane == 0) LDS_ST32(flg + 1 + w, 1u);
    }
    STAMP(4);
    if (isq) {
      u32x2 ml;
      for (unsigned spins = 0;; ++spins) {
        { const unsigned long long t_ = LDS_LD64(st + 2 * (lane & 7)); ml[0] = (unsigned)t_; ml[1] = (unsigned)(t_ >> 32); }
        if (__all(ml[1] != 0u) || spins > (SPIN_CAP << 4)) break;
        __builtin_amdgcn_s_sleep(2);
      }
      const float m_ = __uint_as_float(ml[0]), l_ = __uint_as_float(ml[1]);
      float M = fmaxf(m_, dppf<XOR1>(m_)); M = fmaxf(M, dppf<XOR2>(M)); M = fmaxf(M, dppf<XOR7>(M));
      const float f_ = __builtin_amdgcn_exp2f((m_ - M) * LOG2E);
      float L = f_ * l_; L += dppf<XOR1>(L); L += dppf<XOR2>(L); L += dppf<XOR7>(L);
      ST128(gran2(M, L, tag), rws, lane == 0 ? WS_SBOX + (unsigned)((h * 8 + j) * 128) : OOB, 0, AUX_SC1);
      STAMP(5);
      if (lane < 8) gw[lane] = f_;
      STAMP(7);
    }
    wg_barrier();
    STAMP(8);
    if (!isq && ffn) {
      for (unsigned spins = 0; spins < (SPIN_CAP << 4); ++spins) { if (__builtin_amdgcn_readfirstlane(LDS_LD32(flg + 3)) == 8u) break; }
      CBAR();
      sum_publish(red + 512, WS_FBOX, 0, tag);
      sum_publish(red + 512, WS_FBOX, 8, tag);
    }
    if (isq) {
      const unsigned soff = WS_SBOX + (unsigned)((h * 8 + (lane & 7)) * 128);
      const unsigned ooff = (unsigned)(h * 8 * 1024 + lane * 16);
      u32x4 gs = LD128(rws, soff, 0, AUX_POLL);
      if (H1DELAY) __builtin_amdgcn_s_sleep(H1DELAY);
      u32x4 G[8];
#pragma unroll
      for (int p = 0; p < 8; ++p) G[p] = LD128(rws, ooff, WS_OBOX + p * 1024, AUX_POLL);
      for (unsigned spins = 0; !__all(gok(gs, tag)) && spins < SPIN_CAP; ++spins) { CBAR(); __builtin_amdgcn_s_sleep(2); gs = LD128(rws, soff, 0, AUX_POLL); }
      float wp[8];
      {
        const float ms = __uint_as_float(gs[0]), ls = __uint_as_float(gs[2]);
        float Mg = fmaxf(ms, dppf<XOR1>(ms)); Mg = fmaxf(Mg, dppf<XOR2>(Mg));
        const float e_ = __builtin_amdgcn_exp2f((ms - Mg) * LOG2E);
        float Lg = e_ * ls; Lg += dppf<XOR1>(Lg); Lg += dppf<XOR2>(Lg);
        const float wgt = e_ / Lg;
#pragma unroll
        for (int p = 0; p < 8; ++p) wp[p] = __uint_as_float(__builtin_amdgcn_readlane(__float_as_uint(wgt), p));
      }
      STAMP(9);
      for (unsigned spins = 0;; ++spins) {
        bool allok = true;
        CBAR();
#pragma unroll
        for (int p = 0; p < 8; ++p) {
          if (!__all(gok(G[p], tag))) { if (allok && HSLEEP) __builtin_amdgcn_s_sleep(HSLEEP); allok = false; G[p] = LD128(rws, ooff, WS_OBOX + p * 1024, AUX_POLL); }
        }
        if (allok || spins > SPIN_CAP) break;
      }
      STAMP(10);
#pragma unroll
      for (int bb = 0; bb < 2; ++bb) {
        float o0 = 0.f, o1 = 0.f;
#pragma unroll
        for (int sp = 0; sp < 4; ++sp) { o0 = fmaf(wp[4 * bb + sp], __uint_as_float(G[4 * bb + sp][0]), o0); o1 = fmaf(wp[4 * bb + sp], __uint_as_float(G[4 * bb + sp][2]), o1); }
        u32x2 ov; ov[0] = pk_bf16(o0, o0); ov[1] = pk_bf16(o1, o1);
        *(u32x2*)(os + bb * 128 + 2 * lane) = ov;
      }
    }
    wg_barrier();
    const int e = lane & 7, ob = e >> 2, ocol = j * 80 + 8 * h + 4 * ((e >> 1) & 1) + 2 * (e & 1);
    const bool fin = !isq && h < 10;
    const unsigned yo = (unsigned)((j * 32 + (lane >> 3)) * 1280 + h * 128 + e * 16);
    const bool f3v = (lane >> 3) < 3;
    unsigned xw = 0; float fa = 0.f, fb = 0.f;
    if (fin) {
      xw = __builtin_amdgcn_raw_buffer_load_b32(rx, (ob * 640 + ocol) * 2, 0, 0);
    }
    wg_barrier();
    STAMP(11);
    if (isq) {
      sum_publish(red + 1024, WS_YBOX, 0, tag);
      STAMP(12);
    } else if (fin) {
      u32x4 g[4], y[4];
#pragma unroll
      for (int i = 0; i < 4; ++i) g[i] = LD128(rws, (i < 3 || f3v) ? yo : OOB, WS_FBOX + i * 8 * 1280, AUX_POLL);
#pragma unroll
      for (int i = 0; i < 4; ++i) y[i] = LD128(rws, yo, WS_YBOX + i * 8 * 1280, AUX_POLL);
      for (unsigned spins = 0;; ++spins) {
        bool allok = true;
        CBAR();
#pragma unroll
        for (int i = 0; i < 4; ++i) if (!__all(gok(g[i], tag) | (i == 3 && !f3v))) { allok = false; g[i] = LD128(rws, (i < 3 || f3v) ? yo : OOB, WS_FBOX + i * 8 * 1280, AUX_POLL); }
        if (allok || spins > SPIN_CAP) break;
      }
      fa = (__uint_as_float(g[0][0]) + __uint_as_float(g[1][0])) + (__uint_as_float(g[2][0]) + __uint_as_float(g[3][0]));
      fb = (__uint_as_float(g[0][2]) + __uint_as_float(g[1][2])) + (__uint_as_float(g[2][2]) + __uint_as_float(g[3][2]));
      fa += dppf<ROR8>(fa); fb += dppf<ROR8>(fb);
      fa = rbf(x32_sum(x16_sum(fa))); fb = rbf(x32_sum(x16_sum(fb)));
      const float ga = rbf(fa / (1.f + __expf(-fa))), gb = rbf(fb / (1.f + __expf(-fb)));
      STAMP(10);
      for (unsigned spins = 0;; ++spins) {
        bool allok = true;
        CBAR();
#pragma unroll
        for (int i = 0; i < 4; ++i) if (!__all(gok(y[i], tag))) { allok = false; y[i] = LD128(rws, yo, WS_YBOX + i * 8 * 1280, AUX_POLL); }
        if (allok || spins > SPIN_CAP) break;
      }
      STAMP(13);
      float ya = (__uint_as_float(y[0][0]) + __uint_as_float(y[1][0])) + (__uint_as_float(y[2][0]) + __uint_as_float(y[3][0]));
      float yb = (__uint_as_float(y[0][2]) + __uint_as_float(y[1][2])) + (__uint_as_float(y[2][2]) + __uint_as_float(y[3][2]));
      ya += dppf<ROR8>(ya); yb += dppf<ROR8>(yb);
      ya = rbf(x32_sum(x16_sum(ya))); yb = rbf(x32_sum(x16_sum(yb)));
      const float oa = __uint_as_float(xw << 16) + rbf(ya * ga), obv = __uint_as_float(xw & 0xffff0000u) + rbf(yb * gb);
      if (lane < 8) *(unsigned*)(out + ob * 640 + ocol) = pk_bf16(oa, obv);
      if (blk == 0 && lane == 0) __builtin_amdgcn_raw_buffer_store_b32(tag, rws, 0, WS_EPOCH, AUX_SC1);
      STAMP(12);
    }
  }
  STAMP_FLUSH();
}

extern "C" void kernel_launch(void* const* d_in, const int* in_sizes, int n_in, void* d_out, int out_size, void* d_ws, size_t ws_size, hipStream_t stream) {
  constexpr int LDS = 96 * 1024;
  fprintf(stderr, "ptrs: x=%p k=%p v=%p wq=%p wo=%p w1=%p w2=%p out=%p ws=%p\n", d_in[0], d_in[1], d_in[2], d_in[3], d_in[4], d_in[6], d_in[7], d_out, d_ws);
  (void)hipFuncSetAttribute((const void*)fused_decode, hipFuncAttributeMaxDynamicSharedMemorySize, LDS);
  fused_decode<<<256, 640, LDS, stream>>>((const unsigned short*)d_in[0], (const unsigned short*)d_in[1], (const unsigned short*)d_in[2],
      (const unsigned short*)d_in[3], (const unsigned short*)d_in[4], (const unsigned short*)d_in[6], (const unsigned short*)d_in[7],
      (unsigned short*)d_out, (unsigned char*)d_ws + (WSEND ? ((ws_size - WS_END) & ~(size_t)4095) : 0));
}
```

```cpp
#define BENCH_STAMPS 1
#define PRIO 3
#define WK 24
#define WK2 16
#define PDELAY 24
#define WSEND 0
#define HSLEEP 2
#define H1DELAY 4
#define PW 0
#define PX 1
#include <hip/hip_runtime.h>
#include <hip/hip_bf16.h>
#include <stdint.h>

#define STAMPS 1
#define NSTAMP 16
#define STAMP_OFF (256u << 20)
#define STAMP_REL (WSEND ? (long long)STAMP_OFF - (long long)((536870912ull - WS_END) & ~4095ull) : (long long)STAMP_OFF)
#define STAMP(i) do { SBAR(); stv[i] = (unsigned)__builtin_amdgcn_s_memrealtime() | 1u; SBAR(); } while (0)
#define STAMP_DECL unsigned stv[NSTAMP] = {0,0,0,0,0,0,0,0,0,0,0,0,0,0,0,0};
#define STAMP_FLUSH_OLD() do { if (lane == 0) { unsigned* sp = (unsigned*)(ws + STAMP_REL) + (blk * 10 + w) * NSTAMP; _Pragma("unroll") for (int i_ = 0; i_ < NSTAMP; ++i_) sp[i_] = stv[i_]; } } while (0)
#define STAMP_FLUSH() do { if (w == PW && stv[PX] != 0u) { unsigned d_ = (stv[PX] - stv[0]) * 10u; if (d_ > 40000u) d_ = 40000u; unsigned t_ = (unsigned)__builtin_amdgcn_s_memrealtime(); while ((unsigned)__builtin_amdgcn_s_memrealtime() - t_ < d_) __builtin_amdgcn_s_sleep(8); } } while (0)

typedef unsigned u32x4 __attribute__((ext_vector_type(4)));
typedef unsigned u32x2 __attribute__((ext_vector_type(2)));
typedef float f32x4 __attribute__((ext_vector_type(4)));
typedef __bf16 bf16x8 __attribute__((ext_vector_type(8)));
typedef __bf16 bf16x2 __attribute__((ext_vector_type(2)));

#define POLLAUX 16
constexpr int AUX_NT = 2, AUX_SC1 = 16, AUX_POLL = POLLAUX;
constexpr unsigned OOB = 0x7ffffff0u;
constexpr unsigned WS_EPOCH = 0;
constexpr unsigned WS_Q1 = 4096;
constexpr unsigned WS_H1 = WS_Q1 + 32 * 8 * 1024;
constexpr unsigned WS_Q2 = WS_H1 + 32 * 8 * 1024;
constexpr unsigned WS_H2 = WS_Q2 + 32 * 8 * 128;
constexpr unsigned WS_SBOX = WS_H2 + 32 * 8 * 128;
constexpr unsigned WS_OBOX = WS_SBOX + 32 * 8 * 128;
constexpr unsigned WS_YBOX = WS_OBOX + 32 * 8 * 1024;
constexpr unsigned WS_FBOX = WS_YBOX + 8 * 32 * 1280;
constexpr unsigned WS_END = WS_FBOX + 8 * 32 * 1280;
constexpr unsigned SPIN_CAP = 1u << 16;

#define RSRC(p, n) __builtin_amdgcn_make_buffer_rsrc((void*)(p), 0, (n), 0x00020000)
#define LD128(rs, vo, so, aux) __builtin_amdgcn_raw_buffer_load_b128((rs), (vo), (so), (aux))
#define ST128(v, rs, vo, so, aux) __builtin_amdgcn_raw_buffer_store_b128((v), (rs), (vo), (so), (aux))
#define SBAR() __builtin_amdgcn_sched_barrier(0)
#define LAS __attribute__((address_space(3)))
#define LDS_LD32(p) __hip_atomic_load((LAS unsigned*)(p), __ATOMIC_RELAXED, __HIP_MEMORY_SCOPE_WORKGROUP)
#define LDS_ST32(p, v) __hip_atomic_store((LAS unsigned*)(p), (v), __ATOMIC_RELAXED, __HIP_MEMORY_SCOPE_WORKGROUP)
#define LDS_LD64(p) __hip_atomic_load((LAS unsigned long long*)(p), __ATOMIC_RELAXED, __HIP_MEMORY_SCOPE_WORKGROUP)
#define LDS_ST64(p, v) __hip_atomic_store((LAS unsigned long long*)(p), (v), __ATOMIC_RELAXED, __HIP_MEMORY_SCOPE_WORKGROUP)
#define CBAR() asm volatile("" ::: "memory")

template <int CTRL> __device__ __forceinline__ unsigned dppu(unsigned x) { return (unsigned)__builtin_amdgcn_mov_dpp((int)x, CTRL, 0xf, 0xf, true); }
template <int CTRL> __device__ __forceinline__ float dppf(float x) { return __uint_as_float(dppu<CTRL>(__float_as_uint(x))); }
constexpr int XOR1 = 0xB1, XOR2 = 0x4E, XOR7 = 0x141, ROR8 = 0x128, ROR4 = 0x124, SHL4 = 0x104, SHR4 = 0x114, BCAST = 0x150;

__device__ __forceinline__ float dot2(unsigned k, unsigned q, float acc) {
  return __builtin_amdgcn_fdot2_f32_bf16(__builtin_bit_cast(bf16x2, k), __builtin_bit_cast(bf16x2, q), acc, false);
}
__device__ __forceinline__ unsigned pk_bf16(float lo, float hi) {
  bf16x2 v; v[0] = (__bf16)lo; v[1] = (__bf16)hi; return __builtin_bit_cast(unsigned, v);
}
__device__ __forceinline__ float rbf(float f) { return __uint_as_float(pk_bf16(f, f) & 0xffff0000u); }
__device__ __forceinline__ f32x4 mfma(u32x4 a, u32x4 b, f32x4 c) {
  return __builtin_amdgcn_mfma_f32_16x16x32_bf16(__builtin_bit_cast(bf16x8, a), __builtin_bit_cast(bf16x8, b), c, 0, 0, 0);
}
__device__ __forceinline__ float x16_sum(float x) { auto s = __builtin_amdgcn_permlane16_swap(__float_as_uint(x), __float_as_uint(x), false, false); return __uint_as_float(s[0]) + __uint_as_float(s[1]); }
__device__ __forceinline__ float x32_sum(float x) { auto s = __builtin_amdgcn_permlane32_swap(__float_as_uint(x), __float_as_uint(x), false, false); return __uint_as_float(s[0]) + __uint_as_float(s[1]); }
__device__ __forceinline__ float x16_max(float x) { auto s = __builtin_amdgcn_permlane16_swap(__float_as_uint(x), __float_as_uint(x), false, false); return fmaxf(__uint_as_float(s[0]), __uint_as_float(s[1])); }
__device__ __forceinline__ float x32_max(float x) { auto s = __builtin_amdgcn_permlane32_swap(__float_as_uint(x), __float_as_uint(x), false, false); return fmaxf(__uint_as_float(s[0]), __uint_as_float(s[1])); }
__device__ __forceinline__ float row_sum(float x) { x += dppf<XOR1>(x); x += dppf<XOR2>(x); x += dppf<ROR4>(x); x += dppf<ROR8>(x); return x; }
__device__ __forceinline__ float row_max(float x) { x = fmaxf(x, dppf<XOR1>(x)); x = fmaxf(x, dppf<XOR2>(x)); x = fmaxf(x, dppf<ROR4>(x)); x = fmaxf(x, dppf<ROR8>(x)); return x; }
template <int C0, int C1> __device__ __forceinline__ float trsel(float lo, float hi, bool bit) {
  const float t0 = lo + dppf<C0>(lo), t1 = hi + dppf<C1>(hi);
  return bit ? t1 : t0;
}
__device__ __forceinline__ bool gok(u32x4 g, unsigned tag) { return (g[1] == tag) & (g[3] == tag); }
__device__ __forceinline__ u32x4 gran2(float a, float b, unsigned tag) { u32x4 g; g[0] = __float_as_uint(a); g[1] = tag; g[2] = __float_as_uint(b); g[3] = tag; return g; }
__device__ __forceinline__ void wg_barrier() { asm volatile("s_waitcnt lgkmcnt(0)\n\ts_barrier" ::: "memory"); }

constexpr float LOG2E = 1.4426950408889634f;

#define POLLDELAY() do { if (PDELAY) __builtin_amdgcn_s_sleep(PDELAY); } while (0)
#define NPACE 16
#define MAPX 0
#define KVORD 0
#define ABL_NOX0 0
#define ABL_NOB0 0
#define OSHARED 1
#define ABL 0
#define X0MODE 0
#define ARGPIN 1
#define VMWAIT(n) asm volatile("s_waitcnt vmcnt(%0)" :: "i"(n) : "memory")

__device__ __forceinline__ float2 ld2(const float* p) { return *(const float2*)p; }

__global__ void __launch_bounds__(640, 3)
fused_decode(const unsigned short* x, const unsigned short* kc, const unsigned short* vc,
             const unsigned short* wq, const unsigned short* wo, const unsigned short* w1,
             const unsigned short* w2, unsigned short* out, unsigned char* ws) {
  asm volatile("" : "+s"(x), "+s"(kc), "+s"(vc), "+s"(wq), "+s"(wo), "+s"(w1), "+s"(w2), "+s"(out), "+s"(ws));
  extern __shared__ __attribute__((aligned(16))) unsigned char smem[];
  f32x4* red = (f32x4*)smem;
  unsigned* qs = (unsigned*)(smem + 24576);
  unsigned* hs = (unsigned*)(smem + 24576 + 256);
  float* st = (float*)(smem + 24576 + 768);
  unsigned* flg = (unsigned*)(smem + 24576 + 832);
  float* gw = (float*)(smem + 24576 + 896);
  unsigned* wpl = (unsigned*)(smem + 24576 + 848);
  unsigned* os = (unsigned*)(smem + 24576 + 4096);
  unsigned* xl = (unsigned*)(smem + 24576 + 1024);
  const int blk = blockIdx.x, tid = threadIdx.x, lane = tid & 63, w = __builtin_amdgcn_readfirstlane(tid >> 6);
  STAMP_DECL
  STAMP(0);
  const int j = (blk >> 3) & 7, h = (blk & 7) + 8 * (blk >> 6), b = j >> 2, s = j & 3;
  const int r = lane >> 4, c = lane & 15;
  const bool ffn = h < 27;
  const __amdgpu_buffer_rsrc_t rws = RSRC(ws, WS_END);
  const __amdgpu_buffer_rsrc_t rx = RSRC(x, 2560);
  u32x4 mask;
#pragma unroll
  for (int i = 0; i < 4; ++i) mask[i] = (i == ((c & 7) >> 1)) ? ((c & 1) ? 0xffff0000u : 0xffffu) : 0u;
  auto sum_publish = [&](const f32x4* rr, unsigned box, int cbase, unsigned tag) {
    const int cr = cbase + (lane >> 3), e = lane & 7;
    const float* p = (const float*)rr + ((e >> 1) * 16 + (cr & 15)) * 4 + 2 * (e & 1);
    float2 a[8];
#pragma unroll
    for (int i = 0; i < 8; ++i) a[i] = ld2(p + i * 256);
    const float v0 = ((a[0].x + a[1].x) + (a[2].x + a[3].x)) + ((a[4].x + a[5].x) + (a[6].x + a[7].x));
    const float v1 = ((a[0].y + a[1].y) + (a[2].y + a[3].y)) + ((a[4].y + a[5].y) + (a[6].y + a[7].y));
    ST128(gran2(v0, v1, tag), rws, cr < 10 ? (unsigned)((j * 32 + h) * 1280 + cr * 128 + e * 16) : OOB, box, AUX_SC1);
  };

  if (w >= 2) {
    const int sw = w - 2;
    const size_t kvbase = ((size_t)(b * 32 + h) * 8192 + 6144 + s * 512) * 128;
    const __amdgpu_buffer_rsrc_t rk = RSRC(kc + kvbase, 131072), rv = RSRC(vc + kvbase, 131072);
    const __amdgpu_buffer_rsrc_t ro = RSRC(wo + (size_t)h * 128 * 640 + j * 80, 128 * 1280 - j * 160);
    const __amdgpu_buffer_rsrc_t r2 = RSRC(w2 + (size_t)h * 64 * 640 + j * 80, ffn ? 64 * 1280 - j * 160 : 0);
    const int rot = sw * 4 + ((blk * 37) & 127);
    const int voff = lane * 16;
#define KVOFF(t) ((((((t) >> 2) * 32 + ((t) & 3) + rot) & 127) << 10))
    u32x4 K[16], V[16], WO[4], W2[2];
    unsigned pp = 0;
    auto scores = [&]() {
      CBAR();
      const u32x4 q4 = *(const u32x4*)(qs + 4 * c);
      float sb[4];
#pragma unroll
      for (int be = 0; be < 4; ++be) {
        float d[4];
#pragma unroll
        for (int tt = 0; tt < 4; ++tt) {
          const u32x4 kk = K[4 * be + tt];
          d[tt] = dot2(kk[3], q4[3], dot2(kk[2], q4[2], dot2(kk[1], q4[1], dot2(kk[0], q4[0], 0.f))));
        }
        const float e0 = trsel<XOR1, XOR1>(d[0], d[1], c & 1);
        const float e1 = trsel<XOR1, XOR1>(d[2], d[3], c & 1);
        sb[be] = trsel<XOR2, XOR2>(e0, e1, c & 2);
      }
      const float f0 = trsel<SHL4, SHR4>(sb[0], sb[1], c & 4);
      const float f1 = trsel<SHL4, SHR4>(sb[2], sb[3], c & 4);
      const float dotv = trsel<ROR8, ROR8>(f0, f1, c & 8);
      const float sc = rbf(rbf(dotv) * 0.08838834764831845f);
      const float mw = x32_max(x16_max(row_max(sc)));
      const float ew = __builtin_amdgcn_exp2f((sc - mw) * LOG2E);
      pp = pk_bf16(ew, ew);
      const float lw = x32_sum(x16_sum(row_sum(ew)));
      if (lane == 0) LDS_ST64(st + 2 * sw, (unsigned long long)__float_as_uint(mw) | ((unsigned long long)__float_as_uint(lw) << 32));
    };
    if (PRIO) __builtin_amdgcn_s_setprio(PRIO == 3 ? 3 : 2);
    wg_barrier();
#pragma unroll
    for (int i = 0; i < 24; ++i) {
      if (PRIO == 1 && i == 16) __builtin_amdgcn_s_setprio(1);
      if (PRIO == 3 && i == 16) __builtin_amdgcn_s_setprio(2);
      if (PRIO == 2 && i == 8) __builtin_amdgcn_s_setprio(1);
      if (PRIO == 2 && i == 16) __builtin_amdgcn_s_setprio(0);
      if (i >= WK) VMWAIT(WK - 1);
      if (i < 16) K[i] = LD128(rk, voff, KVOFF(i), AUX_NT); else V[i - 16] = LD128(rv, voff, KVOFF(i - 16), AUX_NT);
      SBAR();
      if (i == WK - 1) STAMP(1);
    }
    STAMP(4);
    const bool early = __builtin_amdgcn_readfirstlane(LDS_LD32(flg + 1)) != 0u;
    if (early) { scores(); STAMP(5); }
    SBAR();
    if (PRIO == 1) __builtin_amdgcn_s_setprio(0);
    if (PRIO == 3) __builtin_amdgcn_s_setprio(1);
    {
      const unsigned vo2 = c < 10 ? (unsigned)(r * 2 * 1280 + c * 16) : OOB;
#pragma unroll
      for (int i = 0; i < 2; ++i) { VMWAIT(WK2 - 1); W2[i] = LD128(r2, vo2, (8 * sw + i) * 1280, AUX_NT); SBAR(); }
    }
#pragma unroll
    for (int t = 8; t < 16; ++t) {
      VMWAIT(WK2 - 1);
      V[t] = LD128(rv, voff, KVOFF(t), AUX_NT);
      SBAR();
    }
    if (!early) {
      for (unsigned spins = 0; spins < (SPIN_CAP << 4); ++spins) {
        if (__builtin_amdgcn_readfirstlane(LDS_LD32(flg + 1)) != 0u) break;
        __builtin_amdgcn_s_sleep(1);
      }
      scores();
      STAMP(6);
    }
    SBAR();
    {
      if (PRIO == 3) __builtin_amdgcn_s_setprio(0);
      const unsigned vo4 = c < 10 ? (unsigned)(r * 4 * 1280 + c * 16) : OOB;
#pragma unroll
      for (int i = 0; i < 4; ++i) { VMWAIT(WK2 - 1); WO[i] = LD128(ro, vo4, (16 * sw + i) * 1280, AUX_NT); SBAR(); }
    }
    STAMP(7);
    const unsigned tag = __builtin_amdgcn_readfirstlane(LDS_LD32(flg));
    f32x4 oacc = {0.f, 0.f, 0.f, 0.f};
#define PVSTEP(t) oacc = mfma(mask & dppu<BCAST + (t)>(pp), V[t], oacc);
    PVSTEP(0) PVSTEP(1) PVSTEP(2) PVSTEP(3) PVSTEP(4) PVSTEP(5) PVSTEP(6) PVSTEP(7)
    PVSTEP(8) PVSTEP(9) PVSTEP(10) PVSTEP(11) PVSTEP(12) PVSTEP(13) PVSTEP(14) PVSTEP(15)
#undef PVSTEP
    red[sw * 64 + lane] = oacc;
    STAMP(8);
    wg_barrier();
    {
      const int u = lane & 7;
      const float* p = (const float*)red + (((u >> 1) & 1) * 16 + 2 * sw + (u >> 2)) * 4 + 2 * (u & 1);
      const f32x4 g0 = *(const f32x4*)gw, g1 = *(const f32x4*)(gw + 4);
      float2 a[8];
#pragma unroll
      for (int i = 0; i < 8; ++i) a[i] = ld2(p + i * 256);
      const float v0 = ((a[0].x * g0[0] + a[1].x * g0[1]) + (a[2].x * g0[2] + a[3].x * g0[3])) + ((a[4].x * g1[0] + a[5].x * g1[1]) + (a[6].x * g1[2] + a[7].x * g1[3]));
      const float v1 = ((a[0].y * g0[0] + a[1].y * g0[1]) + (a[2].y * g0[2] + a[3].y * g0[3])) + ((a[4].y * g1[0] + a[5].y * g1[1]) + (a[6].y * g1[2] + a[7].y * g1[3]));
      ST128(gran2(v0, v1, tag), rws, lane < 8 ? (unsigned)((h * 8 + j) * 1024 + sw * 128 + u * 16) : OOB, WS_OBOX, AUX_SC1);
    }
    STAMP(9);
    if (ffn) {
      const u32x2 hv = *(const u32x2*)(hs + (c >> 3) * 64 + 8 * sw + 2 * r);
      f32x4 facc = {0.f, 0.f, 0.f, 0.f};
      facc = mfma(mask & hv[0], W2[0], facc);
      facc = mfma(mask & hv[1], W2[1], facc);
      red[(8 + sw) * 64 + lane] = facc;
      asm volatile("s_waitcnt lgkmcnt(0)" ::: "memory");
      if (lane == 0) __hip_atomic_fetch_add((LAS unsigned*)(flg + 3), 1u, __ATOMIC_RELAXED, __HIP_MEMORY_SCOPE_WORKGROUP);
    }
    VMWAIT(0);
    STAMP(15);
    wg_barrier();
    STAMP(10);
    {
      const u32x4 ov = *(const u32x4*)(os + (c >> 3) * 128 + 16 * sw + 4 * r);
      f32x4 y = {0.f, 0.f, 0.f, 0.f}, y2 = {0.f, 0.f, 0.f, 0.f};
      y = mfma(mask & ov[0], WO[0], y);
      y2 = mfma(mask & ov[1], WO[1], y2);
      y = mfma(mask & ov[2], WO[2], y);
      y2 = mfma(mask & ov[3], WO[3], y2);
      red[(16 + sw) * 64 + lane] = y + y2;
    }
    STAMP(11);
    wg_barrier();
    if (sw == 0) sum_publish(red + 1024, WS_YBOX, 8, tag);
  } else {
    __builtin_amdgcn_s_setprio(3);
    const bool isq = (w == 0);
    const unsigned epoch = __builtin_amdgcn_raw_buffer_load_b32(rws, 0, WS_EPOCH, AUX_SC1);
    u32x2 xs[5];
#pragma unroll
    for (int i = 0; i < 5; ++i) xs[i] = __builtin_amdgcn_raw_buffer_load_b64(rx, (lane >> 5) * 1280 + (lane & 31) * 8, i * 256, 0);
    const int row0 = isq ? 160 * (j >> 1) : 80 * j;
    const int nrow = isq ? 160 : 80;
    unsigned short xr[5];
#pragma unroll
    for (int i = 0; i < 5; ++i) xr[i] = __builtin_amdgcn_raw_buffer_load_b16(rx, (lane >> 5) * 1280 + (lane & 31) * 2, (row0 + 32 * i) * 2, 0);
    u32x4 wa[20];
    {
      const __amdgpu_buffer_rsrc_t rw = isq ? RSRC(wq + h * 128 + (j & 1) * 64, 640 * 4096 * 2 - h * 256 - (j & 1) * 128) : RSRC(w1 + h * 64, ffn ? 640 * 1728 * 2 - h * 128 : 0);
      const int rstride = isq ? 8192 : 3456;
      const unsigned vo = (unsigned)((lane >> 3) * rstride + (lane & 7) * 16);
#pragma unroll
      for (int i = 0; i < 10; ++i) wa[i] = LD128(rw, vo, (row0 + 8 * i) * rstride, AUX_NT);
      if (isq) {
#pragma unroll
        for (int i = 10; i < 20; ++i) wa[i] = LD128(rw, vo, (row0 + 8 * i) * rstride, AUX_NT);
      }
    }
    if (isq && lane < 28) LDS_ST32(st + lane, 0u);
    STAMP(1);
    wg_barrier();
    float xf[20];
#pragma unroll
    for (int i = 0; i < 5; ++i) {
      xf[4 * i + 0] = __uint_as_float(xs[i][0] << 16); xf[4 * i + 1] = __uint_as_float(xs[i][0] & 0xffff0000u);
      xf[4 * i + 2] = __uint_as_float(xs[i][1] << 16); xf[4 * i + 3] = __uint_as_float(xs[i][1] & 0xffff0000u);
    }
    float sum = 0.f;
#pragma unroll
    for (int i = 0; i < 20; ++i) sum += xf[i];
    sum = x16_sum(row_sum(sum));
    const float mean = sum * (1.f / 640.f);
    float vs = 0.f;
#pragma unroll
    for (int i = 0; i < 20; ++i) { const float d = xf[i] - mean; vs = fmaf(d, d, vs); }
    vs = x16_sum(row_sum(vs));
    const float mb = rbf(mean), den = rbf(sqrtf(rbf(rbf(vs * (1.f / 640.f)) + 1e-5f)));
    unsigned* xw_ = xl + w * 320;
#pragma unroll
    for (int i = 0; i < 5; ++i) {
      const int row = (lane & 31) + 32 * i;
      const float xn = rbf(__uint_as_float((unsigned)xr[i] << 16) - mb) / den;
      if (row < nrow) xw_[(lane >> 5) * 160 + ((row >> 1) & 3) * 40 + (row >> 3) * 2 + (row & 1)] = pk_bf16(xn, xn);
    }
    const unsigned tag = __builtin_amdgcn_readfirstlane(epoch) + 0x9E3779B9u;
    if (isq && lane == 0) LDS_ST32(flg, tag);
    asm volatile("s_waitcnt lgkmcnt(0)" ::: "memory");
    STAMP(2);
    f32x4 acc0 = {0.f, 0.f, 0.f, 0.f}, acc1 = {0.f, 0.f, 0.f, 0.f};
    const unsigned* xrd = xw_ + (c >> 3) * 160 + r * 40;
#pragma unroll
    for (int i = 0; i < 10; i += 2) {
      const u32x4 xa = *(const u32x4*)(xrd + 2 * i);
      acc0 = mfma(mask & xa[0], wa[i], acc0); acc1 = mfma(mask & xa[1], wa[i], acc1);
      acc0 = mfma(mask & xa[2], wa[i + 1], acc0); acc1 = mfma(mask & xa[3], wa[i + 1], acc1);
    }
    if (isq) {
#pragma unroll
      for (int i = 10; i < 20; i += 2) {
        const u32x4 xa = *(const u32x4*)(xrd + 2 * i);
        acc0 = mfma(mask & xa[0], wa[i], acc0); acc1 = mfma(mask & xa[1], wa[i], acc1);
        acc0 = mfma(mask & xa[2], wa[i + 1], acc0); acc1 = mfma(mask & xa[3], wa[i + 1], acc1);
      }
    }
    if (isq || ffn) {
      f32x4 v;
#pragma unroll
      for (int i = 0; i < 4; ++i) { const float t_ = c < 8 ? acc0[i] : acc1[i]; v[i] = t_ + dppf<ROR8>(t_); }
      const u32x4 pub = gran2(c < 8 ? v[0] : v[2], c < 8 ? v[1] : v[3], tag);
      const unsigned cu = (unsigned)(h * 8 + j);
      if (isq) {
        if (X0MODE != 1 && X0MODE != 3) ST128(pub, rws, cu * 1024 + lane * 16, WS_Q1, AUX_SC1);
        STAMP(3);
        POLLDELAY();
        const unsigned goff = (unsigned)((h * 8 + (lane >> 5)) * 1024 + (b * 32 + (lane & 31)) * 16);
        u32x4 G[4];
#pragma unroll
        for (int p = 0; p < 4; ++p) G[p] = (X0MODE == 1 || X0MODE == 2) ? pub : LD128(rws, goff, WS_Q1 + p * 2048, AUX_POLL);
        for (unsigned spins = 0; X0MODE == 0 || X0MODE == 4; ++spins) {
          bool allok = true;
          CBAR();
#pragma unroll
          for (int p = 0; p < 4; ++p) if (!__all(gok(G[p], tag))) { allok = false; G[p] = LD128(rws, goff, WS_Q1 + p * 2048, AUX_POLL); }
          if (spins == 0) STAMP(14);
          if (allok || spins > SPIN_CAP) break;
        }
        const float a0 = (__uint_as_float(G[0][0]) + __uint_as_float(G[1][0])) + (__uint_as_float(G[2][0]) + __uint_as_float(G[3][0]));
        const float a1 = (__uint_as_float(G[0][2]) + __uint_as_float(G[1][2])) + (__uint_as_float(G[2][2]) + __uint_as_float(G[3][2]));
        qs[(lane >> 5) * 32 + (c & 7) * 4 + (r & 1) * 2 + (c >> 3)] = pk_bf16(a0, a1);
      } else {
        if (X0MODE == 0 || X0MODE == 2) ST128(pub, rws, (unsigned)(((h * 8 + (c & 7)) * 8 + j) * 128 + (c >> 3) * 64 + r * 16), WS_H1, AUX_SC1);
        STAMP(3);
        POLLDELAY(); POLLDELAY();
        const unsigned goff = cu * 1024 + lane * 16;
        u32x4 G0 = (X0MODE == 0 || X0MODE == 3) ? LD128(rws, goff, WS_H1, AUX_POLL) : pub;
        for (unsigned spins = 0; X0MODE == 0 && !__all(gok(G0, tag)) && spins < SPIN_CAP; ++spins) { CBAR(); G0 = LD128(rws, goff, WS_H1, AUX_POLL); }
        STAMP(14);
        float a0 = __uint_as_float(G0[0]), a1 = __uint_as_float(G0[2]);
        a0 += dppf<ROR8>(a0); a1 += dppf<ROR8>(a1);
        a0 = fmaxf(x32_sum(x16_sum(a0)), 0.f); a1 = fmaxf(x32_sum(x16_sum(a1)), 0.f);
        u32x2 g2; g2[0] = pk_bf16(a0, a1); g2[1] = tag;
        if (X0MODE == 0 || X0MODE == 2) __builtin_amdgcn_raw_buffer_store_b64(g2, rws, lane < 8 ? cu * 128 + lane * 8 : OOB, WS_H2, AUX_SC1);
        const unsigned g2off = (unsigned)((h * 8 + (lane >> 3)) * 128 + (lane & 7) * 8);
        POLLDELAY(); POLLDELAY(); POLLDELAY();
        u32x2 q2 = (X0MODE == 0 || X0MODE == 3) ? __builtin_amdgcn_raw_buffer_load_b64(rws, g2off, WS_H2, AUX_POLL) : g2;
        for (unsigned spins = 0; X0MODE == 0 && !__all(q2[1] == tag) && spins < SPIN_CAP; ++spins) { CBAR(); q2 = __builtin_amdgcn_raw_buffer_load_b64(rws, g2off, WS_H2, AUX_POLL); }
        u32x2 hv; hv[0] = (q2[0] & 0xffffu) | (q2[0] << 16); hv[1] = (q2[0] >> 16) | (q2[0] & 0xffff0000u);
        *(u32x2*)(hs + ((lane >> 1) & 1) * 64 + (lane >> 3) * 8 + (lane & 1) * 4 + ((lane >> 2) & 1) * 2) = hv;
      }
      asm volatile("s_waitcnt lgkmcnt(0)" ::: "memory");
      if (lane == 0) LDS_ST32(flg + 1 + w, 1u);
    }
    STAMP(4);
    if (isq) {
      u32x2 ml;
      for (unsigned spins = 0;; ++spins) {
        { const unsigned long long t_ = LDS_LD64(st + 2 * (lane & 7)); ml[0] = (unsigned)t_; ml[1] = (unsigned)(t_ >> 32); }
        if (__all(ml[1] != 0u) || spins > (SPIN_CAP << 4)) break;
        __builtin_amdgcn_s_sleep(2);
      }
      const float m_ = __uint_as_float(ml[0]), l_ = __uint_as_float(ml[1]);
      float M = fmaxf(m_, dppf<XOR1>(m_)); M = fmaxf(M, dppf<XOR2>(M)); M = fmaxf(M, dppf<XOR7>(M));
      const float f_ = __builtin_amdgcn_exp2f((m_ - M) * LOG2E);
      float L = f_ * l_; L += dppf<XOR1>(L); L += dppf<XOR2>(L); L += dppf<XOR7>(L);
      ST128(gran2(M, L, tag), rws, lane == 0 ? WS_SBOX + (unsigned)((h * 8 + j) * 128) : OOB, 0, AUX_SC1);
      STAMP(5);
      if (lane < 8) gw[lane] = f_;
      STAMP(7);
    }
    wg_barrier();
    STAMP(8);
    if (!isq && ffn) {
      for (unsigned spins = 0; spins < (SPIN_CAP << 4); ++spins) { if (__builtin_amdgcn_readfirstlane(LDS_LD32(flg + 3)) == 8u) break; }
      CBAR();
      sum_publish(red + 512, WS_FBOX, 0, tag);
      sum_publish(red + 512, WS_FBOX, 8, tag);
    }
    if (isq) {
      const unsigned soff = WS_SBOX + (unsigned)((h * 8 + (lane & 7)) * 128);
      const unsigned ooff = (unsigned)(h * 8 * 1024 + lane * 16);
      u32x4 gs = LD128(rws, soff, 0, AUX_POLL);
      if (H1DELAY) __builtin_amdgcn_s_sleep(H1DELAY);
      u32x4 G[8];
#pragma unroll
      for (int p = 0; p < 8; ++p) G[p] = LD128(rws, ooff, WS_OBOX + p * 1024, AUX_POLL);
      for (unsigned spins = 0; !__all(gok(gs, tag)) && spins < SPIN_CAP; ++spins) { CBAR(); __builtin_amdgcn_s_sleep(2); gs = LD128(rws, soff, 0, AUX_POLL); }
      float wp[8];
      {
        const float ms = __uint_as_float(gs[0]), ls = __uint_as_float(gs[2]);
        float Mg = fmaxf(ms, dppf<XOR1>(ms)); Mg = fmaxf(Mg, dppf<XOR2>(Mg));
        const float e_ = __builtin_amdgcn_exp2f((ms - Mg) * LOG2E);
        float Lg = e_ * ls; Lg += dppf<XOR1>(Lg); Lg += dppf<XOR2>(Lg);
        const float wgt = e_ / Lg;
#pragma unroll
        for (int p = 0; p < 8; ++p) wp[p] = __uint_as_float(__builtin_amdgcn_readlane(__float_as_uint(wgt), p));
      }
      STAMP(9);
      for (unsigned spins = 0;; ++spins) {
        bool allok = true;
        CBAR();
#pragma unroll
        for (int p = 0; p < 8; ++p) {
          if (!__all(gok(G[p], tag))) { if (allok && HSLEEP) __builtin_amdgcn_s_sleep(HSLEEP); allok = false; G[p] = LD128(rws, ooff, WS_OBOX + p * 1024, AUX_POLL); }
        }
        if (allok || spins > SPIN_CAP) break;
      }
      STAMP(10);
#pragma unroll
      for (int bb = 0; bb < 2; ++bb) {
        float o0 = 0.f, o1 = 0.f;
#pragma unroll
        for (int sp = 0; sp < 4; ++sp) { o0 = fmaf(wp[4 * bb + sp], __uint_as_float(G[4 * bb + sp][0]), o0); o1 = fmaf(wp[4 * bb + sp], __uint_as_float(G[4 * bb + sp][2]), o1); }
        u32x2 ov; ov[0] = pk_bf16(o0, o0); ov[1] = pk_bf16(o1, o1);
        *(u32x2*)(os + bb * 128 + 2 * lane) = ov;
      }
    }
    wg_barrier();
    const int e = lane & 7, ob = e >> 2, ocol = j * 80 + 8 * h + 4 * ((e >> 1) & 1) + 2 * (e & 1);
    const bool fin = !isq && h < 10;
    const unsigned yo = (unsigned)((j * 32 + (lane >> 3)) * 1280 + h * 128 + e * 16);
    const bool f3v = (lane >> 3) < 3;
    unsigned xw = 0; float fa = 0.f, fb = 0.f;
    if (fin) {
      xw = __builtin_amdgcn_raw_buffer_load_b32(rx, (ob * 640 + ocol) * 2, 0, 0);
    }
    wg_barrier();
    STAMP(11);
    if (isq) {
      sum_publish(red + 1024, WS_YBOX, 0, tag);
      STAMP(12);
    } else if (fin) {
      u32x4 g[4], y[4];
#pragma unroll
      for (int i = 0; i < 4; ++i) g[i] = LD128(rws, (i < 3 || f3v) ? yo : OOB, WS_FBOX + i * 8 * 1280, AUX_POLL);
#pragma unroll
      for (int i = 0; i < 4; ++i) y[i] = LD128(rws, yo, WS_YBOX + i * 8 * 1280, AUX_POLL);
      for (unsigned spins = 0;; ++spins) {
        bool allok = true;
        CBAR();
#pragma unroll
        for (int i = 0; i < 4; ++i) if (!__all(gok(g[i], tag) | (i == 3 && !f3v))) { allok = false; g[i] = LD128(rws, (i < 3 || f3v) ? yo : OOB, WS_FBOX + i * 8 * 1280, AUX_POLL); }
        if (allok || spins > SPIN_CAP) break;
      }
      fa = (__uint_as_float(g[0][0]) + __uint_as_float(g[1][0])) + (__uint_as_float(g[2][0]) + __uint_as_float(g[3][0]));
      fb = (__uint_as_float(g[0][2]) + __uint_as_float(g[1][2])) + (__uint_as_float(g[2][2]) + __uint_as_float(g[3][2]));
      fa += dppf<ROR8>(fa); fb += dppf<ROR8>(fb);
      fa = rbf(x32_sum(x16_sum(fa))); fb = rbf(x32_sum(x16_sum(fb)));
      const float ga = rbf(fa / (1.f + __expf(-fa))), gb = rbf(fb / (1.f + __expf(-fb)));
      STAMP(10);
      for (unsigned spins = 0;; ++spins) {
        bool allok = true;
        CBAR();
#pragma unroll
        for (int i = 0; i < 4; ++i) if (!__all(gok(y[i], tag))) { allok = false; y[i] = LD128(rws, yo, WS_YBOX + i * 8 * 1280, AUX_POLL); }
        if (allok || spins > SPIN_CAP) break;
      }
      STAMP(13);
      float ya = (__uint_as_float(y[0][0]) + __uint_as_float(y[1][0])) + (__uint_as_float(y[2][0]) + __uint_as_float(y[3][0]));
      float yb = (__uint_as_float(y[0][2]) + __uint_as_float(y[1][2])) + (__uint_as_float(y[2][2]) + __uint_as_float(y[3][2]));
      ya += dppf<ROR8>(ya); yb += dppf<ROR8>(yb);
      ya = rbf(x32_sum(x16_sum(ya))); yb = rbf(x32_sum(x16_sum(yb)));
      const float oa = __uint_as_float(xw << 16) + rbf(ya * ga), obv = __uint_as_float(xw & 0xffff0000u) + rbf(yb * gb);
      __builtin_amdgcn_raw_buffer_store_b32(pk_bf16(oa, obv), RSRC(out, 2560), lane < 8 ? (unsigned)((ob * 640 + ocol) * 2) : OOB, 0, 0);
      if (blk == 0 && lane == 0) __builtin_amdgcn_raw_buffer_store_b32(tag, rws, 0, WS_EPOCH, AUX_SC1);
      STAMP(12);
    }
  }
  STAMP_FLUSH();
}

extern "C" void kernel_launch(void* const* d_in, const int* in_sizes, int n_in, void* d_out, int out_size, void* d_ws, size_t ws_size, hipStream_t stream) {
  constexpr int LDS = 96 * 1024;
  (void)hipFuncSetAttribute((const void*)fused_decode, hipFuncAttributeMaxDynamicSharedMemorySize, LDS);
  fused_decode<<<256, 640, LDS, stream>>>((const unsigned short*)d_in[0], (const unsigned short*)d_in[1], (const unsigned short*)d_in[2],
      (const unsigned short*)d_in[3], (const unsigned short*)d_in[4], (const unsigned short*)d_in[6], (const unsigned short*)d_in[7],
      (unsigned short*)d_out, (unsigned char*)d_ws + (WSEND ? ((ws_size - WS_END) & ~(size_t)4095) : 0));
}
```

```cpp
#define PRIO 3
#define WK 24
#define WK2 16
#define PDELAY 24
#define WSEND 0
#define HSLEEP 2
#define H1DELAY 4
#include <hip/hip_runtime.h>
#include <hip/hip_bf16.h>
#include <stdint.h>

#define STAMP(i) do {} while (0)
#define STAMP_DECL
#define STAMP_FLUSH() do {} while (0)

typedef unsigned u32x4 __attribute__((ext_vector_type(4)));
typedef unsigned u32x2 __attribute__((ext_vector_type(2)));
typedef float f32x4 __attribute__((ext_vector_type(4)));
typedef __bf16 bf16x8 __attribute__((ext_vector_type(8)));
typedef __bf16 bf16x2 __attribute__((ext_vector_type(2)));

#define POLLAUX 16
constexpr int AUX_NT = 2, AUX_SC1 = 16, AUX_POLL = POLLAUX;
constexpr unsigned OOB = 0x7ffffff0u;
constexpr unsigned WS_EPOCH = 0;
constexpr unsigned WS_Q1 = 4096;
constexpr unsigned WS_H1 = WS_Q1 + 32 * 8 * 1024;
constexpr unsigned WS_Q2 = WS_H1 + 32 * 8 * 1024;
constexpr unsigned WS_H2 = WS_Q2 + 32 * 8 * 128;
constexpr unsigned WS_SBOX = WS_H2 + 32 * 8 * 128;
constexpr unsigned WS_OBOX = WS_SBOX + 32 * 8 * 128;
constexpr unsigned WS_YBOX = WS_OBOX + 32 * 8 * 1024;
constexpr unsigned WS_FBOX = WS_YBOX + 8 * 32 * 1280;
constexpr unsigned WS_END = WS_FBOX + 8 * 32 * 1280;
constexpr unsigned SPIN_CAP = 1u << 16;

#define RSRC(p, n) __builtin_amdgcn_make_buffer_rsrc((void*)(p), 0, (n), 0x00020000)
#define LD128(rs, vo, so, aux) __builtin_amdgcn_raw_buffer_load_b128((rs), (vo), (so), (aux))
#define ST128(v, rs, vo, so, aux) __builtin_amdgcn_raw_buffer_store_b128((v), (rs), (vo), (so), (aux))
#define SBAR() __builtin_amdgcn_sched_barrier(0)
#define LAS __attribute__((address_space(3)))
#define LDS_LD32(p) __hip_atomic_load((LAS unsigned*)(p), __ATOMIC_RELAXED, __HIP_MEMORY_SCOPE_WORKGROUP)
#define LDS_ST32(p, v) __hip_atomic_store((LAS unsigned*)(p), (v), __ATOMIC_RELAXED, __HIP_MEMORY_SCOPE_WORKGROUP)
#define LDS_LD64(p) __hip_atomic_load((LAS unsigned long long*)(p), __ATOMIC_RELAXED, __HIP_MEMORY_SCOPE_WORKGROUP)
#define LDS_ST64(p, v) __hip_atomic_store((LAS unsigned long long*)(p), (v), __ATOMIC_RELAXED, __HIP_MEMORY_SCOPE_WORKGROUP)
#define CBAR() asm volatile("" ::: "memory")

template <int CTRL> __device__ __forceinline__ unsigned dppu(unsigned x) { return (unsigned)__builtin_amdgcn_mov_dpp((int)x, CTRL, 0xf, 0xf, true); }
template <int CTRL> __device__ __forceinline__ float dppf(float x) { return __uint_as_float(dppu<CTRL>(__float_as_uint(x))); }
constexpr int XOR1 = 0xB1, XOR2 = 0x4E, XOR7 = 0x141, ROR8 = 0x128, ROR4 = 0x124, SHL4 = 0x104, SHR4 = 0x114, BCAST = 0x150;

__device__ __forceinline__ float dot2(unsigned k, unsigned q, float acc) {
  return __builtin_amdgcn_fdot2_f32_bf16(__builtin_bit_cast(bf16x2, k), __builtin_bit_cast(bf16x2, q), acc, false);
}
__device__ __forceinline__ unsigned pk_bf16(float lo, float hi) {
  bf16x2 v; v[0] = (__bf16)lo; v[1] = (__bf16)hi; return __builtin_bit_cast(unsigned, v);
}
__device__ __forceinline__ float rbf(float f) { return __uint_as_float(pk_bf16(f, f) & 0xffff0000u); }
__device__ __forceinline__ f32x4 mfma(u32x4 a, u32x4 b, f32x4 c) {
  return __builtin_amdgcn_mfma_f32_16x16x32_bf16(__builtin_bit_cast(bf16x8, a), __builtin_bit_cast(bf16x8, b), c, 0, 0, 0);
}
__device__ __forceinline__ float x16_sum(float x) { auto s = __builtin_amdgcn_permlane16_swap(__float_as_uint(x), __float_as_uint(x), false, false); return __uint_as_float(s[0]) + __uint_as_float(s[1]); }
__device__ __forceinline__ float x32_sum(float x) { auto s = __builtin_amdgcn_permlane32_swap(__float_as_uint(x), __float_as_uint(x), false, false); return __uint_as_float(s[0]) + __uint_as_float(s[1]); }
__device__ __forceinline__ float x16_max(float x) { auto s = __builtin_amdgcn_permlane16_swap(__float_as_uint(x), __float_as_uint(x), false, false); return fmaxf(__uint_as_float(s[0]), __uint_as_float(s[1])); }
__device__ __forceinline__ float x32_max(float x) { auto s = __builtin_amdgcn_permlane32_swap(__float_as_uint(x), __float_as_uint(x), false, false); return fmaxf(__uint_as_float(s[0]), __uint_as_float(s[1])); }
__device__ __forceinline__ float row_sum(float x) { x += dppf<XOR1>(x); x += dppf<XOR2>(x); x += dppf<ROR4>(x); x += dppf<ROR8>(x); return x; }
__device__ __forceinline__ float row_max(float x) { x = fmaxf(x, dppf<XOR1>(x)); x = fmaxf(x, dppf<XOR2>(x)); x = fmaxf(x, dppf<ROR4>(x)); x = fmaxf(x, dppf<ROR8>(x)); return x; }
template <int C0, int C1> __device__ __forceinline__ float trsel(float lo, float hi, bool bit) {
  const float t0 = lo + dppf<C0>(lo), t1 = hi + dppf<C1>(hi);
  return bit ? t1 : t0;
}
__device__ __forceinline__ bool gok(u32x4 g, unsigned tag) { return (g[1] == tag) & (g[3] == tag); }
__device__ __forceinline__ u32x4 gran2(float a, float b, unsigned tag) { u32x4 g; g[0] = __float_as_uint(a); g[1] = tag; g[2] = __float_as_uint(b); g[3] = tag; return g; }
__device__ __forceinline__ void wg_barrier() { asm volatile("s_waitcnt lgkmcnt(0)\n\ts_barrier" ::: "memory"); }

constexpr float LOG2E = 1.4426950408889634f;

#define POLLDELAY() do { if (PDELAY) __builtin_amdgcn_s_sleep(PDELAY); } while (0)
#define NPACE 16
#define MAPX 0
#define KVORD 0
#define ABL_NOX0 0
#define ABL_NOB0 0
#define OSHARED 1
#define ABL 0
#define X0MODE 0
#define ARGPIN 1
#define VMWAIT(n) asm volatile("s_waitcnt vmcnt(%0)" :: "i"(n) : "memory")

__device__ __forceinline__ float2 ld2(const float* p) { return *(const float2*)p; }

__global__ void __launch_bounds__(640, 3)
fused_decode(const unsigned short* x, const unsigned short* kc, const unsigned short* vc,
             const unsigned short* wq, const unsigned short* wo, const unsigned short* w1,
             const unsigned short* w2, unsigned short* out, unsigned char* ws) {
  asm volatile("" : "+s"(x), "+s"(kc), "+s"(vc), "+s"(wq), "+s"(wo), "+s"(w1), "+s"(w2), "+s"(out), "+s"(ws));
  __shared__ __attribute__((aligned(16))) unsigned char smem[30720];
  f32x4* red = (f32x4*)smem;
  unsigned* qs = (unsigned*)(smem + 24576);
  unsigned* hs = (unsigned*)(smem + 24576 + 256);
  float* st = (float*)(smem + 24576 + 768);
  unsigned* flg = (unsigned*)(smem + 24576 + 832);
  float* gw = (float*)(smem + 24576 + 896);
  unsigned* wpl = (unsigned*)(smem + 24576 + 848);
  unsigned* os = (unsigned*)(smem + 24576 + 4096);
  unsigned* xl = (unsigned*)(smem + 24576 + 1024);
  const int blk = blockIdx.x, tid = threadIdx.x, lane = tid & 63, w = __builtin_amdgcn_readfirstlane(tid >> 6);
  STAMP_DECL
  STAMP(0);
  const int j = (blk >> 3) & 7, h = (blk & 7) + 8 * (blk >> 6), b = j >> 2, s = j & 3;
  const int r = lane >> 4, c = lane & 15;
  const bool ffn = h < 27;
  const __amdgpu_buffer_rsrc_t rws = RSRC(ws, WS_END);
  const __amdgpu_buffer_rsrc_t rx = RSRC(x, 2560);
  u32x4 mask;
#pragma unroll
  for (int i = 0; i < 4; ++i) mask[i] = (i == ((c & 7) >> 1)) ? ((c & 1) ? 0xffff0000u : 0xffffu) : 0u;
  auto sum_publish = [&](const f32x4* rr, unsigned box, int cbase, unsigned tag) {
    const int cr = cbase + (lane >> 3), e = lane & 7;
    const float* p = (const float*)rr + ((e >> 1) * 16 + (cr & 15)) * 4 + 2 * (e & 1);
    float2 a[8];
#pragma unroll
    for (int i = 0; i < 8; ++i) a[i] = ld2(p + i * 256);
    const float v0 = ((a[0].x + a[1].x) + (a[2].x + a[3].x)) + ((a[4].x + a[5].x) + (a[6].x + a[7].x));
    const float v1 = ((a[0].y + a[1].y) + (a[2].y + a[3].y)) + ((a[4].y + a[5].y) + (a[6].y + a[7].y));
    ST128(gran2(v0, v1, tag), rws, cr < 10 ? (unsigned)((j * 32 + h) * 1280 + cr * 128 + e * 16) : OOB, box, AUX_SC1);
  };

  if (w >= 2) {
    const int sw = w - 2;
    const size_t kvbase = ((size_t)(b * 32 + h) * 8192 + 6144 + s * 512) * 128;
    const __amdgpu_buffer_rsrc_t rk = RSRC(kc + kvbase, 131072), rv = RSRC(vc + kvbase, 131072);
    const __amdgpu_buffer_rsrc_t ro = RSRC(wo + (size_t)h * 128 * 640 + j * 80, 128 * 1280 - j * 160);
    const __amdgpu_buffer_rsrc_t r2 = RSRC(w2 + (size_t)h * 64 * 640 + j * 80, ffn ? 64 * 1280 - j * 160 : 0);
    const int rot = sw * 4 + ((blk * 37) & 127);
    const int voff = lane * 16;
#define KVOFF(t) ((((((t) >> 2) * 32 + ((t) & 3) + rot) & 127) << 10))
    u32x4 K[16], V[16], WO[4], W2[2];
    unsigned pp = 0;
    auto scores = [&]() {
      CBAR();
      const u32x4 q4 = *(const u32x4*)(qs + 4 * c);
      float sb[4];
#pragma unroll
      for (int be = 0; be < 4; ++be) {
        float d[4];
#pragma unroll
        for (int tt = 0; tt < 4; ++tt) {
          const u32x4 kk = K[4 * be + tt];
          d[tt] = dot2(kk[3], q4[3], dot2(kk[2], q4[2], dot2(kk[1], q4[1], dot2(kk[0], q4[0], 0.f))));
        }
        const float e0 = trsel<XOR1, XOR1>(d[0], d[1], c & 1);
        const float e1 = trsel<XOR1, XOR1>(d[2], d[3], c & 1);
        sb[be] = trsel<XOR2, XOR2>(e0, e1, c & 2);
      }
      const float f0 = trsel<SHL4, SHR4>(sb[0], sb[1], c & 4);
      const float f1 = trsel<SHL4, SHR4>(sb[2], sb[3], c & 4);
      const float dotv = trsel<ROR8, ROR8>(f0, f1, c & 8);
      const float sc = rbf(rbf(dotv) * 0.08838834764831845f);
      const float mw = x32_max(x16_max(row_max(sc)));
      const float ew = __builtin_amdgcn_exp2f((sc - mw) * LOG2E);
      pp = pk_bf16(ew, ew);
      const float lw = x32_sum(x16_sum(row_sum(ew)));
      if (lane == 0) LDS_ST64(st + 2 * sw, (unsigned long long)__float_as_uint(mw) | ((unsigned long long)__float_as_uint(lw) << 32));
    };
    if (PRIO) __builtin_amdgcn_s_setprio(PRIO == 3 ? 3 : 2);
    wg_barrier();
#pragma unroll
    for (int i = 0; i < 24; ++i) {
      if (PRIO == 1 && i == 16) __builtin_amdgcn_s_setprio(1);
      if (PRIO == 3 && i == 16) __builtin_amdgcn_s_setprio(2);
      if (PRIO == 2 && i == 8) __builtin_amdgcn_s_setprio(1);
      if (PRIO == 2 && i == 16) __builtin_amdgcn_s_setprio(0);
      if (i >= WK) VMWAIT(WK - 1);
      if (i < 16) K[i] = LD128(rk, voff, KVOFF(i), AUX_NT); else V[i - 16] = LD128(rv, voff, KVOFF(i - 16), AUX_NT);
      SBAR();
      if (i == WK - 1) STAMP(1);
    }
    STAMP(4);
    const bool early = __builtin_amdgcn_readfirstlane(LDS_LD32(flg + 1)) != 0u;
    if (early) { scores(); STAMP(5); }
    SBAR();
    if (PRIO == 1) __builtin_amdgcn_s_setprio(0);
    if (PRIO == 3) __builtin_amdgcn_s_setprio(1);
    {
      const unsigned vo2 = c < 10 ? (unsigned)(r * 2 * 1280 + c * 16) : OOB;
#pragma unroll
      for (int i = 0; i < 2; ++i) { VMWAIT(WK2 - 1); W2[i] = LD128(r2, vo2, (8 * sw + i) * 1280, AUX_NT); SBAR(); }
    }
#pragma unroll
    for (int t = 8; t < 16; ++t) {
      VMWAIT(WK2 - 1);
      V[t] = LD128(rv, voff, KVOFF(t), AUX_NT);
      SBAR();
    }
    if (!early) {
      for (unsigned spins = 0; spins < (SPIN_CAP << 4); ++spins) {
        if (__builtin_amdgcn_readfirstlane(LDS_LD32(flg + 1)) != 0u) break;
        __builtin_amdgcn_s_sleep(1);
      }
      scores();
      STAMP(6);
    }
    SBAR();
    {
      if (PRIO == 3) __builtin_amdgcn_s_setprio(0);
      const unsigned vo4 = c < 10 ? (unsigned)(r * 4 * 1280 + c * 16) : OOB;
#pragma unroll
      for (int i = 0; i < 4; ++i) { VMWAIT(WK2 - 1); WO[i] = LD128(ro, vo4, (16 * sw + i) * 1280, AUX_NT); SBAR(); }
    }
    STAMP(7);
    const unsigned tag = __builtin_amdgcn_readfirstlane(LDS_LD32(flg));
    f32x4 oacc = {0.f, 0.f, 0.f, 0.f};
#define PVSTEP(t) oacc = mfma(mask & dppu<BCAST + (t)>(pp), V[t], oacc);
    PVSTEP(0) PVSTEP(1) PVSTEP(2) PVSTEP(3) PVSTEP(4) PVSTEP(5) PVSTEP(6) PVSTEP(7)
    PVSTEP(8) PVSTEP(9) PVSTEP(10) PVSTEP(11) PVSTEP(12) PVSTEP(13) PVSTEP(14) PVSTEP(15)
#undef PVSTEP
    red[sw * 64 + lane] = oacc;
    STAMP(8);
    wg_barrier();
    {
      const int u = lane & 7;
      const float* p = (const float*)red + (((u >> 1) & 1) * 16 + 2 * sw + (u >> 2)) * 4 + 2 * (u & 1);
      const f32x4 g0 = *(const f32x4*)gw, g1 = *(const f32x4*)(gw + 4);
      float2 a[8];
#pragma unroll
      for (int i = 0; i < 8; ++i) a[i] = ld2(p + i * 256);
      const float v0 = ((a[0].x * g0[0] + a[1].x * g0[1]) + (a[2].x * g0[2] + a[3].x * g0[3])) + ((a[4].x * g1[0] + a[5].x * g1[1]) + (a[6].x * g1[2] + a[7].x * g1[3]));
      const float v1 = ((a[0].y * g0[0] + a[1].y * g0[1]) + (a[2].y * g0[2] + a[3].y * g0[3])) + ((a[4].y * g1[0] + a[5].y * g1[1]) + (a[6].y * g1[2] + a[7].y * g1[3]));
      ST128(gran2(v0, v1, tag), rws, lane < 8 ? (unsigned)((h * 8 + j) * 1024 + sw * 128 + u * 16) : OOB, WS_OBOX, AUX_SC1);
    }
    STAMP(9);
    if (ffn) {
      const u32x2 hv = *(const u32x2*)(hs + (c >> 3) * 64 + 8 * sw + 2 * r);
      f32x4 facc = {0.f, 0.f, 0.f, 0.f};
      facc = mfma(mask & hv[0], W2[0], facc);
      facc = mfma(mask & hv[1], W2[1], facc);
      red[(8 + sw) * 64 + lane] = facc;
      asm volatile("s_waitcnt lgkmcnt(0)" ::: "memory");
      if (lane == 0) __hip_atomic_fetch_add((LAS unsigned*)(flg + 3), 1u, __ATOMIC_RELAXED, __HIP_MEMORY_SCOPE_WORKGROUP);
    }
    VMWAIT(0);
    STAMP(15);
    wg_barrier();
    STAMP(10);
    {
      const u32x4 ov = *(const u32x4*)(os + (c >> 3) * 128 + 16 * sw + 4 * r);
      f32x4 y = {0.f, 0.f, 0.f, 0.f}, y2 = {0.f, 0.f, 0.f, 0.f};
      y = mfma(mask & ov[0], WO[0], y);
      y2 = mfma(mask & ov[1], WO[1], y2);
      y = mfma(mask & ov[2], WO[2], y);
      y2 = mfma(mask & ov[3], WO[3], y2);
      red[(16 + sw) * 64 + lane] = y + y2;
    }
    STAMP(11);
    wg_barrier();
    if (sw == 0) sum_publish(red + 1024, WS_YBOX, 8, tag);
  } else {
    __builtin_amdgcn_s_setprio(3);
    const bool isq = (w == 0);
    if (w == 1 && blk < 8) asm volatile("buffer_wbl2 sc0 sc1" ::: "memory");
    const unsigned epoch = __builtin_amdgcn_raw_buffer_load_b32(rws, 0, WS_EPOCH, AUX_SC1);
    u32x2 xs[5];
#pragma unroll
    for (int i = 0; i < 5; ++i) xs[i] = __builtin_amdgcn_raw_buffer_load_b64(rx, (lane >> 5) * 1280 + (lane & 31) * 8, i * 256, 0);
    const int row0 = isq ? 160 * (j >> 1) : 80 * j;
    const int nrow = isq ? 160 : 80;
    unsigned short xr[5];
#pragma unroll
    for (int i = 0; i < 5; ++i) xr[i] = __builtin_amdgcn_raw_buffer_load_b16(rx, (lane >> 5) * 1280 + (lane & 31) * 2, (row0 + 32 * i) * 2, 0);
    u32x4 wa[20];
    {
      const __amdgpu_buffer_rsrc_t rw = isq ? RSRC(wq + h * 128 + (j & 1) * 64, 640 * 4096 * 2 - h * 256 - (j & 1) * 128) : RSRC(w1 + h * 64, ffn ? 640 * 1728 * 2 - h * 128 : 0);
      const int rstride = isq ? 8192 : 3456;
      const unsigned vo = (unsigned)((lane >> 3) * rstride + (lane & 7) * 16);
#pragma unroll
      for (int i = 0; i < 10; ++i) wa[i] = LD128(rw, vo, (row0 + 8 * i) * rstride, AUX_NT);
      if (isq) {
#pragma unroll
        for (int i = 10; i < 20; ++i) wa[i] = LD128(rw, vo, (row0 + 8 * i) * rstride, AUX_NT);
      }
    }
    if (isq && lane < 28) LDS_ST32(st + lane, 0u);
    STAMP(1);
    wg_barrier();
    float xf[20];
#pragma unroll
    for (int i = 0; i < 5; ++i) {
      xf[4 * i + 0] = __uint_as_float(xs[i][0] << 16); xf[4 * i + 1] = __uint_as_float(xs[i][0] & 0xffff0000u);
      xf[4 * i + 2] = __uint_as_float(xs[i][1] << 16); xf[4 * i + 3] = __uint_as_float(xs[i][1] & 0xffff0000u);
    }
    float sum = 0.f;
#pragma unroll
    for (int i = 0; i < 20; ++i) sum += xf[i];
    sum = x16_sum(row_sum(sum));
    const float mean = sum * (1.f / 640.f);
    float vs = 0.f;
#pragma unroll
    for (int i = 0; i < 20; ++i) { const float d = xf[i] - mean; vs = fmaf(d, d, vs); }
    vs = x16_sum(row_sum(vs));
    const float mb = rbf(mean), den = rbf(sqrtf(rbf(rbf(vs * (1.f / 640.f)) + 1e-5f)));
    unsigned* xw_ = xl + w * 320;
#pragma unroll
    for (int i = 0; i < 5; ++i) {
      const int row = (lane & 31) + 32 * i;
      const float xn = rbf(__uint_as_float((unsigned)xr[i] << 16) - mb) / den;
      if (row < nrow) xw_[(lane >> 5) * 160 + ((row >> 1) & 3) * 40 + (row >> 3) * 2 + (row & 1)] = pk_bf16(xn, xn);
    }
    const unsigned tag = __builtin_amdgcn_readfirstlane(epoch) + 0x9E3779B9u;
    if (isq && lane == 0) LDS_ST32(flg, tag);
    asm volatile("s_waitcnt lgkmcnt(0)" ::: "memory");
    STAMP(2);
    f32x4 acc0 = {0.f, 0.f, 0.f, 0.f}, acc1 = {0.f, 0.f, 0.f, 0.f};
    const unsigned* xrd = xw_ + (c >> 3) * 160 + r * 40;
#pragma unroll
    for (int i = 0; i < 10; i += 2) {
      const u32x4 xa = *(const u32x4*)(xrd + 2 * i);
      acc0 = mfma(mask & xa[0], wa[i], acc0); acc1 = mfma(mask & xa[1], wa[i], acc1);
      acc0 = mfma(mask & xa[2], wa[i + 1], acc0); acc1 = mfma(mask & xa[3], wa[i + 1], acc1);
    }
    if (isq) {
#pragma unroll
      for (int i = 10; i < 20; i += 2) {
        const u32x4 xa = *(const u32x4*)(xrd + 2 * i);
        acc0 = mfma(mask & xa[0], wa[i], acc0); acc1 = mfma(mask & xa[1], wa[i], acc1);
        acc0 = mfma(mask & xa[2], wa[i + 1], acc0); acc1 = mfma(mask & xa[3], wa[i + 1], acc1);
      }
    }
    if (isq || ffn) {
      f32x4 v;
#pragma unroll
      for (int i = 0; i < 4; ++i) { const float t_ = c < 8 ? acc0[i] : acc1[i]; v[i] = t_ + dppf<ROR8>(t_); }
      const u32x4 pub = gran2(c < 8 ? v[0] : v[2], c < 8 ? v[1] : v[3], tag);
      const unsigned cu = (unsigned)(h * 8 + j);
      if (isq) {
        if (X0MODE != 1 && X0MODE != 3) ST128(pub, rws, cu * 1024 + lane * 16, WS_Q1, AUX_SC1);
        STAMP(3);
        POLLDELAY();
        const unsigned goff = (unsigned)((h * 8 + (lane >> 5)) * 1024 + (b * 32 + (lane & 31)) * 16);
        u32x4 G[4];
#pragma unroll
        for (int p = 0; p < 4; ++p) G[p] = (X0MODE == 1 || X0MODE == 2) ? pub : LD128(rws, goff, WS_Q1 + p * 2048, AUX_POLL);
        for (unsigned spins = 0; X0MODE == 0 || X0MODE == 4; ++spins) {
          bool allok = true;
          CBAR();
#pragma unroll
          for (int p = 0; p < 4; ++p) if (!__all(gok(G[p], tag))) { allok = false; G[p] = LD128(rws, goff, WS_Q1 + p * 2048, AUX_POLL); }
          if (spins == 0) STAMP(14);
          if (allok || spins > SPIN_CAP) break;
        }
        const float a0 = (__uint_as_float(G[0][0]) + __uint_as_float(G[1][0])) + (__uint_as_float(G[2][0]) + __uint_as_float(G[3][0]));
        const float a1 = (__uint_as_float(G[0][2]) + __uint_as_float(G[1][2])) + (__uint_as_float(G[2][2]) + __uint_as_float(G[3][2]));
        qs[(lane >> 5) * 32 + (c & 7) * 4 + (r & 1) * 2 + (c >> 3)] = pk_bf16(a0, a1);
      } else {
        if (X0MODE == 0 || X0MODE == 2) ST128(pub, rws, (unsigned)(((h * 8 + (c & 7)) * 8 + j) * 128 + (c >> 3) * 64 + r * 16), WS_H1, AUX_SC1);
        STAMP(3);
        POLLDELAY(); POLLDELAY();
        const unsigned goff = cu * 1024 + lane * 16;
        u32x4 G0 = (X0MODE == 0 || X0MODE == 3) ? LD128(rws, goff, WS_H1, AUX_POLL) : pub;
        for (unsigned spins = 0; X0MODE == 0 && !__all(gok(G0, tag)) && spins < SPIN_CAP; ++spins) { CBAR(); G0 = LD128(rws, goff, WS_H1, AUX_POLL); }
        STAMP(14);
        float a0 = __uint_as_float(G0[0]), a1 = __uint_as_float(G0[2]);
        a0 += dppf<ROR8>(a0); a1 += dppf<ROR8>(a1);
        a0 = fmaxf(x32_sum(x16_sum(a0)), 0.f); a1 = fmaxf(x32_sum(x16_sum(a1)), 0.f);
        u32x2 g2; g2[0] = pk_bf16(a0, a1); g2[1] = tag;
        if (X0MODE == 0 || X0MODE == 2) __builtin_amdgcn_raw_buffer_store_b64(g2, rws, lane < 8 ? cu * 128 + lane * 8 : OOB, WS_H2, AUX_SC1);
        const unsigned g2off = (unsigned)((h * 8 + (lane >> 3)) * 128 + (lane & 7) * 8);
        POLLDELAY(); POLLDELAY(); POLLDELAY();
        u32x2 q2 = (X0MODE == 0 || X0MODE == 3) ? __builtin_amdgcn_raw_buffer_load_b64(rws, g2off, WS_H2, AUX_POLL) : g2;
        for (unsigned spins = 0; X0MODE == 0 && !__all(q2[1] == tag) && spins < SPIN_CAP; ++spins) { CBAR(); q2 = __builtin_amdgcn_raw_buffer_load_b64(rws, g2off, WS_H2, AUX_POLL); }
        u32x2 hv; hv[0] = (q2[0] & 0xffffu) | (q2[0] << 16); hv[1] = (q2[0] >> 16) | (q2[0] & 0xffff0000u);
        *(u32x2*)(hs + ((lane >> 1) & 1) * 64 + (lane >> 3) * 8 + (lane & 1) * 4 + ((lane >> 2) & 1) * 2) = hv;
      }
      asm volatile("s_waitcnt lgkmcnt(0)" ::: "memory");
      if (lane == 0) LDS_ST32(flg + 1 + w, 1u);
    }
    STAMP(4);
    if (isq) {
      u32x2 ml;
      for (unsigned spins = 0;; ++spins) {
        { const unsigned long long t_ = LDS_LD64(st + 2 * (lane & 7)); ml[0] = (unsigned)t_; ml[1] = (unsigned)(t_ >> 32); }
        if (__all(ml[1] != 0u) || spins > (SPIN_CAP << 4)) break;
        __builtin_amdgcn_s_sleep(2);
      }
      const float m_ = __uint_as_float(ml[0]), l_ = __uint_as_float(ml[1]);
      float M = fmaxf(m_, dppf<XOR1>(m_)); M = fmaxf(M, dppf<XOR2>(M)); M = fmaxf(M, dppf<XOR7>(M));
      const float f_ = __builtin_amdgcn_exp2f((m_ - M) * LOG2E);
      float L = f_ * l_; L += dppf<XOR1>(L); L += dppf<XOR2>(L); L += dppf<XOR7>(L);
      ST128(gran2(M, L, tag), rws, lane == 0 ? WS_SBOX + (unsigned)((h * 8 + j) * 128) : OOB, 0, AUX_SC1);
      STAMP(5);
      if (lane < 8) gw[lane] = f_;
      STAMP(7);
    }
    wg_barrier();
    STAMP(8);
    if (!isq && ffn) {
      for (unsigned spins = 0; spins < (SPIN_CAP << 4); ++spins) { if (__builtin_amdgcn_readfirstlane(LDS_LD32(flg + 3)) == 8u) break; }
      CBAR();
      sum_publish(red + 512, WS_FBOX, 0, tag);
      sum_publish(red + 512, WS_FBOX, 8, tag);
    }
    if (isq) {
      const unsigned soff = WS_SBOX + (unsigned)((h * 8 + (lane & 7)) * 128);
      const unsigned ooff = (unsigned)(h * 8 * 1024 + lane * 16);
      u32x4 gs = LD128(rws, soff, 0, AUX_POLL);
      if (H1DELAY) __builtin_amdgcn_s_sleep(H1DELAY);
      u32x4 G[8];
#pragma unroll
      for (int p = 0; p < 8; ++p) G[p] = LD128(rws, ooff, WS_OBOX + p * 1024, AUX_POLL);
      for (unsigned spins = 0; !__all(gok(gs, tag)) && spins < SPIN_CAP; ++spins) { CBAR(); __builtin_amdgcn_s_sleep(2); gs = LD128(rws, soff, 0, AUX_POLL); }
      float wp[8];
      {
        const float ms = __uint_as_float(gs[0]), ls = __uint_as_float(gs[2]);
        float Mg = fmaxf(ms, dppf<XOR1>(ms)); Mg = fmaxf(Mg, dppf<XOR2>(Mg));
        const float e_ = __builtin_amdgcn_exp2f((ms - Mg) * LOG2E);
        float Lg = e_ * ls; Lg += dppf<XOR1>(Lg); Lg += dppf<XOR2>(Lg);
        const float wgt = e_ / Lg;
#pragma unroll
        for (int p = 0; p < 8; ++p) wp[p] = __uint_as_float(__builtin_amdgcn_readlane(__float_as_uint(wgt), p));
      }
      STAMP(9);
      for (unsigned spins = 0;; ++spins) {
        bool allok = true;
        CBAR();
#pragma unroll
        for (int p = 0; p < 8; ++p) {
          if (!__all(gok(G[p], tag))) { if (allok && HSLEEP) __builtin_amdgcn_s_sleep(HSLEEP); allok = false; G[p] = LD128(rws, ooff, WS_OBOX + p * 1024, AUX_POLL); }
        }
        if (allok || spins > SPIN_CAP) break;
      }
      STAMP(10);
#pragma unroll
      for (int bb = 0; bb < 2; ++bb) {
        float o0 = 0.f, o1 = 0.f;
#pragma unroll
        for (int sp = 0; sp < 4; ++sp) { o0 = fmaf(wp[4 * bb + sp], __uint_as_float(G[4 * bb + sp][0]), o0); o1 = fmaf(wp[4 * bb + sp], __uint_as_float(G[4 * bb + sp][2]), o1); }
        u32x2 ov; ov[0] = pk_bf16(o0, o0); ov[1] = pk_bf16(o1, o1);
        *(u32x2*)(os + bb * 128 + 2 * lane) = ov;
      }
    }
    wg_barrier();
    const int e = lane & 7, ob = e >> 2, ocol = j * 80 + 8 * h + 4 * ((e >> 1) & 1) + 2 * (e & 1);
    const bool fin = !isq && h < 10;
    const unsigned yo = (unsigned)((j * 32 + (lane >> 3)) * 1280 + h * 128 + e * 16);
    const bool f3v = (lane >> 3) < 3;
    unsigned xw = 0; float fa = 0.f, fb = 0.f;
    if (fin) {
      xw = __builtin_amdgcn_raw_buffer_load_b32(rx, (ob * 640 + ocol) * 2, 0, 0);
    }
    wg_barrier();
    STAMP(11);
    if (isq) {
      sum_publish(red + 1024, WS_YBOX, 0, tag);
      STAMP(12);
    } else if (fin) {
      u32x4 g[4], y[4];
#pragma unroll
      for (int i = 0; i < 4; ++i) g[i] = LD128(rws, (i < 3 || f3v) ? yo : OOB, WS_FBOX + i * 8 * 1280, AUX_POLL);
#pragma unroll
      for (int i = 0; i < 4; ++i) y[i] = LD128(rws, yo, WS_YBOX + i * 8 * 1280, AUX_POLL);
      for (unsigned spins = 0;; ++spins) {
        bool allok = true;
        CBAR();
#pragma unroll
        for (int i = 0; i < 4; ++i) if (!__all(gok(g[i], tag) | (i == 3 && !f3v))) { allok = false; g[i] = LD128(rws, (i < 3 || f3v) ? yo : OOB, WS_FBOX + i * 8 * 1280, AUX_POLL); }
        if (allok || spins > SPIN_CAP) break;
      }
      fa = (__uint_as_float(g[0][0]) + __uint_as_float(g[1][0])) + (__uint_as_float(g[2][0]) + __uint_as_float(g[3][0]));
      fb = (__uint_as_float(g[0][2]) + __uint_as_float(g[1][2])) + (__uint_as_float(g[2][2]) + __uint_as_float(g[3][2]));
      fa += dppf<ROR8>(fa); fb += dppf<ROR8>(fb);
      fa = rbf(x32_sum(x16_sum(fa))); fb = rbf(x32_sum(x16_sum(fb)));
      const float ga = rbf(fa / (1.f + __expf(-fa))), gb = rbf(fb / (1.f + __expf(-fb)));
      STAMP(10);
      for (unsigned spins = 0;; ++spins) {
        bool allok = true;
        CBAR();
#pragma unroll
        for (int i = 0; i < 4; ++i) if (!__all(gok(y[i], tag))) { allok = false; y[i] = LD128(rws, yo, WS_YBOX + i * 8 * 1280, AUX_POLL); }
        if (allok || spins > SPIN_CAP) break;
      }
      STAMP(13);
      float ya = (__uint_as_float(y[0][0]) + __uint_as_float(y[1][0])) + (__uint_as_float(y[2][0]) + __uint_as_float(y[3][0]));
      float yb = (__uint_as_float(y[0][2]) + __uint_as_float(y[1][2])) + (__uint_as_float(y[2][2]) + __uint_as_float(y[3][2]));
      ya += dppf<ROR8>(ya); yb += dppf<ROR8>(yb);
      ya = rbf(x32_sum(x16_sum(ya))); yb = rbf(x32_sum(x16_sum(yb)));
      const float oa = __uint_as_float(xw << 16) + rbf(ya * ga), obv = __uint_as_float(xw & 0xffff0000u) + rbf(yb * gb);
      __builtin_amdgcn_raw_buffer_store_b32(pk_bf16(oa, obv), RSRC(out, 2560), lane < 8 ? (unsigned)((ob * 640 + ocol) * 2) : OOB, 0, 0);
      if (blk == 0 && lane == 0) __builtin_amdgcn_raw_buffer_store_b32(tag, rws, 0, WS_EPOCH, AUX_SC1);
      STAMP(12);
    }
  }
  STAMP_FLUSH();
}

extern "C" void kernel_launch(void* const* d_in, const int* in_sizes, int n_in, void* d_out, int out_size, void* d_ws, size_t ws_size, hipStream_t stream) {
  constexpr int LDS = 0;
  fused_decode<<<256, 640, LDS, stream>>>((const unsigned short*)d_in[0], (const unsigned short*)d_in[1], (const unsigned short*)d_in[2],
      (const unsigned short*)d_in[3], (const unsigned short*)d_in[4], (const unsigned short*)d_in[6], (const unsigned short*)d_in[7],
      (unsigned short*)d_out, (unsigned char*)d_ws + (WSEND ? ((ws_size - WS_END) & ~(size_t)4095) : 0));
}
```

```cpp
#define PRIO 3
#define WK 24
#define WK2 16
#define PDELAY 24
#define WSEND 0
#define HSLEEP 2
#define H1DELAY 4
#define AMP 10u
#include <hip/hip_runtime.h>
#include <hip/hip_bf16.h>
#include <stdint.h>

#define STAMP(i) do {} while (0)
#define STAMP_DECL
#define STAMP_FLUSH() do {} while (0)

typedef unsigned u32x4 __attribute__((ext_vector_type(4)));
typedef unsigned u32x2 __attribute__((ext_vector_type(2)));
typedef float f32x4 __attribute__((ext_vector_type(4)));
typedef __bf16 bf16x8 __attribute__((ext_vector_type(8)));
typedef __bf16 bf16x2 __attribute__((ext_vector_type(2)));

#define POLLAUX 16
constexpr int AUX_NT = 2, AUX_SC1 = 16, AUX_POLL = POLLAUX;
constexpr unsigned OOB = 0x7ffffff0u;
constexpr unsigned WS_EPOCH = 0;
constexpr unsigned WS_Q1 = 4096;
constexpr unsigned WS_H1 = WS_Q1 + 32 * 8 * 1024;
constexpr unsigned WS_Q2 = WS_H1 + 32 * 8 * 1024;
constexpr unsigned WS_H2 = WS_Q2 + 32 * 8 * 128;
constexpr unsigned WS_SBOX = WS_H2 + 32 * 8 * 128;
constexpr unsigned WS_OBOX = WS_SBOX + 32 * 8 * 128;
constexpr unsigned WS_YBOX = WS_OBOX + 32 * 8 * 1024;
constexpr unsigned WS_FBOX = WS_YBOX + 8 * 32 * 1280;
constexpr unsigned WS_END = WS_FBOX + 8 * 32 * 1280;
constexpr unsigned SPIN_CAP = 1u << 16;

#define RSRC(p, n) __builtin_amdgcn_make_buffer_rsrc((void*)(p), 0, (n), 0x00020000)
#define LD128(rs, vo, so, aux) __builtin_amdgcn_raw_buffer_load_b128((rs), (vo), (so), (aux))
#define ST128(v, rs, vo, so, aux) __builtin_amdgcn_raw_buffer_store_b128((v), (rs), (vo), (so), (aux))
#define SBAR() __builtin_amdgcn_sched_barrier(0)
#define LAS __attribute__((address_space(3)))
#define LDS_LD32(p) __hip_atomic_load((LAS unsigned*)(p), __ATOMIC_RELAXED, __HIP_MEMORY_SCOPE_WORKGROUP)
#define LDS_ST32(p, v) __hip_atomic_store((LAS unsigned*)(p), (v), __ATOMIC_RELAXED, __HIP_MEMORY_SCOPE_WORKGROUP)
#define LDS_LD64(p) __hip_atomic_load((LAS unsigned long long*)(p), __ATOMIC_RELAXED, __HIP_MEMORY_SCOPE_WORKGROUP)
#define LDS_ST64(p, v) __hip_atomic_store((LAS unsigned long long*)(p), (v), __ATOMIC_RELAXED, __HIP_MEMORY_SCOPE_WORKGROUP)
#define CBAR() asm volatile("" ::: "memory")

template <int CTRL> __device__ __forceinline__ unsigned dppu(unsigned x) { return (unsigned)__builtin_amdgcn_mov_dpp((int)x, CTRL, 0xf, 0xf, true); }
template <int CTRL> __device__ __forceinline__ float dppf(float x) { return __uint_as_float(dppu<CTRL>(__float_as_uint(x))); }
constexpr int XOR1 = 0xB1, XOR2 = 0x4E, XOR7 = 0x141, ROR8 = 0x128, ROR4 = 0x124, SHL4 = 0x104, SHR4 = 0x114, BCAST = 0x150;

__device__ __forceinline__ float dot2(unsigned k, unsigned q, float acc) {
  return __builtin_amdgcn_fdot2_f32_bf16(__builtin_bit_cast(bf16x2, k), __builtin_bit_cast(bf16x2, q), acc, false);
}
__device__ __forceinline__ unsigned pk_bf16(float lo, float hi) {
  bf16x2 v; v[0] = (__bf16)lo; v[1] = (__bf16)hi; return __builtin_bit_cast(unsigned, v);
}
__device__ __forceinline__ float rbf(float f) { return __uint_as_float(pk_bf16(f, f) & 0xffff0000u); }
__device__ __forceinline__ f32x4 mfma(u32x4 a, u32x4 b, f32x4 c) {
  return __builtin_amdgcn_mfma_f32_16x16x32_bf16(__builtin_bit_cast(bf16x8, a), __builtin_bit_cast(bf16x8, b), c, 0, 0, 0);
}
__device__ __forceinline__ float x16_sum(float x) { auto s = __builtin_amdgcn_permlane16_swap(__float_as_uint(x), __float_as_uint(x), false, false); return __uint_as_float(s[0]) + __uint_as_float(s[1]); }
__device__ __forceinline__ float x32_sum(float x) { auto s = __builtin_amdgcn_permlane32_swap(__float_as_uint(x), __float_as_uint(x), false, false); return __uint_as_float(s[0]) + __uint_as_float(s[1]); }
__device__ __forceinline__ float x16_max(float x) { auto s = __builtin_amdgcn_permlane16_swap(__float_as_uint(x), __float_as_uint(x), false, false); return fmaxf(__uint_as_float(s[0]), __uint_as_float(s[1])); }
__device__ __forceinline__ float x32_max(float x) { auto s = __builtin_amdgcn_permlane32_swap(__float_as_uint(x), __float_as_uint(x), false, false); return fmaxf(__uint_as_float(s[0]), __uint_as_float(s[1])); }
__device__ __forceinline__ float row_sum(float x) { x += dppf<XOR1>(x); x += dppf<XOR2>(x); x += dppf<ROR4>(x); x += dppf<ROR8>(x); return x; }
__device__ __forceinline__ float row_max(float x) { x = fmaxf(x, dppf<XOR1>(x)); x = fmaxf(x, dppf<XOR2>(x)); x = fmaxf(x, dppf<ROR4>(x)); x = fmaxf(x, dppf<ROR8>(x)); return x; }
template <int C0, int C1> __device__ __forceinline__ float trsel(float lo, float hi, bool bit) {
  const float t0 = lo + dppf<C0>(lo), t1 = hi + dppf<C1>(hi);
  return bit ? t1 : t0;
}
__device__ __forceinline__ bool gok(u32x4 g, unsigned tag) { return (g[1] == tag) & (g[3] == tag); }
__device__ __forceinline__ u32x4 gran2(float a, float b, unsigned tag) { u32x4 g; g[0] = __float_as_uint(a); g[1] = tag; g[2] = __float_as_uint(b); g[3] = tag; return g; }
__device__ __forceinline__ void wg_barrier() { asm volatile("s_waitcnt lgkmcnt(0)\n\ts_barrier" ::: "memory"); }

constexpr float LOG2E = 1.4426950408889634f;

#define POLLDELAY() do { if (PDELAY) __builtin_amdgcn_s_sleep(PDELAY); } while (0)
#define NPACE 16
#define MAPX 0
#define KVORD 0
#define ABL_NOX0 0
#define ABL_NOB0 0
#define OSHARED 1
#define ABL 0
#define X0MODE 0
#define ARGPIN 1
#define VMWAIT(n) asm volatile("s_waitcnt vmcnt(%0)" :: "i"(n) : "memory")

__device__ __forceinline__ float2 ld2(const float* p) { return *(const float2*)p; }

__global__ void __launch_bounds__(640, 3)
fused_decode(const unsigned short* x, const unsigned short* kc, const unsigned short* vc,
             const unsigned short* wq, const unsigned short* wo, const unsigned short* w1,
             const unsigned short* w2, unsigned short* out, unsigned char* ws) {
  typedef unsigned long long u64x8 __attribute__((ext_vector_type(8)));
  unsigned long long te0_, te1_, a8_; u64x8 a_;
  asm volatile("s_memrealtime %0\n\ts_load_dwordx16 %2, %4, 0x0\n\ts_load_dwordx2 %3, %4, 0x40\n\ts_waitcnt lgkmcnt(0)\n\ts_memrealtime %1\n\ts_waitcnt lgkmcnt(0)"
               : "=&s"(te0_), "=&s"(te1_), "=&s"(a_), "=&s"(a8_) : "s"(__builtin_amdgcn_kernarg_segment_ptr()));
  x = (const unsigned short*)a_[0]; kc = (const unsigned short*)a_[1]; vc = (const unsigned short*)a_[2]; wq = (const unsigned short*)a_[3]; wo = (const unsigned short*)a_[4];
  w1 = (const unsigned short*)a_[5]; w2 = (const unsigned short*)a_[6]; out = (unsigned short*)a_[7]; ws = (unsigned char*)a8_;
  extern __shared__ __attribute__((aligned(16))) unsigned char smem[];
  f32x4* red = (f32x4*)smem;
  unsigned* qs = (unsigned*)(smem + 24576);
  unsigned* hs = (unsigned*)(smem + 24576 + 256);
  float* st = (float*)(smem + 24576 + 768);
  unsigned* flg = (unsigned*)(smem + 24576 + 832);
  float* gw = (float*)(smem + 24576 + 896);
  unsigned* wpl = (unsigned*)(smem + 24576 + 848);
  unsigned* os = (unsigned*)(smem + 24576 + 4096);
  unsigned* xl = (unsigned*)(smem + 24576 + 1024);
  const int blk = blockIdx.x, tid = threadIdx.x, lane = tid & 63, w = __builtin_amdgcn_readfirstlane(tid >> 6);
  STAMP_DECL
  STAMP(0);
  const int j = (blk >> 3) & 7, h = (blk & 7) + 8 * (blk >> 6), b = j >> 2, s = j & 3;
  const int r = lane >> 4, c = lane & 15;
  const bool ffn = h < 27;
  const __amdgpu_buffer_rsrc_t rws = RSRC(ws, WS_END);
  const __amdgpu_buffer_rsrc_t rx = RSRC(x, 2560);
  u32x4 mask;
#pragma unroll
  for (int i = 0; i < 4; ++i) mask[i] = (i == ((c & 7) >> 1)) ? ((c & 1) ? 0xffff0000u : 0xffffu) : 0u;
  auto sum_publish = [&](const f32x4* rr, unsigned box, int cbase, unsigned tag) {
    const int cr = cbase + (lane >> 3), e = lane & 7;
    const float* p = (const float*)rr + ((e >> 1) * 16 + (cr & 15)) * 4 + 2 * (e & 1);
    float2 a[8];
#pragma unroll
    for (int i = 0; i < 8; ++i) a[i] = ld2(p + i * 256);
    const float v0 = ((a[0].x + a[1].x) + (a[2].x + a[3].x)) + ((a[4].x + a[5].x) + (a[6].x + a[7].x));
    const float v1 = ((a[0].y + a[1].y) + (a[2].y + a[3].y)) + ((a[4].y + a[5].y) + (a[6].y + a[7].y));
    ST128(gran2(v0, v1, tag), rws, cr < 10 ? (unsigned)((j * 32 + h) * 1280 + cr * 128 + e * 16) : OOB, box, AUX_SC1);
  };

  if (w >= 2) {
    const int sw = w - 2;
    const size_t kvbase = ((size_t)(b * 32 + h) * 8192 + 6144 + s * 512) * 128;
    const __amdgpu_buffer_rsrc_t rk = RSRC(kc + kvbase, 131072), rv = RSRC(vc + kvbase, 131072);
    const __amdgpu_buffer_rsrc_t ro = RSRC(wo + (size_t)h * 128 * 640 + j * 80, 128 * 1280 - j * 160);
    const __amdgpu_buffer_rsrc_t r2 = RSRC(w2 + (size_t)h * 64 * 640 + j * 80, ffn ? 64 * 1280 - j * 160 : 0);
    const int rot = sw * 4 + ((blk * 37) & 127);
    const int voff = lane * 16;
#define KVOFF(t) ((((((t) >> 2) * 32 + ((t) & 3) + rot) & 127) << 10))
    u32x4 K[16], V[16], WO[4], W2[2];
    unsigned pp = 0;
    auto scores = [&]() {
      CBAR();
      const u32x4 q4 = *(const u32x4*)(qs + 4 * c);
      float sb[4];
#pragma unroll
      for (int be = 0; be < 4; ++be) {
        float d[4];
#pragma unroll
        for (int tt = 0; tt < 4; ++tt) {
          const u32x4 kk = K[4 * be + tt];
          d[tt] = dot2(kk[3], q4[3], dot2(kk[2], q4[2], dot2(kk[1], q4[1], dot2(kk[0], q4[0], 0.f))));
        }
        const float e0 = trsel<XOR1, XOR1>(d[0], d[1], c & 1);
        const float e1 = trsel<XOR1, XOR1>(d[2], d[3], c & 1);
        sb[be] = trsel<XOR2, XOR2>(e0, e1, c & 2);
      }
      const float f0 = trsel<SHL4, SHR4>(sb[0], sb[1], c & 4);
      const float f1 = trsel<SHL4, SHR4>(sb[2], sb[3], c & 4);
      const float dotv = trsel<ROR8, ROR8>(f0, f1, c & 8);
      const float sc = rbf(rbf(dotv) * 0.08838834764831845f);
      const float mw = x32_max(x16_max(row_max(sc)));
      const float ew = __builtin_amdgcn_exp2f((sc - mw) * LOG2E);
      pp = pk_bf16(ew, ew);
      const float lw = x32_sum(x16_sum(row_sum(ew)));
      if (lane == 0) LDS_ST64(st + 2 * sw, (unsigned long long)__float_as_uint(mw) | ((unsigned long long)__float_as_uint(lw) << 32));
    };
    if (PRIO) __builtin_amdgcn_s_setprio(PRIO == 3 ? 3 : 2);
    wg_barrier();
#pragma unroll
    for (int i = 0; i < 24; ++i) {
      if (PRIO == 1 && i == 16) __builtin_amdgcn_s_setprio(1);
      if (PRIO == 3 && i == 16) __builtin_amdgcn_s_setprio(2);
      if (PRIO == 2 && i == 8) __builtin_amdgcn_s_setprio(1);
      if (PRIO == 2 && i == 16) __builtin_amdgcn_s_setprio(0);
      if (i >= WK) VMWAIT(WK - 1);
      if (i < 16) K[i] = LD128(rk, voff, KVOFF(i), AUX_NT); else V[i - 16] = LD128(rv, voff, KVOFF(i - 16), AUX_NT);
      SBAR();
      if (i == WK - 1) STAMP(1);
    }
    STAMP(4);
    const bool early = __builtin_amdgcn_readfirstlane(LDS_LD32(flg + 1)) != 0u;
    if (early) { scores(); STAMP(5); }
    SBAR();
    if (PRIO == 1) __builtin_amdgcn_s_setprio(0);
    if (PRIO == 3) __builtin_amdgcn_s_setprio(1);
    {
      const unsigned vo2 = c < 10 ? (unsigned)(r * 2 * 1280 + c * 16) : OOB;
#pragma unroll
      for (int i = 0; i < 2; ++i) { VMWAIT(WK2 - 1); W2[i] = LD128(r2, vo2, (8 * sw + i) * 1280, AUX_NT); SBAR(); }
    }
#pragma unroll
    for (int t = 8; t < 16; ++t) {
      VMWAIT(WK2 - 1);
      V[t] = LD128(rv, voff, KVOFF(t), AUX_NT);
      SBAR();
    }
    if (!early) {
      for (unsigned spins = 0; spins < (SPIN_CAP << 4); ++spins) {
        if (__builtin_amdgcn_readfirstlane(LDS_LD32(flg + 1)) != 0u) break;
        __builtin_amdgcn_s_sleep(1);
      }
      scores();
      STAMP(6);
    }
    SBAR();
    {
      if (PRIO == 3) __builtin_amdgcn_s_setprio(0);
      const unsigned vo4 = c < 10 ? (unsigned)(r * 4 * 1280 + c * 16) : OOB;
#pragma unroll
      for (int i = 0; i < 4; ++i) { VMWAIT(WK2 - 1); WO[i] = LD128(ro, vo4, (16 * sw + i) * 1280, AUX_NT); SBAR(); }
    }
    STAMP(7);
    const unsigned tag = __builtin_amdgcn_readfirstlane(LDS_LD32(flg));
    f32x4 oacc = {0.f, 0.f, 0.f, 0.f};
#define PVSTEP(t) oacc = mfma(mask & dppu<BCAST + (t)>(pp), V[t], oacc);
    PVSTEP(0) PVSTEP(1) PVSTEP(2) PVSTEP(3) PVSTEP(4) PVSTEP(5) PVSTEP(6) PVSTEP(7)
    PVSTEP(8) PVSTEP(9) PVSTEP(10) PVSTEP(11) PVSTEP(12) PVSTEP(13) PVSTEP(14) PVSTEP(15)
#undef PVSTEP
    red[sw * 64 + lane] = oacc;
    STAMP(8);
    wg_barrier();
    {
      const int u = lane & 7;
      const float* p = (const float*)red + (((u >> 1) & 1) * 16 + 2 * sw + (u >> 2)) * 4 + 2 * (u & 1);
      const f32x4 g0 = *(const f32x4*)gw, g1 = *(const f32x4*)(gw + 4);
      float2 a[8];
#pragma unroll
      for (int i = 0; i < 8; ++i) a[i] = ld2(p + i * 256);
      const float v0 = ((a[0].x * g0[0] + a[1].x * g0[1]) + (a[2].x * g0[2] + a[3].x * g0[3])) + ((a[4].x * g1[0] + a[5].x * g1[1]) + (a[6].x * g1[2] + a[7].x * g1[3]));
      const float v1 = ((a[0].y * g0[0] + a[1].y * g0[1]) + (a[2].y * g0[2] + a[3].y * g0[3])) + ((a[4].y * g1[0] + a[5].y * g1[1]) + (a[6].y * g1[2] + a[7].y * g1[3]));
      ST128(gran2(v0, v1, tag), rws, lane < 8 ? (unsigned)((h * 8 + j) * 1024 + sw * 128 + u * 16) : OOB, WS_OBOX, AUX_SC1);
    }
    STAMP(9);
    if (ffn) {
      const u32x2 hv = *(const u32x2*)(hs + (c >> 3) * 64 + 8 * sw + 2 * r);
      f32x4 facc = {0.f, 0.f, 0.f, 0.f};
      facc = mfma(mask & hv[0], W2[0], facc);
      facc = mfma(mask & hv[1], W2[1], facc);
      red[(8 + sw) * 64 + lane] = facc;
      asm volatile("s_waitcnt lgkmcnt(0)" ::: "memory");
      if (lane == 0) __hip_atomic_fetch_add((LAS unsigned*)(flg + 3), 1u, __ATOMIC_RELAXED, __HIP_MEMORY_SCOPE_WORKGROUP);
    }
    VMWAIT(0);
    STAMP(15);
    wg_barrier();
    STAMP(10);
    {
      const u32x4 ov = *(const u32x4*)(os + (c >> 3) * 128 + 16 * sw + 4 * r);
      f32x4 y = {0.f, 0.f, 0.f, 0.f}, y2 = {0.f, 0.f, 0.f, 0.f};
      y = mfma(mask & ov[0], WO[0], y);
      y2 = mfma(mask & ov[1], WO[1], y2);
      y = mfma(mask & ov[2], WO[2], y);
      y2 = mfma(mask & ov[3], WO[3], y2);
      red[(16 + sw) * 64 + lane] = y + y2;
    }
    STAMP(11);
    wg_barrier();
    if (sw == 0) sum_publish(red + 1024, WS_YBOX, 8, tag);
  } else {
    __builtin_amdgcn_s_setprio(3);
    const bool isq = (w == 0);
    const unsigned epoch = __builtin_amdgcn_raw_buffer_load_b32(rws, 0, WS_EPOCH, AUX_SC1);
    u32x2 xs[5];
#pragma unroll
    for (int i = 0; i < 5; ++i) xs[i] = __builtin_amdgcn_raw_buffer_load_b64(rx, (lane >> 5) * 1280 + (lane & 31) * 8, i * 256, 0);
    const int row0 = isq ? 160 * (j >> 1) : 80 * j;
    const int nrow = isq ? 160 : 80;
    unsigned short xr[5];
#pragma unroll
    for (int i = 0; i < 5; ++i) xr[i] = __builtin_amdgcn_raw_buffer_load_b16(rx, (lane >> 5) * 1280 + (lane & 31) * 2, (row0 + 32 * i) * 2, 0);
    u32x4 wa[20];
    {
      const __amdgpu_buffer_rsrc_t rw = isq ? RSRC(wq + h * 128 + (j & 1) * 64, 640 * 4096 * 2 - h * 256 - (j & 1) * 128) : RSRC(w1 + h * 64, ffn ? 640 * 1728 * 2 - h * 128 : 0);
      const int rstride = isq ? 8192 : 3456;
      const unsigned vo = (unsigned)((lane >> 3) * rstride + (lane & 7) * 16);
#pragma unroll
      for (int i = 0; i < 10; ++i) wa[i] = LD128(rw, vo, (row0 + 8 * i) * rstride, AUX_NT);
      if (isq) {
#pragma unroll
        for (int i = 10; i < 20; ++i) wa[i] = LD128(rw, vo, (row0 + 8 * i) * rstride, AUX_NT);
      }
    }
    if (isq && lane < 28) LDS_ST32(st + lane, 0u);
    STAMP(1);
    wg_barrier();
    float xf[20];
#pragma unroll
    for (int i = 0; i < 5; ++i) {
      xf[4 * i + 0] = __uint_as_float(xs[i][0] << 16); xf[4 * i + 1] = __uint_as_float(xs[i][0] & 0xffff0000u);
      xf[4 * i + 2] = __uint_as_float(xs[i][1] << 16); xf[4 * i + 3] = __uint_as_float(xs[i][1] & 0xffff0000u);
    }
    float sum = 0.f;
#pragma unroll
    for (int i = 0; i < 20; ++i) sum += xf[i];
    sum = x16_sum(row_sum(sum));
    const float mean = sum * (1.f / 640.f);
    float vs = 0.f;
#pragma unroll
    for (int i = 0; i < 20; ++i) { const float d = xf[i] - mean; vs = fmaf(d, d, vs); }
    vs = x16_sum(row_sum(vs));
    const float mb = rbf(mean), den = rbf(sqrtf(rbf(rbf(vs * (1.f / 640.f)) + 1e-5f)));
    unsigned* xw_ = xl + w * 320;
#pragma unroll
    for (int i = 0; i < 5; ++i) {
      const int row = (lane & 31) + 32 * i;
      const float xn = rbf(__uint_as_float((unsigned)xr[i] << 16) - mb) / den;
      if (row < nrow) xw_[(lane >> 5) * 160 + ((row >> 1) & 3) * 40 + (row >> 3) * 2 + (row & 1)] = pk_bf16(xn, xn);
    }
    const unsigned tag = __builtin_amdgcn_readfirstlane(epoch) + 0x9E3779B9u;
    if (isq && lane == 0) LDS_ST32(flg, tag);
    asm volatile("s_waitcnt lgkmcnt(0)" ::: "memory");
    STAMP(2);
    f32x4 acc0 = {0.f, 0.f, 0.f, 0.f}, acc1 = {0.f, 0.f, 0.f, 0.f};
    const unsigned* xrd = xw_ + (c >> 3) * 160 + r * 40;
#pragma unroll
    for (int i = 0; i < 10; i += 2) {
      const u32x4 xa = *(const u32x4*)(xrd + 2 * i);
      acc0 = mfma(mask & xa[0], wa[i], acc0); acc1 = mfma(mask & xa[1], wa[i], acc1);
      acc0 = mfma(mask & xa[2], wa[i + 1], acc0); acc1 = mfma(mask & xa[3], wa[i + 1], acc1);
    }
    if (isq) {
#pragma unroll
      for (int i = 10; i < 20; i += 2) {
        const u32x4 xa = *(const u32x4*)(xrd + 2 * i);
        acc0 = mfma(mask & xa[0], wa[i], acc0); acc1 = mfma(mask & xa[1], wa[i], acc1);
        acc0 = mfma(mask & xa[2], wa[i + 1], acc0); acc1 = mfma(mask & xa[3], wa[i + 1], acc1);
      }
    }
    if (isq || ffn) {
      f32x4 v;
#pragma unroll
      for (int i = 0; i < 4; ++i) { const float t_ = c < 8 ? acc0[i] : acc1[i]; v[i] = t_ + dppf<ROR8>(t_); }
      const u32x4 pub = gran2(c < 8 ? v[0] : v[2], c < 8 ? v[1] : v[3], tag);
      const unsigned cu = (unsigned)(h * 8 + j);
      if (isq) {
        if (X0MODE != 1 && X0MODE != 3) ST128(pub, rws, cu * 1024 + lane * 16, WS_Q1, AUX_SC1);
        STAMP(3);
        POLLDELAY();
        const unsigned goff = (unsigned)((h * 8 + (lane >> 5)) * 1024 + (b * 32 + (lane & 31)) * 16);
        u32x4 G[4];
#pragma unroll
        for (int p = 0; p < 4; ++p) G[p] = (X0MODE == 1 || X0MODE == 2) ? pub : LD128(rws, goff, WS_Q1 + p * 2048, AUX_POLL);
        for (unsigned spins = 0; X0MODE == 0 || X0MODE == 4; ++spins) {
          bool allok = true;
          CBAR();
#pragma unroll
          for (int p = 0; p < 4; ++p) if (!__all(gok(G[p], tag))) { allok = false; G[p] = LD128(rws, goff, WS_Q1 + p * 2048, AUX_POLL); }
          if (spins == 0) STAMP(14);
          if (allok || spins > SPIN_CAP) break;
        }
        const float a0 = (__uint_as_float(G[0][0]) + __uint_as_float(G[1][0])) + (__uint_as_float(G[2][0]) + __uint_as_float(G[3][0]));
        const float a1 = (__uint_as_float(G[0][2]) + __uint_as_float(G[1][2])) + (__uint_as_float(G[2][2]) + __uint_as_float(G[3][2]));
        qs[(lane >> 5) * 32 + (c & 7) * 4 + (r & 1) * 2 + (c >> 3)] = pk_bf16(a0, a1);
      } else {
        if (X0MODE == 0 || X0MODE == 2) ST128(pub, rws, (unsigned)(((h * 8 + (c & 7)) * 8 + j) * 128 + (c >> 3) * 64 + r * 16), WS_H1, AUX_SC1);
        STAMP(3);
        POLLDELAY(); POLLDELAY();
        const unsigned goff = cu * 1024 + lane * 16;
        u32x4 G0 = (X0MODE == 0 || X0MODE == 3) ? LD128(rws, goff, WS_H1, AUX_POLL) : pub;
        for (unsigned spins = 0; X0MODE == 0 && !__all(gok(G0, tag)) && spins < SPIN_CAP; ++spins) { CBAR(); G0 = LD128(rws, goff, WS_H1, AUX_POLL); }
        STAMP(14);
        float a0 = __uint_as_float(G0[0]), a1 = __uint_as_float(G0[2]);
        a0 += dppf<ROR8>(a0); a1 += dppf<ROR8>(a1);
        a0 = fmaxf(x32_sum(x16_sum(a0)), 0.f); a1 = fmaxf(x32_sum(x16_sum(a1)), 0.f);
        u32x2 g2; g2[0] = pk_bf16(a0, a1); g2[1] = tag;
        if (X0MODE == 0 || X0MODE == 2) __builtin_amdgcn_raw_buffer_store_b64(g2, rws, lane < 8 ? cu * 128 + lane * 8 : OOB, WS_H2, AUX_SC1);
        const unsigned g2off = (unsigned)((h * 8 + (lane >> 3)) * 128 + (lane & 7) * 8);
        POLLDELAY(); POLLDELAY(); POLLDELAY();
        u32x2 q2 = (X0MODE == 0 || X0MODE == 3) ? __builtin_amdgcn_raw_buffer_load_b64(rws, g2off, WS_H2, AUX_POLL) : g2;
        for (unsigned spins = 0; X0MODE == 0 && !__all(q2[1] == tag) && spins < SPIN_CAP; ++spins) { CBAR(); q2 = __builtin_amdgcn_raw_buffer_load_b64(rws, g2off, WS_H2, AUX_POLL); }
        u32x2 hv; hv[0] = (q2[0] & 0xffffu) | (q2[0] << 16); hv[1] = (q2[0] >> 16) | (q2[0] & 0xffff0000u);
        *(u32x2*)(hs + ((lane >> 1) & 1) * 64 + (lane >> 3) * 8 + (lane & 1) * 4 + ((lane >> 2) & 1) * 2) = hv;
      }
      asm volatile("s_waitcnt lgkmcnt(0)" ::: "memory");
      if (lane == 0) LDS_ST32(flg + 1 + w, 1u);
    }
    STAMP(4);
    if (isq) {
      u32x2 ml;
      for (unsigned spins = 0;; ++spins) {
        { const unsigned long long t_ = LDS_LD64(st + 2 * (lane & 7)); ml[0] = (unsigned)t_; ml[1] = (unsigned)(t_ >> 32); }
        if (__all(ml[1] != 0u) || spins > (SPIN_CAP << 4)) break;
        __builtin_amdgcn_s_sleep(2);
      }
      const float m_ = __uint_as_float(ml[0]), l_ = __uint_as_float(ml[1]);
      float M = fmaxf(m_, dppf<XOR1>(m_)); M = fmaxf(M, dppf<XOR2>(M)); M = fmaxf(M, dppf<XOR7>(M));
      const float f_ = __builtin_amdgcn_exp2f((m_ - M) * LOG2E);
      float L = f_ * l_; L += dppf<XOR1>(L); L += dppf<XOR2>(L); L += dppf<XOR7>(L);
      ST128(gran2(M, L, tag), rws, lane == 0 ? WS_SBOX + (unsigned)((h * 8 + j) * 128) : OOB, 0, AUX_SC1);
      STAMP(5);
      if (lane < 8) gw[lane] = f_;
      STAMP(7);
    }
    wg_barrier();
    STAMP(8);
    if (!isq && ffn) {
      for (unsigned spins = 0; spins < (SPIN_CAP << 4); ++spins) { if (__builtin_amdgcn_readfirstlane(LDS_LD32(flg + 3)) == 8u) break; }
      CBAR();
      sum_publish(red + 512, WS_FBOX, 0, tag);
      sum_publish(red + 512, WS_FBOX, 8, tag);
    }
    if (isq) {
      const unsigned soff = WS_SBOX + (unsigned)((h * 8 + (lane & 7)) * 128);
      const unsigned ooff = (unsigned)(h * 8 * 1024 + lane * 16);
      u32x4 gs = LD128(rws, soff, 0, AUX_POLL);
      if (H1DELAY) __builtin_amdgcn_s_sleep(H1DELAY);
      u32x4 G[8];
#pragma unroll
      for (int p = 0; p < 8; ++p) G[p] = LD128(rws, ooff, WS_OBOX + p * 1024, AUX_POLL);
      for (unsigned spins = 0; !__all(gok(gs, tag)) && spins < SPIN_CAP; ++spins) { CBAR(); __builtin_amdgcn_s_sleep(2); gs = LD128(rws, soff, 0, AUX_POLL); }
      float wp[8];
      {
        const float ms = __uint_as_float(gs[0]), ls = __uint_as_float(gs[2]);
        float Mg = fmaxf(ms, dppf<XOR1>(ms)); Mg = fmaxf(Mg, dppf<XOR2>(Mg));
        const float e_ = __builtin_amdgcn_exp2f((ms - Mg) * LOG2E);
        float Lg = e_ * ls; Lg += dppf<XOR1>(Lg); Lg += dppf<XOR2>(Lg);
        const float wgt = e_ / Lg;
#pragma unroll
        for (int p = 0; p < 8; ++p) wp[p] = __uint_as_float(__builtin_amdgcn_readlane(__float_as_uint(wgt), p));
      }
      STAMP(9);
      for (unsigned spins = 0;; ++spins) {
        bool allok = true;
        CBAR();
#pragma unroll
        for (int p = 0; p < 8; ++p) {
          if (!__all(gok(G[p], tag))) { if (allok && HSLEEP) __builtin_amdgcn_s_sleep(HSLEEP); allok = false; G[p] = LD128(rws, ooff, WS_OBOX + p * 1024, AUX_POLL); }
        }
        if (allok || spins > SPIN_CAP) break;
      }
      STAMP(10);
#pragma unroll
      for (int bb = 0; bb < 2; ++bb) {
        float o0 = 0.f, o1 = 0.f;
#pragma unroll
        for (int sp = 0; sp < 4; ++sp) { o0 = fmaf(wp[4 * bb + sp], __uint_as_float(G[4 * bb + sp][0]), o0); o1 = fmaf(wp[4 * bb + sp], __uint_as_float(G[4 * bb + sp][2]), o1); }
        u32x2 ov; ov[0] = pk_bf16(o0, o0); ov[1] = pk_bf16(o1, o1);
        *(u32x2*)(os + bb * 128 + 2 * lane) = ov;
      }
    }
    wg_barrier();
    const int e = lane & 7, ob = e >> 2, ocol = j * 80 + 8 * h + 4 * ((e >> 1) & 1) + 2 * (e & 1);
    const bool fin = !isq && h < 10;
    const unsigned yo = (unsigned)((j * 32 + (lane >> 3)) * 1280 + h * 128 + e * 16);
    const bool f3v = (lane >> 3) < 3;
    unsigned xw = 0; float fa = 0.f, fb = 0.f;
    if (fin) {
      xw = __builtin_amdgcn_raw_buffer_load_b32(rx, (ob * 640 + ocol) * 2, 0, 0);
    }
    wg_barrier();
    STAMP(11);
    if (isq) {
      sum_publish(red + 1024, WS_YBOX, 0, tag);
      STAMP(12);
    } else if (fin) {
      u32x4 g[4], y[4];
#pragma unroll
      for (int i = 0; i < 4; ++i) g[i] = LD128(rws, (i < 3 || f3v) ? yo : OOB, WS_FBOX + i * 8 * 1280, AUX_POLL);
#pragma unroll
      for (int i = 0; i < 4; ++i) y[i] = LD128(rws, yo, WS_YBOX + i * 8 * 1280, AUX_POLL);
      for (unsigned spins = 0;; ++spins) {
        bool allok = true;
        CBAR();
#pragma unroll
        for (int i = 0; i < 4; ++i) if (!__all(gok(g[i], tag) | (i == 3 && !f3v))) { allok = false; g[i] = LD128(rws, (i < 3 || f3v) ? yo : OOB, WS_FBOX + i * 8 * 1280, AUX_POLL); }
        if (allok || spins > SPIN_CAP) break;
      }
      fa = (__uint_as_float(g[0][0]) + __uint_as_float(g[1][0])) + (__uint_as_float(g[2][0]) + __uint_as_float(g[3][0]));
      fb = (__uint_as_float(g[0][2]) + __uint_as_float(g[1][2])) + (__uint_as_float(g[2][2]) + __uint_as_float(g[3][2]));
      fa += dppf<ROR8>(fa); fb += dppf<ROR8>(fb);
      fa = rbf(x32_sum(x16_sum(fa))); fb = rbf(x32_sum(x16_sum(fb)));
      const float ga = rbf(fa / (1.f + __expf(-fa))), gb = rbf(fb / (1.f + __expf(-fb)));
      STAMP(10);
      for (unsigned spins = 0;; ++spins) {
        bool allok = true;
        CBAR();
#pragma unroll
        for (int i = 0; i < 4; ++i) if (!__all(gok(y[i], tag))) { allok = false; y[i] = LD128(rws, yo, WS_YBOX + i * 8 * 1280, AUX_POLL); }
        if (allok || spins > SPIN_CAP) break;
      }
      STAMP(13);
      float ya = (__uint_as_float(y[0][0]) + __uint_as_float(y[1][0])) + (__uint_as_float(y[2][0]) + __uint_as_float(y[3][0]));
      float yb = (__uint_as_float(y[0][2]) + __uint_as_float(y[1][2])) + (__uint_as_float(y[2][2]) + __uint_as_float(y[3][2]));
      ya += dppf<ROR8>(ya); yb += dppf<ROR8>(yb);
      ya = rbf(x32_sum(x16_sum(ya))); yb = rbf(x32_sum(x16_sum(yb)));
      const float oa = __uint_as_float(xw << 16) + rbf(ya * ga), obv = __uint_as_float(xw & 0xffff0000u) + rbf(yb * gb);
      __builtin_amdgcn_raw_buffer_store_b32(pk_bf16(oa, obv), RSRC(out, 2560), lane < 8 ? (unsigned)((ob * 640 + ocol) * 2) : OOB, 0, 0);
      if (blk == 0 && lane == 0) __builtin_amdgcn_raw_buffer_store_b32(tag, rws, 0, WS_EPOCH, AUX_SC1);
      STAMP(12);
    }
  }
  if (w == 0) { unsigned d_ = (unsigned)(te1_ - te0_) * AMP; if (d_ > 40000u) d_ = 40000u; unsigned t_ = (unsigned)__builtin_amdgcn_s_memrealtime(); while ((unsigned)__builtin_amdgcn_s_memrealtime() - t_ < d_) __builtin_amdgcn_s_sleep(8); }
}

extern "C" void kernel_launch(void* const* d_in, const int* in_sizes, int n_in, void* d_out, int out_size, void* d_ws, size_t ws_size, hipStream_t stream) {
  constexpr int LDS = 96 * 1024;
  (void)hipFuncSetAttribute((const void*)fused_decode, hipFuncAttributeMaxDynamicSharedMemorySize, LDS);
  fused_decode<<<256, 640, LDS, stream>>>((const unsigned short*)d_in[0], (const unsigned short*)d_in[1], (const unsigned short*)d_in[2],
      (const unsigned short*)d_in[3], (const unsigned short*)d_in[4], (const unsigned short*)d_in[6], (const unsigned short*)d_in[7],
      (unsigned short*)d_out, (unsigned char*)d_ws + (WSEND ? ((ws_size - WS_END) & ~(size_t)4095) : 0));
}
```

```cpp
#define PRIO 3
#define WK 24
#define WK2 16
#define PDELAY 24
#define WSEND 0
#define HSLEEP 2
#define H1DELAY 4
#include <hip/hip_runtime.h>
#include <hip/hip_bf16.h>
#include <stdint.h>

#define STAMP(i) do {} while (0)
#define STAMP_DECL
#define STAMP_FLUSH() do {} while (0)

typedef unsigned u32x4 __attribute__((ext_vector_type(4)));
typedef unsigned u32x2 __attribute__((ext_vector_type(2)));
typedef float f32x4 __attribute__((ext_vector_type(4)));
typedef __bf16 bf16x8 __attribute__((ext_vector_type(8)));
typedef __bf16 bf16x2 __attribute__((ext_vector_type(2)));

#define POLLAUX 16
constexpr int AUX_NT = 2, AUX_SC1 = 16, AUX_POLL = POLLAUX;
constexpr unsigned OOB = 0x7ffffff0u;
constexpr unsigned WS_EPOCH = 0;
constexpr unsigned WS_Q1 = 4096;
constexpr unsigned WS_H1 = WS_Q1 + 32 * 8 * 1024;
constexpr unsigned WS_Q2 = WS_H1 + 32 * 8 * 1024;
constexpr unsigned WS_H2 = WS_Q2 + 32 * 8 * 128;
constexpr unsigned WS_SBOX = WS_H2 + 32 * 8 * 128;
constexpr unsigned WS_OBOX = WS_SBOX + 32 * 8 * 128;
constexpr unsigned WS_YBOX = WS_OBOX + 32 * 8 * 1024;
constexpr unsigned WS_FBOX = WS_YBOX + 8 * 32 * 1280;
constexpr unsigned WS_END = WS_FBOX + 8 * 32 * 1280;
constexpr unsigned SPIN_CAP = 1u << 16;

#define RSRC(p, n) __builtin_amdgcn_make_buffer_rsrc((void*)(p), 0, (n), 0x00020000)
#define LD128(rs, vo, so, aux) __builtin_amdgcn_raw_buffer_load_b128((rs), (vo), (so), (aux))
#define ST128(v, rs, vo, so, aux) __builtin_amdgcn_raw_buffer_store_b128((v), (rs), (vo), (so), (aux))
#define SBAR() __builtin_amdgcn_sched_barrier(0)
#define LAS __attribute__((address_space(3)))
#define LDS_LD32(p) __hip_atomic_load((LAS unsigned*)(p), __ATOMIC_RELAXED, __HIP_MEMORY_SCOPE_WORKGROUP)
#define LDS_ST32(p, v) __hip_atomic_store((LAS unsigned*)(p), (v), __ATOMIC_RELAXED, __HIP_MEMORY_SCOPE_WORKGROUP)
#define LDS_LD64(p) __hip_atomic_load((LAS unsigned long long*)(p), __ATOMIC_RELAXED, __HIP_MEMORY_SCOPE_WORKGROUP)
#define LDS_ST64(p, v) __hip_atomic_store((LAS unsigned long long*)(p), (v), __ATOMIC_RELAXED, __HIP_MEMORY_SCOPE_WORKGROUP)
#define CBAR() asm volatile("" ::: "memory")

template <int CTRL> __device__ __forceinline__ unsigned dppu(unsigned x) { return (unsigned)__builtin_amdgcn_mov_dpp((int)x, CTRL, 0xf, 0xf, true); }
template <int CTRL> __device__ __forceinline__ float dppf(float x) { return __uint_as_float(dppu<CTRL>(__float_as_uint(x))); }
constexpr int XOR1 = 0xB1, XOR2 = 0x4E, XOR7 = 0x141, ROR8 = 0x128, ROR4 = 0x124, SHL4 = 0x104, SHR4 = 0x114, BCAST = 0x150;

__device__ __forceinline__ float dot2(unsigned k, unsigned q, float acc) {
  return __builtin_amdgcn_fdot2_f32_bf16(__builtin_bit_cast(bf16x2, k), __builtin_bit_cast(bf16x2, q), acc, false);
}
__device__ __forceinline__ unsigned pk_bf16(float lo, float hi) {
  bf16x2 v; v[0] = (__bf16)lo; v[1] = (__bf16)hi; return __builtin_bit_cast(unsigned, v);
}
__device__ __forceinline__ float rbf(float f) { return __uint_as_float(pk_bf16(f, f) & 0xffff0000u); }
__device__ __forceinline__ f32x4 mfma(u32x4 a, u32x4 b, f32x4 c) {
  return __builtin_amdgcn_mfma_f32_16x16x32_bf16(__builtin_bit_cast(bf16x8, a), __builtin_bit_cast(bf16x8, b), c, 0, 0, 0);
}
__device__ __forceinline__ float x16_sum(float x) { auto s = __builtin_amdgcn_permlane16_swap(__float_as_uint(x), __float_as_uint(x), false, false); return __uint_as_float(s[0]) + __uint_as_float(s[1]); }
__device__ __forceinline__ float x32_sum(float x) { auto s = __builtin_amdgcn_permlane32_swap(__float_as_uint(x), __float_as_uint(x), false, false); return __uint_as_float(s[0]) + __uint_as_float(s[1]); }
__device__ __forceinline__ float x16_max(float x) { auto s = __builtin_amdgcn_permlane16_swap(__float_as_uint(x), __float_as_uint(x), false, false); return fmaxf(__uint_as_float(s[0]), __uint_as_float(s[1])); }
__device__ __forceinline__ float x32_max(float x) { auto s = __builtin_amdgcn_permlane32_swap(__float_as_uint(x), __float_as_uint(x), false, false); return fmaxf(__uint_as_float(s[0]), __uint_as_float(s[1])); }
__device__ __forceinline__ float row_sum(float x) { x += dppf<XOR1>(x); x += dppf<XOR2>(x); x += dppf<ROR4>(x); x += dppf<ROR8>(x); return x; }
__device__ __forceinline__ float row_max(float x) { x = fmaxf(x, dppf<XOR1>(x)); x = fmaxf(x, dppf<XOR2>(x)); x = fmaxf(x, dppf<ROR4>(x)); x = fmaxf(x, dppf<ROR8>(x)); return x; }
template <int C0, int C1> __device__ __forceinline__ float trsel(float lo, float hi, bool bit) {
  const float t0 = lo + dppf<C0>(lo), t1 = hi + dppf<C1>(hi);
  return bit ? t1 : t0;
}
__device__ __forceinline__ bool gok(u32x4 g, unsigned tag) { return (g[1] == tag) & (g[3] == tag); }
__device__ __forceinline__ u32x4 gran2(float a, float b, unsigned tag) { u32x4 g; g[0] = __float_as_uint(a); g[1] = tag; g[2] = __float_as_uint(b); g[3] = tag; return g; }
__device__ __forceinline__ void wg_barrier() { asm volatile("s_waitcnt lgkmcnt(0)\n\ts_barrier" ::: "memory"); }

constexpr float LOG2E = 1.4426950408889634f;

#define POLLDELAY() do { if (PDELAY) __builtin_amdgcn_s_sleep(PDELAY); } while (0)
#define NPACE 16
#define MAPX 0
#define KVORD 0
#define ABL_NOX0 0
#define ABL_NOB0 0
#define OSHARED 1
#define ABL 0
#define X0MODE 0
#define ARGPIN 1
#define VMWAIT(n) asm volatile("s_waitcnt vmcnt(%0)" :: "i"(n) : "memory")

__device__ __forceinline__ float2 ld2(const float* p) { return *(const float2*)p; }

__global__ void __launch_bounds__(640, 3)
fused_decode(const unsigned short* x, const unsigned short* kc, const unsigned short* vc,
             const unsigned short* wq, const unsigned short* wo, const unsigned short* w1,
             const unsigned short* w2, unsigned short* out, unsigned char* ws) {
  asm volatile("" : "+s"(x), "+s"(kc), "+s"(vc), "+s"(wq), "+s"(wo), "+s"(w1), "+s"(w2), "+s"(out), "+s"(ws));
  extern __shared__ __attribute__((aligned(16))) unsigned char smem[];
  f32x4* red = (f32x4*)smem;
  unsigned* qs = (unsigned*)(smem + 24576);
  unsigned* hs = (unsigned*)(smem + 24576 + 256);
  float* st = (float*)(smem + 24576 + 768);
  unsigned* flg = (unsigned*)(smem + 24576 + 832);
  float* gw = (float*)(smem + 24576 + 896);
  unsigned* wpl = (unsigned*)(smem + 24576 + 848);
  unsigned* os = (unsigned*)(smem + 24576 + 4096);
  unsigned* xl = (unsigned*)(smem + 24576 + 1024);
  const int blk = blockIdx.x, tid = threadIdx.x, lane = tid & 63, w = __builtin_amdgcn_readfirstlane(tid >> 6);
  STAMP_DECL
  STAMP(0);
  const int j = (blk >> 3) & 7, h = (blk & 7) + 8 * (blk >> 6), b = j >> 2, s = j & 3;
  const int r = lane >> 4, c = lane & 15;
  const bool ffn = h < 27;
  const __amdgpu_buffer_rsrc_t rws = RSRC(ws, WS_END);
  const __amdgpu_buffer_rsrc_t rx = RSRC(x, 2560);
  u32x4 mask;
#pragma unroll
  for (int i = 0; i < 4; ++i) mask[i] = (i == ((c & 7) >> 1)) ? ((c & 1) ? 0xffff0000u : 0xffffu) : 0u;
  auto sum_publish = [&](const f32x4* rr, unsigned box, int cbase, unsigned tag) {
    const int cr = cbase + (lane >> 3), e = lane & 7;
    const float* p = (const float*)rr + ((e >> 1) * 16 + (cr & 15)) * 4 + 2 * (e & 1);
    float2 a[8];
#pragma unroll
    for (int i = 0; i < 8; ++i) a[i] = ld2(p + i * 256);
    const float v0 = ((a[0].x + a[1].x) + (a[2].x + a[3].x)) + ((a[4].x + a[5].x) + (a[6].x + a[7].x));
    const float v1 = ((a[0].y + a[1].y) + (a[2].y + a[3].y)) + ((a[4].y + a[5].y) + (a[6].y + a[7].y));
    ST128(gran2(v0, v1, tag), rws, cr < 10 ? (unsigned)((j * 32 + h) * 1280 + cr * 128 + e * 16) : OOB, box, AUX_SC1);
  };

  if (w >= 2) {
    const int sw = w - 2;
    const size_t kvbase = ((size_t)(b * 32 + h) * 8192 + 6144 + s * 512) * 128;
    const __amdgpu_buffer_rsrc_t rk = RSRC(kc + kvbase, 131072), rv = RSRC(vc + kvbase, 131072);
    const __amdgpu_buffer_rsrc_t ro = RSRC(wo + (size_t)h * 128 * 640 + j * 80, 128 * 1280 - j * 160);
    const __amdgpu_buffer_rsrc_t r2 = RSRC(w2 + (size_t)h * 64 * 640 + j * 80, ffn ? 64 * 1280 - j * 160 : 0);
    const int rot = sw * 4 + ((blk * 37) & 127);
    const int voff = lane * 16;
#define KVOFF(t) ((((((t) >> 2) * 32 + ((t) & 3) + rot) & 127) << 10))
    u32x4 K[16], V[16], WO[4], W2[2];
    unsigned pp = 0;
    auto scores = [&]() {
      CBAR();
      const u32x4 q4 = *(const u32x4*)(qs + 4 * c);
      float sb[4];
#pragma unroll
      for (int be = 0; be < 4; ++be) {
        float d[4];
#pragma unroll
        for (int tt = 0; tt < 4; ++tt) {
          const u32x4 kk = K[4 * be + tt];
          d[tt] = dot2(kk[3], q4[3], dot2(kk[2], q4[2], dot2(kk[1], q4[1], dot2(kk[0], q4[0], 0.f))));
        }
        const float e0 = trsel<XOR1, XOR1>(d[0], d[1], c & 1);
        const float e1 = trsel<XOR1, XOR1>(d[2], d[3], c & 1);
        sb[be] = trsel<XOR2, XOR2>(e0, e1, c & 2);
      }
      const float f0 = trsel<SHL4, SHR4>(sb[0], sb[1], c & 4);
      const float f1 = trsel<SHL4, SHR4>(sb[2], sb[3], c & 4);
      const float dotv = trsel<ROR8, ROR8>(f0, f1, c & 8);
      const float sc = rbf(rbf(dotv) * 0.08838834764831845f);
      const float mw = x32_max(x16_max(row_max(sc)));
      const float ew = __builtin_amdgcn_exp2f((sc - mw) * LOG2E);
      pp = pk_bf16(ew, ew);
      const float lw = x32_sum(x16_sum(row_sum(ew)));
      if (lane == 0) LDS_ST64(st + 2 * sw, (unsigned long long)__float_as_uint(mw) | ((unsigned long long)__float_as_uint(lw) << 32));
    };
    if (PRIO) __builtin_amdgcn_s_setprio(PRIO == 3 ? 3 : 2);
    wg_barrier();
#pragma unroll
    for (int i = 0; i < 24; ++i) {
      if (PRIO == 1 && i == 16) __builtin_amdgcn_s_setprio(1);
      if (PRIO == 3 && i == 16) __builtin_amdgcn_s_setprio(2);
      if (PRIO == 2 && i == 8) __builtin_amdgcn_s_setprio(1);
      if (PRIO == 2 && i == 16) __builtin_amdgcn_s_setprio(0);
      if (i >= WK) VMWAIT(WK - 1);
      if (i < 16) K[i] = LD128(rk, voff, KVOFF(i), AUX_NT); else V[i - 16] = LD128(rv, voff, KVOFF(i - 16), AUX_NT);
      SBAR();
      if (i == WK - 1) STAMP(1);
    }
    STAMP(4);
    const bool early = __builtin_amdgcn_readfirstlane(LDS_LD32(flg + 1)) != 0u;
    if (early) { scores(); STAMP(5); }
    SBAR();
    if (PRIO == 1) __builtin_amdgcn_s_setprio(0);
    if (PRIO == 3) __builtin_amdgcn_s_setprio(1);
    {
      const unsigned vo2 = c < 10 ? (unsigned)(r * 2 * 1280 + c * 16) : OOB;
#pragma unroll
      for (int i = 0; i < 2; ++i) { VMWAIT(WK2 - 1); W2[i] = LD128(r2, vo2, (8 * sw + i) * 1280, AUX_NT); SBAR(); }
    }
#pragma unroll
    for (int t = 8; t < 16; ++t) {
      VMWAIT(WK2 - 1);
      V[t] = LD128(rv, voff, KVOFF(t), AUX_NT);
      SBAR();
    }
    if (!early) {
      for (unsigned spins = 0; spins < (SPIN_CAP << 4); ++spins) {
        if (__builtin_amdgcn_readfirstlane(LDS_LD32(flg + 1)) != 0u) break;
        __builtin_amdgcn_s_sleep(1);
      }
      scores();
      STAMP(6);
    }
    SBAR();
    {
      if (PRIO == 3) __builtin_amdgcn_s_setprio(0);
      const unsigned vo4 = c < 10 ? (unsigned)(r * 4 * 1280 + c * 16) : OOB;
#pragma unroll
      for (int i = 0; i < 4; ++i) { VMWAIT(WK2 - 1); WO[i] = LD128(ro, vo4, (16 * sw + i) * 1280, AUX_NT); SBAR(); }
    }
    STAMP(7);
    const unsigned tag = __builtin_amdgcn_readfirstlane(LDS_LD32(flg));
    f32x4 oacc = {0.f, 0.f, 0.f, 0.f};
#define PVSTEP(t) oacc = mfma(mask & dppu<BCAST + (t)>(pp), V[t], oacc);
    PVSTEP(0) PVSTEP(1) PVSTEP(2) PVSTEP(3) PVSTEP(4) PVSTEP(5) PVSTEP(6) PVSTEP(7)
    PVSTEP(8) PVSTEP(9) PVSTEP(10) PVSTEP(11) PVSTEP(12) PVSTEP(13) PVSTEP(14) PVSTEP(15)
#undef PVSTEP
    red[sw * 64 + lane] = oacc;
    STAMP(8);
    wg_barrier();
    {
      const int u = lane & 7;
      const float* p = (const float*)red + (((u >> 1) & 1) * 16 + 2 * sw + (u >> 2)) * 4 + 2 * (u & 1);
      const f32x4 g0 = *(const f32x4*)gw, g1 = *(const f32x4*)(gw + 4);
      float2 a[8];
#pragma unroll
      for (int i = 0; i < 8; ++i) a[i] = ld2(p + i * 256);
      const float v0 = ((a[0].x * g0[0] + a[1].x * g0[1]) + (a[2].x * g0[2] + a[3].x * g0[3])) + ((a[4].x * g1[0] + a[5].x * g1[1]) + (a[6].x * g1[2] + a[7].x * g1[3]));
      const float v1 = ((a[0].y * g0[0] + a[1].y * g0[1]) + (a[2].y * g0[2] + a[3].y * g0[3])) + ((a[4].y * g1[0] + a[5].y * g1[1]) + (a[6].y * g1[2] + a[7].y * g1[3]));
      ST128(gran2(v0, v1, tag), rws, lane < 8 ? (unsigned)((h * 8 + j) * 1024 + sw * 128 + u * 16) : OOB, WS_OBOX, AUX_SC1);
    }
    STAMP(9);
    if (ffn) {
      const u32x2 hv = *(const u32x2*)(hs + (c >> 3) * 64 + 8 * sw + 2 * r);
      f32x4 facc = {0.f, 0.f, 0.f, 0.f};
      facc = mfma(mask & hv[0], W2[0], facc);
      facc = mfma(mask & hv[1], W2[1], facc);
      red[(8 + sw) * 64 + lane] = facc;
      asm volatile("s_waitcnt lgkmcnt(0)" ::: "memory");
      if (lane == 0) __hip_atomic_fetch_add((LAS unsigned*)(flg + 3), 1u, __ATOMIC_RELAXED, __HIP_MEMORY_SCOPE_WORKGROUP);
    }
    VMWAIT(0);
    STAMP(15);
    wg_barrier();
    STAMP(10);
    {
      const u32x4 ov = *(const u32x4*)(os + (c >> 3) * 128 + 16 * sw + 4 * r);
      f32x4 y = {0.f, 0.f, 0.f, 0.f}, y2 = {0.f, 0.f, 0.f, 0.f};
      y = mfma(mask & ov[0], WO[0], y);
      y2 = mfma(mask & ov[1], WO[1], y2);
      y = mfma(mask & ov[2], WO[2], y);
      y2 = mfma(mask & ov[3], WO[3], y2);
      red[(16 + sw) * 64 + lane] = y + y2;
    }
    STAMP(11);
    wg_barrier();
    if (sw == 0) sum_publish(red + 1024, WS_YBOX, 8, tag);
  } else {
    __builtin_amdgcn_s_setprio(3);
    const bool isq = (w == 0);
    const unsigned epoch = __builtin_amdgcn_raw_buffer_load_b32(rws, 0, WS_EPOCH, AUX_SC1);
    u32x2 xs[5];
#pragma unroll
    for (int i = 0; i < 5; ++i) xs[i] = __builtin_amdgcn_raw_buffer_load_b64(rx, (lane >> 5) * 1280 + (lane & 31) * 8, i * 256, 0);
    const int row0 = isq ? 160 * (j >> 1) : 80 * j;
    const int nrow = isq ? 160 : 80;
    unsigned short xr[5];
#pragma unroll
    for (int i = 0; i < 5; ++i) xr[i] = __builtin_amdgcn_raw_buffer_load_b16(rx, (lane >> 5) * 1280 + (lane & 31) * 2, (row0 + 32 * i) * 2, 0);
    u32x4 wa[20];
    {
      const __amdgpu_buffer_rsrc_t rw = isq ? RSRC(wq + h * 128 + (j & 1) * 64, 640 * 4096 * 2 - h * 256 - (j & 1) * 128) : RSRC(w1 + h * 64, ffn ? 640 * 1728 * 2 - h * 128 : 0);
      const int rstride = isq ? 8192 : 3456;
      const unsigned vo = (unsigned)((lane >> 3) * rstride + (lane & 7) * 16);
#pragma unroll
      for (int i = 0; i < 10; ++i) wa[i] = LD128(rw, vo, (row0 + 8 * i) * rstride, AUX_NT);
      if (isq) {
#pragma unroll
        for (int i = 10; i < 20; ++i) wa[i] = LD128(rw, vo, (row0 + 8 * i) * rstride, AUX_NT);
      }
    }
    if (isq && lane < 28) LDS_ST32(st + lane, 0u);
    STAMP(1);
    wg_barrier();
    float xf[20];
#pragma unroll
    for (int i = 0; i < 5; ++i) {
      xf[4 * i + 0] = __uint_as_float(xs[i][0] << 16); xf[4 * i + 1] = __uint_as_float(xs[i][0] & 0xffff0000u);
      xf[4 * i + 2] = __uint_as_float(xs[i][1] << 16); xf[4 * i + 3] = __uint_as_float(xs[i][1] & 0xffff0000u);
    }
    float sum = 0.f;
#pragma unroll
    for (int i = 0; i < 20; ++i) sum += xf[i];
    sum = x16_sum(row_sum(sum));
    const float mean = sum * (1.f / 640.f);
    float vs = 0.f;
#pragma unroll
    for (int i = 0; i < 20; ++i) { const float d = xf[i] - mean; vs = fmaf(d, d, vs); }
    vs = x16_sum(row_sum(vs));
    const float mb = rbf(mean), den = rbf(sqrtf(rbf(rbf(vs * (1.f / 640.f)) + 1e-5f)));
    unsigned* xw_ = xl + w * 320;
#pragma unroll
    for (int i = 0; i < 5; ++i) {
      const int row = (lane & 31) + 32 * i;
      const float xn = rbf(__uint_as_float((unsigned)xr[i] << 16) - mb) / den;
      if (row < nrow) xw_[(lane >> 5) * 160 + ((row >> 1) & 3) * 40 + (row >> 3) * 2 + (row & 1)] = pk_bf16(xn, xn);
    }
    const unsigned tag = __builtin_amdgcn_readfirstlane(epoch) + 0x9E3779B9u;
    if (isq && lane == 0) LDS_ST32(flg, tag);
    asm volatile("s_waitcnt lgkmcnt(0)" ::: "memory");
    STAMP(2);
    f32x4 acc0 = {0.f, 0.f, 0.f, 0.f}, acc1 = {0.f, 0.f, 0.f, 0.f};
    const unsigned* xrd = xw_ + (c >> 3) * 160 + r * 40;
#pragma unroll
    for (int i = 0; i < 10; i += 2) {
      const u32x4 xa = *(const u32x4*)(xrd + 2 * i);
      acc0 = mfma(mask & xa[0], wa[i], acc0); acc1 = mfma(mask & xa[1], wa[i], acc1);
      acc0 = mfma(mask & xa[2], wa[i + 1], acc0); acc1 = mfma(mask & xa[3], wa[i + 1], acc1);
    }
    if (isq) {
#pragma unroll
      for (int i = 10; i < 20; i += 2) {
        const u32x4 xa = *(const u32x4*)(xrd + 2 * i);
        acc0 = mfma(mask & xa[0], wa[i], acc0); acc1 = mfma(mask & xa[1], wa[i], acc1);
        acc0 = mfma(mask & xa[2], wa[i + 1], acc0); acc1 = mfma(mask & xa[3], wa[i + 1], acc1);
      }
    }
    if (isq || ffn) {
      f32x4 v;
#pragma unroll
      for (int i = 0; i < 4; ++i) { const float t_ = c < 8 ? acc0[i] : acc1[i]; v[i] = t_ + dppf<ROR8>(t_); }
      const u32x4 pub = gran2(c < 8 ? v[0] : v[2], c < 8 ? v[1] : v[3], tag);
      const unsigned cu = (unsigned)(h * 8 + j);
      if (isq) {
        if (X0MODE != 1 && X0MODE != 3) ST128(pub, rws, cu * 1024 + lane * 16, WS_Q1, AUX_SC1);
        STAMP(3);
        POLLDELAY();
        const unsigned goff = (unsigned)((h * 8 + (lane >> 5)) * 1024 + (b * 32 + (lane & 31)) * 16);
        u32x4 G[4];
#pragma unroll
        for (int p = 0; p < 4; ++p) G[p] = (X0MODE == 1 || X0MODE == 2) ? pub : LD128(rws, goff, WS_Q1 + p * 2048, AUX_POLL);
        for (unsigned spins = 0; X0MODE == 0 || X0MODE == 4; ++spins) {
          bool allok = true;
          CBAR();
#pragma unroll
          for (int p = 0; p < 4; ++p) if (!__all(gok(G[p], tag))) { allok = false; G[p] = LD128(rws, goff, WS_Q1 + p * 2048, AUX_POLL); }
          if (spins == 0) STAMP(14);
          if (allok || spins > SPIN_CAP) break;
        }
        const float a0 = (__uint_as_float(G[0][0]) + __uint_as_float(G[1][0])) + (__uint_as_float(G[2][0]) + __uint_as_float(G[3][0]));
        const float a1 = (__uint_as_float(G[0][2]) + __uint_as_float(G[1][2])) + (__uint_as_float(G[2][2]) + __uint_as_float(G[3][2]));
        qs[(lane >> 5) * 32 + (c & 7) * 4 + (r & 1) * 2 + (c >> 3)] = pk_bf16(a0, a1);
      } else {
        if (X0MODE == 0 || X0MODE == 2) ST128(pub, rws, (unsigned)(((h * 8 + (c & 7)) * 8 + j) * 128 + (c >> 3) * 64 + r * 16), WS_H1, AUX_SC1);
        STAMP(3);
        POLLDELAY(); POLLDELAY();
        const unsigned goff = cu * 1024 + lane * 16;
        u32x4 G0 = (X0MODE == 0 || X0MODE == 3) ? LD128(rws, goff, WS_H1, AUX_POLL) : pub;
        for (unsigned spins = 0; X0MODE == 0 && !__all(gok(G0, tag)) && spins < SPIN_CAP; ++spins) { CBAR(); G0 = LD128(rws, goff, WS_H1, AUX_POLL); }
        STAMP(14);
        float a0 = __uint_as_float(G0[0]), a1 = __uint_as_float(G0[2]);
        a0 += dppf<ROR8>(a0); a1 += dppf<ROR8>(a1);
        a0 = fmaxf(x32_sum(x16_sum(a0)), 0.f); a1 = fmaxf(x32_sum(x16_sum(a1)), 0.f);
        u32x2 g2; g2[0] = pk_bf16(a0, a1); g2[1] = tag;
        if (X0MODE == 0 || X0MODE == 2) __builtin_amdgcn_raw_buffer_store_b64(g2, rws, lane < 8 ? cu * 128 + lane * 8 : OOB, WS_H2, AUX_SC1);
        const unsigned g2off = (unsigned)((h * 8 + (lane >> 3)) * 128 + (lane & 7) * 8);
        POLLDELAY(); POLLDELAY(); POLLDELAY();
        u32x2 q2 = (X0MODE == 0 || X0MODE == 3) ? __builtin_amdgcn_raw_buffer_load_b64(rws, g2off, WS_H2, AUX_POLL) : g2;
        for (unsigned spins = 0; X0MODE == 0 && !__all(q2[1] == tag) && spins < SPIN_CAP; ++spins) { CBAR(); q2 = __builtin_amdgcn_raw_buffer_load_b64(rws, g2off, WS_H2, AUX_POLL); }
        u32x2 hv; hv[0] = (q2[0] & 0xffffu) | (q2[0] << 16); hv[1] = (q2[0] >> 16) | (q2[0] & 0xffff0000u);
        *(u32x2*)(hs + ((lane >> 1) & 1) * 64 + (lane >> 3) * 8 + (lane & 1) * 4 + ((lane >> 2) & 1) * 2) = hv;
      }
      asm volatile("s_waitcnt lgkmcnt(0)" ::: "memory");
      if (lane == 0) LDS_ST32(flg + 1 + w, 1u);
    }
    STAMP(4);
    if (isq) {
      u32x2 ml;
      for (unsigned spins = 0;; ++spins) {
        { const unsigned long long t_ = LDS_LD64(st + 2 * (lane & 7)); ml[0] = (unsigned)t_; ml[1] = (unsigned)(t_ >> 32); }
        if (__all(ml[1] != 0u) || spins > (SPIN_CAP << 4)) break;
        __builtin_amdgcn_s_sleep(2);
      }
      const float m_ = __uint_as_float(ml[0]), l_ = __uint_as_float(ml[1]);
      float M = fmaxf(m_, dppf<XOR1>(m_)); M = fmaxf(M, dppf<XOR2>(M)); M = fmaxf(M, dppf<XOR7>(M));
      const float f_ = __builtin_amdgcn_exp2f((m_ - M) * LOG2E);
      float L = f_ * l_; L += dppf<XOR1>(L); L += dppf<XOR2>(L); L += dppf<XOR7>(L);
      ST128(gran2(M, L, tag), rws, lane == 0 ? WS_SBOX + (unsigned)((h * 8 + j) * 128) : OOB, 0, AUX_SC1);
      STAMP(5);
      if (lane < 8) gw[lane] = f_;
      STAMP(7);
    }
    wg_barrier();
    STAMP(8);
    if (!isq && ffn) {
      for (unsigned spins = 0; spins < (SPIN_CAP << 4); ++spins) { if (__builtin_amdgcn_readfirstlane(LDS_LD32(flg + 3)) == 8u) break; }
      CBAR();
      sum_publish(red + 512, WS_FBOX, 0, tag);
      sum_publish(red + 512, WS_FBOX, 8, tag);
    }
    if (isq) {
      const unsigned soff = WS_SBOX + (unsigned)((h * 8 + (lane & 7)) * 128);
      const unsigned ooff = (unsigned)(h * 8 * 1024 + lane * 16);
      u32x4 gs = LD128(rws, soff, 0, AUX_POLL);
      if (H1DELAY) __builtin_amdgcn_s_sleep(H1DELAY);
      u32x4 G[8];
#pragma unroll
      for (int p = 0; p < 8; ++p) G[p] = LD128(rws, ooff, WS_OBOX + p * 1024, AUX_POLL);
      for (unsigned spins = 0; !__all(gok(gs, tag)) && spins < SPIN_CAP; ++spins) { CBAR(); __builtin_amdgcn_s_sleep(2); gs = LD128(rws, soff, 0, AUX_POLL); }
      float wp[8];
      {
        const float ms = __uint_as_float(gs[0]), ls = __uint_as_float(gs[2]);
        float Mg = fmaxf(ms, dppf<XOR1>(ms)); Mg = fmaxf(Mg, dppf<XOR2>(Mg));
        const float e_ = __builtin_amdgcn_exp2f((ms - Mg) * LOG2E);
        float Lg = e_ * ls; Lg += dppf<XOR1>(Lg); Lg += dppf<XOR2>(Lg);
        const float wgt = e_ / Lg;
#pragma unroll
        for (int p = 0; p < 8; ++p) wp[p] = __uint_as_float(__builtin_amdgcn_readlane(__float_as_uint(wgt), p));
      }
      STAMP(9);
      for (unsigned spins = 0;; ++spins) {
        bool allok = true;
        CBAR();
#pragma unroll
        for (int p = 0; p < 8; ++p) {
          if (!__all(gok(G[p], tag))) { if (allok && HSLEEP) __builtin_amdgcn_s_sleep(HSLEEP); allok = false; G[p] = LD128(rws, ooff, WS_OBOX + p * 1024, AUX_POLL); }
        }
        if (allok || spins > SPIN_CAP) break;
      }
      STAMP(10);
#pragma unroll
      for (int bb = 0; bb < 2; ++bb) {
        float o0 = 0.f, o1 = 0.f;
#pragma unroll
        for (int sp = 0; sp < 4; ++sp) { o0 = fmaf(wp[4 * bb + sp], __uint_as_float(G[4 * bb + sp][0]), o0); o1 = fmaf(wp[4 * bb + sp], __uint_as_float(G[4 * bb + sp][2]), o1); }
        u32x2 ov; ov[0] = pk_bf16(o0, o0); ov[1] = pk_bf16(o1, o1);
        *(u32x2*)(os + bb * 128 + 2 * lane) = ov;
      }
    }
    wg_barrier();
    const int e = lane & 7, ob = e >> 2, ocol = j * 80 + 8 * h + 4 * ((e >> 1) & 1) + 2 * (e & 1);
    const bool fin = !isq && h < 10;
    const unsigned yo = (unsigned)((j * 32 + (lane >> 3)) * 1280 + h * 128 + e * 16);
    const bool f3v = (lane >> 3) < 3;
    unsigned xw = 0; float fa = 0.f, fb = 0.f;
    if (fin) {
      xw = __builtin_amdgcn_raw_buffer_load_b32(rx, (ob * 640 + ocol) * 2, 0, 0);
    }
    wg_barrier();
    STAMP(11);
    if (isq) {
      sum_publish(red + 1024, WS_YBOX, 0, tag);
      STAMP(12);
    } else if (fin) {
      u32x4 g[4], y[4];
#pragma unroll
      for (int i = 0; i < 4; ++i) g[i] = LD128(rws, (i < 3 || f3v) ? yo : OOB, WS_FBOX + i * 8 * 1280, AUX_POLL);
#pragma unroll
      for (int i = 0; i < 4; ++i) y[i] = LD128(rws, yo, WS_YBOX + i * 8 * 1280, AUX_POLL);
      for (unsigned spins = 0;; ++spins) {
        bool allok = true;
        CBAR();
#pragma unroll
        for (int i = 0; i < 4; ++i) if (!__all(gok(g[i], tag) | (i == 3 && !f3v))) { allok = false; g[i] = LD128(rws, (i < 3 || f3v) ? yo : OOB, WS_FBOX + i * 8 * 1280, AUX_POLL); }
        if (allok || spins > SPIN_CAP) break;
      }
      fa = (__uint_as_float(g[0][0]) + __uint_as_float(g[1][0])) + (__uint_as_float(g[2][0]) + __uint_as_float(g[3][0]));
      fb = (__uint_as_float(g[0][2]) + __uint_as_float(g[1][2])) + (__uint_as_float(g[2][2]) + __uint_as_float(g[3][2]));
      fa += dppf<ROR8>(fa); fb += dppf<ROR8>(fb);
      fa = rbf(x32_sum(x16_sum(fa))); fb = rbf(x32_sum(x16_sum(fb)));
      const float ga = rbf(fa / (1.f + __expf(-fa))), gb = rbf(fb / (1.f + __expf(-fb)));
      STAMP(10);
      for (unsigned spins = 0;; ++spins) {
        bool allok = true;
        CBAR();
#pragma unroll
        for (int i = 0; i < 4; ++i) if (!__all(gok(y[i], tag))) { allok = false; y[i] = LD128(rws, yo, WS_YBOX + i * 8 * 1280, AUX_POLL); }
        if (allok || spins > SPIN_CAP) break;
      }
      STAMP(13);
      float ya = (__uint_as_float(y[0][0]) + __uint_as_float(y[1][0])) + (__uint_as_float(y[2][0]) + __uint_as_float(y[3][0]));
      float yb = (__uint_as_float(y[0][2]) + __uint_as_float(y[1][2])) + (__uint_as_float(y[2][2]) + __uint_as_float(y[3][2]));
      ya += dppf<ROR8>(ya); yb += dppf<ROR8>(yb);
      ya = rbf(x32_sum(x16_sum(ya))); yb = rbf(x32_sum(x16_sum(yb)));
      const float oa = __uint_as_float(xw << 16) + rbf(ya * ga), obv = __uint_as_float(xw & 0xffff0000u) + rbf(yb * gb);
      __builtin_amdgcn_raw_buffer_store_b32(pk_bf16(oa, obv), RSRC(out, 2560), lane < 8 ? (unsigned)((ob * 640 + ocol) * 2) : OOB, 0, 0);
      if (blk == 0 && lane == 0) __builtin_amdgcn_raw_buffer_store_b32(tag, rws, 0, WS_EPOCH, AUX_SC1);
      STAMP(12);
    }
  }
  STAMP_FLUSH();
}

__global__ void tiny(unsigned* p) { if (threadIdx.x == 0) p[0] = (unsigned)__builtin_amdgcn_s_memrealtime(); }
extern "C" void kernel_launch(void* const* d_in, const int* in_sizes, int n_in, void* d_out, int out_size, void* d_ws, size_t ws_size, hipStream_t stream) {
  constexpr int LDS = 96 * 1024;
  (void)hipFuncSetAttribute((const void*)fused_decode, hipFuncAttributeMaxDynamicSharedMemorySize, LDS);
  tiny<<<1, 64, 0, stream>>>((unsigned*)((char*)d_ws + (64u << 20)));
  fused_decode<<<256, 640, LDS, stream>>>((const unsigned short*)d_in[0], (const unsigned short*)d_in[1], (const unsigned short*)d_in[2],
      (const unsigned short*)d_in[3], (const unsigned short*)d_in[4], (const unsigned short*)d_in[6], (const unsigned short*)d_in[7],
      (unsigned short*)d_out, (unsigned char*)d_ws + (WSEND ? ((ws_size - WS_END) & ~(size_t)4095) : 0));
}
```

```cpp
#define PRIO 3
#define WK 24
#define WK2 16
#define PDELAY 24
#define WSEND 0
#define HSLEEP 0
#define H1DELAY 0
#include <hip/hip_runtime.h>
#include <hip/hip_bf16.h>
#include <stdint.h>

#define STAMP(i) do {} while (0)
#define STAMP_DECL
#define STAMP_FLUSH() do {} while (0)

typedef unsigned u32x4 __attribute__((ext_vector_type(4)));
typedef unsigned u32x2 __attribute__((ext_vector_type(2)));
typedef float f32x4 __attribute__((ext_vector_type(4)));
typedef float f32x2 __attribute__((ext_vector_type(2)));
typedef __bf16 bf16x8 __attribute__((ext_vector_type(8)));
typedef __bf16 bf16x2 __attribute__((ext_vector_type(2)));

#define POLLAUX 16
constexpr int AUX_NT = 2, AUX_SC1 = 16, AUX_POLL = POLLAUX;
constexpr unsigned OOB = 0x7ffffff0u;
constexpr unsigned WS_EPOCH = 0;
constexpr unsigned WS_Q1 = 4096;
constexpr unsigned WS_H1 = WS_Q1 + 32 * 8 * 1024;
constexpr unsigned WS_Q2 = WS_H1 + 32 * 8 * 1024;
constexpr unsigned WS_H2 = WS_Q2 + 32 * 8 * 128;
constexpr unsigned WS_SBOX = WS_H2 + 32 * 8 * 128;
constexpr unsigned WS_OBOX = WS_SBOX + 32 * 8 * 128;
constexpr unsigned WS_YBOX = WS_OBOX + 32 * 8 * 1024;
constexpr unsigned WS_FBOX = WS_YBOX + 8 * 32 * 1280;
constexpr unsigned WS_END = WS_FBOX + 8 * 32 * 1280;
constexpr unsigned SPIN_CAP = 1u << 16;

#define RSRC(p, n) __builtin_amdgcn_make_buffer_rsrc((void*)(p), 0, (n), 0x00020000)
#define LD128(rs, vo, so, aux) __builtin_amdgcn_raw_buffer_load_b128((rs), (vo), (so), (aux))
#define ST128(v, rs, vo, so, aux) __builtin_amdgcn_raw_buffer_store_b128((v), (rs), (vo), (so), (aux))
#define SBAR() __builtin_amdgcn_sched_barrier(0)
#define LAS __attribute__((address_space(3)))
#define LDS_LD32(p) __hip_atomic_load((LAS unsigned*)(p), __ATOMIC_RELAXED, __HIP_MEMORY_SCOPE_WORKGROUP)
#define LDS_ST32(p, v) __hip_atomic_store((LAS unsigned*)(p), (v), __ATOMIC_RELAXED, __HIP_MEMORY_SCOPE_WORKGROUP)
#define LDS_LD64(p) __hip_atomic_load((LAS unsigned long long*)(p), __ATOMIC_RELAXED, __HIP_MEMORY_SCOPE_WORKGROUP)
#define LDS_ST64(p, v) __hip_atomic_store((LAS unsigned long long*)(p), (v), __ATOMIC_RELAXED, __HIP_MEMORY_SCOPE_WORKGROUP)
#define CBAR() asm volatile("" ::: "memory")

template <int CTRL> __device__ __forceinline__ unsigned dppu(unsigned x) { return (unsigned)__builtin_amdgcn_mov_dpp((int)x, CTRL, 0xf, 0xf, true); }
template <int CTRL> __device__ __forceinline__ float dppf(float x) { return __uint_as_float(dppu<CTRL>(__float_as_uint(x))); }
constexpr int XOR1 = 0xB1, XOR2 = 0x4E, XOR7 = 0x141, ROR8 = 0x128, ROR4 = 0x124, SHL4 = 0x104, SHR4 = 0x114, BCAST = 0x150;

__device__ __forceinline__ float dot2(unsigned k, unsigned q, float acc) {
  return __builtin_amdgcn_fdot2_f32_bf16(__builtin_bit_cast(bf16x2, k), __builtin_bit_cast(bf16x2, q), acc, false);
}
__device__ __forceinline__ unsigned pk_bf16(float lo, float hi) {
  bf16x2 v; v[0] = (__bf16)lo; v[1] = (__bf16)hi; return __builtin_bit_cast(unsigned, v);
}
__device__ __forceinline__ float rbf(float f) { return __uint_as_float(pk_bf16(f, f) & 0xffff0000u); }
__device__ __forceinline__ f32x4 mfma(u32x4 a, u32x4 b, f32x4 c) {
  return __builtin_amdgcn_mfma_f32_16x16x32_bf16(__builtin_bit_cast(bf16x8, a), __builtin_bit_cast(bf16x8, b), c, 0, 0, 0);
}
__device__ __forceinline__ float x16_sum(float x) { auto s = __builtin_amdgcn_permlane16_swap(__float_as_uint(x), __float_as_uint(x), false, false); return __uint_as_float(s[0]) + __uint_as_float(s[1]); }
__device__ __forceinline__ float x32_sum(float x) { auto s = __builtin_amdgcn_permlane32_swap(__float_as_uint(x), __float_as_uint(x), false, false); return __uint_as_float(s[0]) + __uint_as_float(s[1]); }
__device__ __forceinline__ float x16_max(float x) { auto s = __builtin_amdgcn_permlane16_swap(__float_as_uint(x), __float_as_uint(x), false, false); return fmaxf(__uint_as_float(s[0]), __uint_as_float(s[1])); }
__device__ __forceinline__ float x32_max(float x) { auto s = __builtin_amdgcn_permlane32_swap(__float_as_uint(x), __float_as_uint(x), false, false); return fmaxf(__uint_as_float(s[0]), __uint_as_float(s[1])); }
__device__ __forceinline__ float row_sum(float x) { x += dppf<XOR1>(x); x += dppf<XOR2>(x); x += dppf<ROR4>(x); x += dppf<ROR8>(x); return x; }
__device__ __forceinline__ float row_max(float x) { x = fmaxf(x, dppf<XOR1>(x)); x = fmaxf(x, dppf<XOR2>(x)); x = fmaxf(x, dppf<ROR4>(x)); x = fmaxf(x, dppf<ROR8>(x)); return x; }
template <int C0, int C1> __device__ __forceinline__ float trsel(float lo, float hi, bool bit) {
  const float t0 = lo + dppf<C0>(lo), t1 = hi + dppf<C1>(hi);
  return bit ? t1 : t0;
}
__device__ __forceinline__ bool gok(u32x4 g, unsigned tag) { return (g[1] == tag) & (g[3] == tag); }
__device__ __forceinline__ u32x4 gran2(float a, float b, unsigned tag) { u32x4 g; g[0] = __float_as_uint(a); g[1] = tag; g[2] = __float_as_uint(b); g[3] = tag; return g; }
__device__ __forceinline__ void wg_barrier() { asm volatile("s_waitcnt lgkmcnt(0)\n\ts_barrier" ::: "memory"); }

constexpr float LOG2E = 1.4426950408889634f;

#define POLLDELAY() do { if (PDELAY) __builtin_amdgcn_s_sleep(PDELAY); } while (0)
#define NPACE 16
#define MAPX 0
#define KVORD 0
#define ABL_NOX0 0
#define ABL_NOB0 0
#define OSHARED 1
#define ABL 0
#define X0MODE 0
#define ARGPIN 1
#define W2LATE 1
#define PRETOUCH 0
#define VMWAIT(n) asm volatile("s_waitcnt vmcnt(%0)" :: "i"(n) : "memory")

__device__ __forceinline__ f32x2 sum8(const float* p) {
  f32x2 a[8];
#pragma unroll
  for (int i = 0; i < 8; ++i) a[i] = *(const f32x2*)(p + i * 256);
  return ((a[0] + a[1]) + (a[2] + a[3])) + ((a[4] + a[5]) + (a[6] + a[7]));
}

__global__ void __launch_bounds__(640, 3)
fused_decode(const unsigned short* x, const unsigned short* kc, const unsigned short* vc,
             const unsigned short* wq, const unsigned short* wo, const unsigned short* w1,
             const unsigned short* w2, unsigned short* out, unsigned char* ws) {
  asm volatile("" : "+s"(x), "+s"(kc), "+s"(vc), "+s"(wq), "+s"(wo), "+s"(w1), "+s"(w2), "+s"(out), "+s"(ws));
  extern __shared__ __attribute__((aligned(16))) unsigned char smem[];
  f32x4* red = (f32x4*)smem;
  unsigned* qs = (unsigned*)(smem + 24576);
  unsigned* hs = (unsigned*)(smem + 24576 + 256);
  float* st = (float*)(smem + 24576 + 768);
  unsigned* flg = (unsigned*)(smem + 24576 + 832);
  float* gw = (float*)(smem + 24576 + 896);
  unsigned* wpl = (unsigned*)(smem + 24576 + 848);
  unsigned* os = (unsigned*)(smem + 24576 + 4096);
  unsigned* xl = (unsigned*)(smem + 24576 + 1024);
  const int blk = blockIdx.x, tid = threadIdx.x, lane = tid & 63, w = __builtin_amdgcn_readfirstlane(tid >> 6);
  STAMP_DECL
  const unsigned tstart_ = (unsigned)__builtin_amdgcn_s_memrealtime();
  STAMP(0);
  const int j = (blk >> 3) & 7, h = (blk & 7) + 8 * (blk >> 6), b = j >> 2, s = j & 3;
  const int r = lane >> 4, c = lane & 15;
  const bool ffn = h < 27;
  const __amdgpu_buffer_rsrc_t rws = RSRC(ws, WS_END);
  const __amdgpu_buffer_rsrc_t rx = RSRC(x, 2560);
  u32x4 mask;
#pragma unroll
  for (int i = 0; i < 4; ++i) mask[i] = (i == ((c & 7) >> 1)) ? ((c & 1) ? 0xffff0000u : 0xffffu) : 0u;
  auto sum_publish = [&](const f32x4* rr, unsigned box, int cbase, unsigned tag) {
    const int cr = cbase + (lane >> 3), e = lane & 7;
    const float* p = (const float*)rr + ((e >> 1) * 16 + (cr & 15)) * 4 + 2 * (e & 1);
    const f32x2 v = sum8(p);
    const float v0 = v[0], v1 = v[1];
    ST128(gran2(v0, v1, tag), rws, cr < 10 ? (unsigned)((j * 32 + h) * 1280 + cr * 128 + e * 16) : OOB, box, AUX_SC1);
  };

  if (w >= 2) {
    const int sw = w - 2;
    const size_t kvbase = ((size_t)(b * 32 + h) * 8192 + 6144 + s * 512) * 128;
    const __amdgpu_buffer_rsrc_t rk = RSRC(kc + kvbase, 131072), rv = RSRC(vc + kvbase, 131072);
    const __amdgpu_buffer_rsrc_t ro = RSRC(wo + (size_t)h * 128 * 640 + j * 80, 128 * 1280 - j * 160);
    const __amdgpu_buffer_rsrc_t r2 = RSRC(w2 + (size_t)h * 64 * 640 + j * 80, ffn ? 64 * 1280 - j * 160 : 0);
    const int rot = sw * 4 + ((blk * 37) & 127);
    const int voff = lane * 16;
#define KVOFF(t) ((((((t) >> 2) * 32 + ((t) & 3) + rot) & 127) << 10))
    u32x4 K[16], V[16], WO[4], W2[2];
    float sc = 0.f;
    auto scores = [&]() {
      CBAR();
      const u32x4 q4 = *(const u32x4*)(qs + 4 * c);
      float sb[4];
#pragma unroll
      for (int be = 0; be < 4; ++be) {
        float d[4];
#pragma unroll
        for (int tt = 0; tt < 4; ++tt) {
          const u32x4 kk = K[4 * be + tt];
          d[tt] = dot2(kk[3], q4[3], dot2(kk[2], q4[2], dot2(kk[1], q4[1], dot2(kk[0], q4[0], 0.f))));
        }
        const float e0 = trsel<XOR1, XOR1>(d[0], d[1], c & 1);
        const float e1 = trsel<XOR1, XOR1>(d[2], d[3], c & 1);
        sb[be] = trsel<XOR2, XOR2>(e0, e1, c & 2);
      }
      const float f0 = trsel<SHL4, SHR4>(sb[0], sb[1], c & 4);
      const float f1 = trsel<SHL4, SHR4>(sb[2], sb[3], c & 4);
      const float dotv = trsel<ROR8, ROR8>(f0, f1, c & 8);
      sc = rbf(rbf(dotv) * 0.08838834764831845f);
      const float mw = x32_max(x16_max(row_max(sc)));
      const float ew = __builtin_amdgcn_exp2f((sc - mw) * LOG2E);
      const float lw = x32_sum(x16_sum(row_sum(ew)));
      if (lane == 0) LDS_ST64(st + 2 * sw, (unsigned long long)__float_as_uint(mw) | ((unsigned long long)__float_as_uint(lw) << 32));
    };
    if (PRIO) __builtin_amdgcn_s_setprio(PRIO == 3 ? 3 : 2);
    wg_barrier();
#pragma unroll
    for (int i = 0; i < 24; ++i) {
      if (PRIO == 1 && i == 16) __builtin_amdgcn_s_setprio(1);
      if (PRIO == 3 && i == 16) __builtin_amdgcn_s_setprio(2);
      if (PRIO == 2 && i == 8) __builtin_amdgcn_s_setprio(1);
      if (PRIO == 2 && i == 16) __builtin_amdgcn_s_setprio(0);
      if (i >= WK) VMWAIT(WK - 1);
      if (i < 16) K[i] = LD128(rk, voff, KVOFF(i), AUX_NT); else V[i - 16] = LD128(rv, voff, KVOFF(i - 16), AUX_NT);
      SBAR();
      if (i == WK - 1) STAMP(1);
    }
    STAMP(4);
    const bool early = __builtin_amdgcn_readfirstlane(LDS_LD32(flg + 1)) != 0u;
    if (early) { scores(); STAMP(5); }
    SBAR();
    if (PRIO == 1) __builtin_amdgcn_s_setprio(0);
    if (PRIO == 3) __builtin_amdgcn_s_setprio(1);
#pragma unroll
    for (int t = 8; t < 16; ++t) {
      VMWAIT(WK2 - 1);
      V[t] = LD128(rv, voff, KVOFF(t), AUX_NT);
      SBAR();
    }
    if (!early) {
      for (unsigned spins = 0; spins < (SPIN_CAP << 4); ++spins) {
        if (__builtin_amdgcn_readfirstlane(LDS_LD32(flg + 1)) != 0u) break;
        __builtin_amdgcn_s_sleep(1);
      }
      scores();
      STAMP(6);
    }
    SBAR();
    {
      if (PRIO == 3) __builtin_amdgcn_s_setprio(0);
      const unsigned vo4 = c < 10 ? (unsigned)(r * 4 * 1280 + c * 16) : OOB;
#pragma unroll
      for (int i = 0; i < 4; ++i) { VMWAIT(WK2 - 1); WO[i] = LD128(ro, vo4, (16 * sw + i) * 1280, AUX_NT); SBAR(); }
    }
    {
      const unsigned vo2 = c < 10 ? (unsigned)(r * 2 * 1280 + c * 16) : OOB;
#pragma unroll
      for (int i = 0; i < 2; ++i) { VMWAIT(WK2 - 1); W2[i] = LD128(r2, vo2, (8 * sw + i) * 1280, AUX_NT); SBAR(); }
    }
    STAMP(7);
    unsigned pp;
    {
      unsigned long long t_ = LDS_LD64(st + 2 * (lane & 7));
      for (unsigned spins = 0; !__all((unsigned)(t_ >> 32) != 0u) && spins < (SPIN_CAP << 4); ++spins) { __builtin_amdgcn_s_sleep(1); t_ = LDS_LD64(st + 2 * (lane & 7)); }
      float M = __uint_as_float((unsigned)t_);
      M = fmaxf(M, dppf<XOR1>(M)); M = fmaxf(M, dppf<XOR2>(M)); M = fmaxf(M, dppf<XOR7>(M));
      const float pv = __builtin_amdgcn_exp2f((sc - M) * LOG2E);
      pp = pk_bf16(pv, pv);
    }
    const unsigned tag = __builtin_amdgcn_readfirstlane(LDS_LD32(flg));
    f32x4 oacc = {0.f, 0.f, 0.f, 0.f};
#define PVSTEP(t) oacc = mfma(mask & dppu<BCAST + (t)>(pp), V[t], oacc);
    PVSTEP(0) PVSTEP(1) PVSTEP(2) PVSTEP(3) PVSTEP(4) PVSTEP(5) PVSTEP(6) PVSTEP(7)
    PVSTEP(8) PVSTEP(9) PVSTEP(10) PVSTEP(11) PVSTEP(12) PVSTEP(13) PVSTEP(14) PVSTEP(15)
#undef PVSTEP
    red[sw * 64 + lane] = oacc;
    STAMP(8);
    wg_barrier();
    STAMP(9);
    if (ffn) {
      const u32x2 hv = *(const u32x2*)(hs + (c >> 3) * 64 + 8 * sw + 2 * r);
      f32x4 facc = {0.f, 0.f, 0.f, 0.f};
      facc = mfma(mask & hv[0], W2[0], facc);
      facc = mfma(mask & hv[1], W2[1], facc);
      red[(8 + sw) * 64 + lane] = facc;
      asm volatile("s_waitcnt lgkmcnt(0)" ::: "memory");
      if (lane == 0) __hip_atomic_fetch_add((LAS unsigned*)(flg + 3), 1u, __ATOMIC_RELAXED, __HIP_MEMORY_SCOPE_WORKGROUP);
    }
    VMWAIT(0);
    STAMP(15);
    wg_barrier();
    STAMP(10);
    {
      const u32x4 ov = *(const u32x4*)(os + (c >> 3) * 128 + 16 * sw + 4 * r);
      f32x4 y = {0.f, 0.f, 0.f, 0.f}, y2 = {0.f, 0.f, 0.f, 0.f};
      y = mfma(mask & ov[0], WO[0], y);
      y2 = mfma(mask & ov[1], WO[1], y2);
      y = mfma(mask & ov[2], WO[2], y);
      y2 = mfma(mask & ov[3], WO[3], y2);
      red[(16 + sw) * 64 + lane] = y + y2;
    }
    STAMP(11);
    wg_barrier();
    if (sw == 0) sum_publish(red + 1024, WS_YBOX, 8, tag);
  } else {
    __builtin_amdgcn_s_setprio(3);
    const bool isq = (w == 0);
    const unsigned epoch = __builtin_amdgcn_raw_buffer_load_b32(rws, 0, WS_EPOCH, AUX_SC1);
    u32x2 xs[5];
#pragma unroll
    for (int i = 0; i < 5; ++i) xs[i] = __builtin_amdgcn_raw_buffer_load_b64(rx, (lane >> 5) * 1280 + (lane & 31) * 8, i * 256, 0);
    const int row0 = isq ? 160 * (j >> 1) : 80 * j;
    const int nrow = isq ? 160 : 80;
    unsigned short xr[5];
#pragma unroll
    for (int i = 0; i < 5; ++i) xr[i] = __builtin_amdgcn_raw_buffer_load_b16(rx, (lane >> 5) * 1280 + (lane & 31) * 2, (row0 + 32 * i) * 2, 0);
    u32x4 wa[20];
    {
      const __amdgpu_buffer_rsrc_t rw = isq ? RSRC(wq + h * 128 + (j & 1) * 64, 640 * 4096 * 2 - h * 256 - (j & 1) * 128) : RSRC(w1 + h * 64, ffn ? 640 * 1728 * 2 - h * 128 : 0);
      const int rstride = isq ? 8192 : 3456;
      const unsigned vo = (unsigned)((lane >> 3) * rstride + (lane & 7) * 16);
#pragma unroll
      for (int i = 0; i < 10; ++i) wa[i] = LD128(rw, vo, (row0 + 8 * i) * rstride, AUX_NT);
      if (isq) {
#pragma unroll
        for (int i = 10; i < 20; ++i) wa[i] = LD128(rw, vo, (row0 + 8 * i) * rstride, AUX_NT);
      }
    }
    if (isq && lane < 28) LDS_ST32(st + lane, 0u);
    STAMP(1);
    wg_barrier();
    float xf[20];
#pragma unroll
    for (int i = 0; i < 5; ++i) {
      xf[4 * i + 0] = __uint_as_float(xs[i][0] << 16); xf[4 * i + 1] = __uint_as_float(xs[i][0] & 0xffff0000u);
      xf[4 * i + 2] = __uint_as_float(xs[i][1] << 16); xf[4 * i + 3] = __uint_as_float(xs[i][1] & 0xffff0000u);
    }
    float sum = 0.f;
#pragma unroll
    for (int i = 0; i < 20; ++i) sum += xf[i];
    sum = x16_sum(row_sum(sum));
    const float mean = sum * (1.f / 640.f);
    float vs = 0.f;
#pragma unroll
    for (int i = 0; i < 20; ++i) { const float d = xf[i] - mean; vs = fmaf(d, d, vs); }
    vs = x16_sum(row_sum(vs));
    const float mb = rbf(mean), den = rbf(sqrtf(rbf(rbf(vs * (1.f / 640.f)) + 1e-5f)));
    unsigned* xw_ = xl + w * 320;
#pragma unroll
    for (int i = 0; i < 5; ++i) {
      const int row = (lane & 31) + 32 * i;
      const float xn = rbf(__uint_as_float((unsigned)xr[i] << 16) - mb) / den;
      if (row < nrow) xw_[(lane >> 5) * 160 + ((row >> 1) & 3) * 40 + (row >> 3) * 2 + (row & 1)] = pk_bf16(xn, xn);
    }
    const unsigned tag = __builtin_amdgcn_readfirstlane(epoch) + 0x9E3779B9u;
    if (isq && lane == 0) LDS_ST32(flg, tag);
    asm volatile("s_waitcnt lgkmcnt(0)" ::: "memory");
    STAMP(2);
    f32x4 acc0 = {0.f, 0.f, 0.f, 0.f}, acc1 = {0.f, 0.f, 0.f, 0.f};
    const unsigned* xrd = xw_ + (c >> 3) * 160 + r * 40;
#pragma unroll
    for (int i = 0; i < 10; i += 2) {
      const u32x4 xa = *(const u32x4*)(xrd + 2 * i);
      acc0 = mfma(mask & xa[0], wa[i], acc0); acc1 = mfma(mask & xa[1], wa[i], acc1);
      acc0 = mfma(mask & xa[2], wa[i + 1], acc0); acc1 = mfma(mask & xa[3], wa[i + 1], acc1);
    }
    if (isq) {
#pragma unroll
      for (int i = 10; i < 20; i += 2) {
        const u32x4 xa = *(const u32x4*)(xrd + 2 * i);
        acc0 = mfma(mask & xa[0], wa[i], acc0); acc1 = mfma(mask & xa[1], wa[i], acc1);
        acc0 = mfma(mask & xa[2], wa[i + 1], acc0); acc1 = mfma(mask & xa[3], wa[i + 1], acc1);
      }
    }
    if (isq || ffn) {
      f32x4 v;
#pragma unroll
      for (int i = 0; i < 4; ++i) { const float t_ = c < 8 ? acc0[i] : acc1[i]; v[i] = t_ + dppf<ROR8>(t_); }
      const u32x4 pub = gran2(c < 8 ? v[0] : v[2], c < 8 ? v[1] : v[3], tag);
      const unsigned cu = (unsigned)(h * 8 + j);
      if (isq) {
        if (X0MODE != 1 && X0MODE != 3) ST128(pub, rws, cu * 1024 + lane * 16, WS_Q1, AUX_SC1);
        STAMP(3);
        POLLDELAY();
        const unsigned goff = (unsigned)((h * 8 + (lane >> 5)) * 1024 + (b * 32 + (lane & 31)) * 16);
        u32x4 G[4];
#pragma unroll
        for (int p = 0; p < 4; ++p) G[p] = (X0MODE == 1 || X0MODE == 2) ? pub : LD128(rws, goff, WS_Q1 + p * 2048, AUX_POLL);
        for (unsigned spins = 0; X0MODE == 0 || X0MODE == 4; ++spins) {
          bool allok = true;
          CBAR();
#pragma unroll
          for (int p = 0; p < 4; ++p) if (!__all(gok(G[p], tag))) { allok = false; G[p] = LD128(rws, goff, WS_Q1 + p * 2048, AUX_POLL); }
          if (spins == 0) STAMP(14);
          if (allok || spins > SPIN_CAP) break;
        }
        const float a0 = (__uint_as_float(G[0][0]) + __uint_as_float(G[1][0])) + (__uint_as_float(G[2][0]) + __uint_as_float(G[3][0]));
        const float a1 = (__uint_as_float(G[0][2]) + __uint_as_float(G[1][2])) + (__uint_as_float(G[2][2]) + __uint_as_float(G[3][2]));
        qs[(lane >> 5) * 32 + (c & 7) * 4 + (r & 1) * 2 + (c >> 3)] = pk_bf16(a0, a1);
      } else {
        if (X0MODE == 0 || X0MODE == 2) ST128(pub, rws, (unsigned)(((h * 8 + (c & 7)) * 8 + j) * 128 + (c >> 3) * 64 + r * 16), WS_H1, AUX_SC1);
        STAMP(3);
        POLLDELAY(); POLLDELAY();
        const unsigned goff = cu * 1024 + lane * 16;
        u32x4 G0 = (X0MODE == 0 || X0MODE == 3) ? LD128(rws, goff, WS_H1, AUX_POLL) : pub;
        for (unsigned spins = 0; X0MODE == 0 && !__all(gok(G0, tag)) && spins < SPIN_CAP; ++spins) { CBAR(); G0 = LD128(rws, goff, WS_H1, AUX_POLL); }
        STAMP(14);
        float a0 = __uint_as_float(G0[0]), a1 = __uint_as_float(G0[2]);
        a0 += dppf<ROR8>(a0); a1 += dppf<ROR8>(a1);
        a0 = fmaxf(x32_sum(x16_sum(a0)), 0.f); a1 = fmaxf(x32_sum(x16_sum(a1)), 0.f);
        u32x2 g2; g2[0] = pk_bf16(a0, a1); g2[1] = tag;
        if (X0MODE == 0 || X0MODE == 2) __builtin_amdgcn_raw_buffer_store_b64(g2, rws, lane < 8 ? cu * 128 + lane * 8 : OOB, WS_H2, AUX_SC1);
        const unsigned g2off = (unsigned)((h * 8 + (lane >> 3)) * 128 + (lane & 7) * 8);
        POLLDELAY(); POLLDELAY(); POLLDELAY();
        u32x2 q2 = (X0MODE == 0 || X0MODE == 3) ? __builtin_amdgcn_raw_buffer_load_b64(rws, g2off, WS_H2, AUX_POLL) : g2;
        for (unsigned spins = 0; X0MODE == 0 && !__all(q2[1] == tag) && spins < SPIN_CAP; ++spins) { CBAR(); q2 = __builtin_amdgcn_raw_buffer_load_b64(rws, g2off, WS_H2, AUX_POLL); }
        u32x2 hv; hv[0] = (q2[0] & 0xffffu) | (q2[0] << 16); hv[1] = (q2[0] >> 16) | (q2[0] & 0xffff0000u);
        *(u32x2*)(hs + ((lane >> 1) & 1) * 64 + (lane >> 3) * 8 + (lane & 1) * 4 + ((lane >> 2) & 1) * 2) = hv;
      }
      asm volatile("s_waitcnt lgkmcnt(0)" ::: "memory");
      if (lane == 0) LDS_ST32(flg + 1 + w, 1u);
    }
    STAMP(4);
    if (isq) {
      u32x2 ml;
      for (unsigned spins = 0;; ++spins) {
        { const unsigned long long t_ = LDS_LD64(st + 2 * (lane & 7)); ml[0] = (unsigned)t_; ml[1] = (unsigned)(t_ >> 32); }
        if (__all(ml[1] != 0u) || spins > (SPIN_CAP << 4)) break;
        __builtin_amdgcn_s_sleep(2);
      }
      const float m_ = __uint_as_float(ml[0]), l_ = __uint_as_float(ml[1]);
      float M = fmaxf(m_, dppf<XOR1>(m_)); M = fmaxf(M, dppf<XOR2>(M)); M = fmaxf(M, dppf<XOR7>(M));
      const float f_ = __builtin_amdgcn_exp2f((m_ - M) * LOG2E);
      float L = f_ * l_; L += dppf<XOR1>(L); L += dppf<XOR2>(L); L += dppf<XOR7>(L);
      ST128(gran2(M, L, tag), rws, lane == 0 ? WS_SBOX + (unsigned)((h * 8 + j) * 128) : OOB, 0, AUX_SC1);
      STAMP(5);
      STAMP(7);
    }
    wg_barrier();
    STAMP(8);
    if (!isq && ffn) {
      for (unsigned spins = 0; spins < (SPIN_CAP << 4); ++spins) { if (__builtin_amdgcn_readfirstlane(LDS_LD32(flg + 3)) == 8u) break; }
      CBAR();
      sum_publish(red + 512, WS_FBOX, 0, tag);
      sum_publish(red + 512, WS_FBOX, 8, tag);
    }
    if (isq) {
      const unsigned soff = WS_SBOX + (unsigned)((h * 8 + (lane & 7)) * 128);
      const unsigned ooff = (unsigned)(h * 8 * 1024 + lane * 16);
      const f32x2 own = sum8((const float*)red + (((lane >> 1) & 1) * 16 + (lane >> 2)) * 4 + 2 * (lane & 1));
      const u32x4 ownG = gran2(own[0], own[1], tag);
      ST128(ownG, rws, ooff, WS_OBOX + j * 1024, AUX_SC1);
      STAMP(6);
      u32x4 gs = LD128(rws, soff, 0, AUX_POLL);
      if (H1DELAY) __builtin_amdgcn_s_sleep(H1DELAY);
      u32x4 G[8];
#pragma unroll
      for (int p = 0; p < 8; ++p) G[p] = LD128(rws, p == j ? OOB : ooff, WS_OBOX + p * 1024, AUX_POLL);
#pragma unroll
      for (int p = 0; p < 8; ++p) if (p == j) G[p] = ownG;
      for (unsigned spins = 0; !__all(gok(gs, tag)) && spins < SPIN_CAP; ++spins) { CBAR(); __builtin_amdgcn_s_sleep(2); gs = LD128(rws, soff, 0, AUX_POLL); }
      float wp[8];
      {
        const float ms = __uint_as_float(gs[0]), ls = __uint_as_float(gs[2]);
        float Mg = fmaxf(ms, dppf<XOR1>(ms)); Mg = fmaxf(Mg, dppf<XOR2>(Mg));
        const float e_ = __builtin_amdgcn_exp2f((ms - Mg) * LOG2E);
        float Lg = e_ * ls; Lg += dppf<XOR1>(Lg); Lg += dppf<XOR2>(Lg);
        const float wgt = e_ / Lg;
#pragma unroll
        for (int p = 0; p < 8; ++p) wp[p] = __uint_as_float(__builtin_amdgcn_readlane(__float_as_uint(wgt), p));
      }
      STAMP(9);
      for (unsigned spins = 0;; ++spins) {
        bool allok = true;
        CBAR();
#pragma unroll
        for (int p = 0; p < 8; ++p) {
          if (!__all(gok(G[p], tag))) { if (allok && HSLEEP) __builtin_amdgcn_s_sleep(HSLEEP); allok = false; G[p] = LD128(rws, ooff, WS_OBOX + p * 1024, AUX_POLL); }
        }
        if (allok || spins > SPIN_CAP) break;
      }
      STAMP(10);
#pragma unroll
      for (int bb = 0; bb < 2; ++bb) {
        float o0 = 0.f, o1 = 0.f;
#pragma unroll
        for (int sp = 0; sp < 4; ++sp) { o0 = fmaf(wp[4 * bb + sp], __uint_as_float(G[4 * bb + sp][0]), o0); o1 = fmaf(wp[4 * bb + sp], __uint_as_float(G[4 * bb + sp][2]), o1); }
        u32x2 ov; ov[0] = pk_bf16(o0, o0); ov[1] = pk_bf16(o1, o1);
        *(u32x2*)(os + bb * 128 + 2 * lane) = ov;
      }
    }
    wg_barrier();
    const int e = lane & 7, ob = e >> 2, ocol = j * 80 + 8 * h + 4 * ((e >> 1) & 1) + 2 * (e & 1);
    const bool fin = !isq && h < 10;
    const unsigned yo = (unsigned)((j * 32 + (lane >> 3)) * 1280 + h * 128 + e * 16);
    const bool f3v = (lane >> 3) < 3;
    unsigned xw = 0; float fa = 0.f, fb = 0.f;
    if (fin) {
      xw = __builtin_amdgcn_raw_buffer_load_b32(rx, (ob * 640 + ocol) * 2, 0, 0);
    }
    wg_barrier();
    STAMP(11);
    if (isq) {
      sum_publish(red + 1024, WS_YBOX, 0, tag);
      STAMP(12);
    } else if (fin) {
      u32x4 g[4], y[4];
#pragma unroll
      for (int i = 0; i < 4; ++i) g[i] = LD128(rws, (i < 3 || f3v) ? yo : OOB, WS_FBOX + i * 8 * 1280, AUX_POLL);
#pragma unroll
      for (int i = 0; i < 4; ++i) y[i] = LD128(rws, yo, WS_YBOX + i * 8 * 1280, AUX_POLL);
      for (unsigned spins = 0;; ++spins) {
        bool allok = true;
        CBAR();
#pragma unroll
        for (int i = 0; i < 4; ++i) if (!__all(gok(g[i], tag) | (i == 3 && !f3v))) { allok = false; g[i] = LD128(rws, (i < 3 || f3v) ? yo : OOB, WS_FBOX + i * 8 * 1280, AUX_POLL); }
        if (allok || spins > SPIN_CAP) break;
      }
      fa = (__uint_as_float(g[0][0]) + __uint_as_float(g[1][0])) + (__uint_as_float(g[2][0]) + __uint_as_float(g[3][0]));
      fb = (__uint_as_float(g[0][2]) + __uint_as_float(g[1][2])) + (__uint_as_float(g[2][2]) + __uint_as_float(g[3][2]));
      fa += dppf<ROR8>(fa); fb += dppf<ROR8>(fb);
      fa = rbf(x32_sum(x16_sum(fa))); fb = rbf(x32_sum(x16_sum(fb)));
      const float ga = rbf(fa / (1.f + __expf(-fa))), gb = rbf(fb / (1.f + __expf(-fb)));
      STAMP(10);
      for (unsigned spins = 0;; ++spins) {
        bool allok = true;
        CBAR();
#pragma unroll
        for (int i = 0; i < 4; ++i) if (!__all(gok(y[i], tag))) { allok = false; y[i] = LD128(rws, yo, WS_YBOX + i * 8 * 1280, AUX_POLL); }
        if (allok || spins > SPIN_CAP) break;
      }
      STAMP(13);
      float ya = (__uint_as_float(y[0][0]) + __uint_as_float(y[1][0])) + (__uint_as_float(y[2][0]) + __uint_as_float(y[3][0]));
      float yb = (__uint_as_float(y[0][2]) + __uint_as_float(y[1][2])) + (__uint_as_float(y[2][2]) + __uint_as_float(y[3][2]));
      ya += dppf<ROR8>(ya); yb += dppf<ROR8>(yb);
      ya = rbf(x32_sum(x16_sum(ya))); yb = rbf(x32_sum(x16_sum(yb)));
      const float oa = __uint_as_float(xw << 16) + rbf(ya * ga), obv = __uint_as_float(xw & 0xffff0000u) + rbf(yb * gb);
      __builtin_amdgcn_raw_buffer_store_b32(pk_bf16(oa, obv), RSRC(out, 2560), lane < 8 ? (unsigned)((ob * 640 + ocol) * 2) : OOB, 0, 0);
      if (blk == 0 && lane == 0) __builtin_amdgcn_raw_buffer_store_b32(tag, rws, 0, WS_EPOCH, AUX_SC1);
      STAMP(12);
    }
  }
  if (w == 1 && h < 10) { asm volatile("s_waitcnt vmcnt(0)" ::: "memory"); unsigned t_ = (unsigned)__builtin_amdgcn_s_memrealtime(); unsigned d_ = (t_ - tstart_) * 10u; if (d_ > 60000u) d_ = 60000u; while ((unsigned)__builtin_amdgcn_s_memrealtime() - t_ < d_) __builtin_amdgcn_s_sleep(8); }
}

extern "C" void kernel_launch(void* const* d_in, const int* in_sizes, int n_in, void* d_out, int out_size, void* d_ws, size_t ws_size, hipStream_t stream) {
  constexpr int LDS = 96 * 1024;
  (void)hipFuncSetAttribute((const void*)fused_decode, hipFuncAttributeMaxDynamicSharedMemorySize, LDS);
  fused_decode<<<256, 640, LDS, stream>>>((const unsigned short*)d_in[0], (const unsigned short*)d_in[1], (const unsigned short*)d_in[2],
      (const unsigned short*)d_in[3], (const unsigned short*)d_in[4], (const unsigned short*)d_in[6], (const unsigned short*)d_in[7],
      (unsigned short*)d_out, (unsigned char*)d_ws + (WSEND ? ((ws_size - WS_END) & ~(size_t)4095) : 0));
}
```

```cpp
#define PRIO 3
#define WK 24
#define WK2 16
#define PDELAY 24
#define WSEND 0
#define HSLEEP 0
#define H1DELAY 0
#define PRETOUCH 0
#define CODEPF 2
#include <hip/hip_runtime.h>
#include <hip/hip_bf16.h>
#include <stdint.h>

#define STAMP(i) do {} while (0)
#define STAMP_DECL
#define STAMP_FLUSH() do {} while (0)

typedef unsigned u32x4 __attribute__((ext_vector_type(4)));
typedef unsigned u32x2 __attribute__((ext_vector_type(2)));
typedef float f32x4 __attribute__((ext_vector_type(4)));
typedef float f32x2 __attribute__((ext_vector_type(2)));
typedef __bf16 bf16x8 __attribute__((ext_vector_type(8)));
typedef __bf16 bf16x2 __attribute__((ext_vector_type(2)));

#define POLLAUX 16
constexpr int AUX_NT = 2, AUX_SC1 = 16, AUX_POLL = POLLAUX;
constexpr unsigned OOB = 0x7ffffff0u;
constexpr unsigned WS_EPOCH = 0;
constexpr unsigned WS_Q1 = 4096;
constexpr unsigned WS_H1 = WS_Q1 + 256 * 1024;
constexpr unsigned WS_H2 = WS_H1 + 256 * 1280;
constexpr unsigned WS_SBOX = WS_H2 + 256 * 128;
constexpr unsigned WS_FBOX = WS_SBOX + 256 * 128;
constexpr unsigned WS_END = WS_FBOX + 256 * 1280;
constexpr unsigned WS_OBOX = WS_Q1, WS_YBOX = WS_H1;
constexpr unsigned SPIN_CAP = 1u << 16;

#define RSRC(p, n) __builtin_amdgcn_make_buffer_rsrc((void*)(p), 0, (n), 0x00020000)
#define LD128(rs, vo, so, aux) __builtin_amdgcn_raw_buffer_load_b128((rs), (vo), (so), (aux))
#define ST128(v, rs, vo, so, aux) __builtin_amdgcn_raw_buffer_store_b128((v), (rs), (vo), (so), (aux))
#define SBAR() __builtin_amdgcn_sched_barrier(0)
#define LAS __attribute__((address_space(3)))
#define LDS_LD32(p) __hip_atomic_load((LAS unsigned*)(p), __ATOMIC_RELAXED, __HIP_MEMORY_SCOPE_WORKGROUP)
#define LDS_ST32(p, v) __hip_atomic_store((LAS unsigned*)(p), (v), __ATOMIC_RELAXED, __HIP_MEMORY_SCOPE_WORKGROUP)
#define LDS_LD64(p) __hip_atomic_load((LAS unsigned long long*)(p), __ATOMIC_RELAXED, __HIP_MEMORY_SCOPE_WORKGROUP)
#define LDS_ST64(p, v) __hip_atomic_store((LAS unsigned long long*)(p), (v), __ATOMIC_RELAXED, __HIP_MEMORY_SCOPE_WORKGROUP)
#define LDS_ADD(p) asm volatile("ds_add_u32 %0, %1" :: "v"((unsigned)(size_t)(LAS unsigned*)(p)), "v"(1u) : "memory")
#define CBAR() asm volatile("" ::: "memory")

template <int CTRL> __device__ __forceinline__ unsigned dppu(unsigned x) { return (unsigned)__builtin_amdgcn_mov_dpp((int)x, CTRL, 0xf, 0xf, true); }
template <int CTRL> __device__ __forceinline__ float dppf(float x) { return __uint_as_float(dppu<CTRL>(__float_as_uint(x))); }
constexpr int XOR1 = 0xB1, XOR2 = 0x4E, XOR7 = 0x141, ROR8 = 0x128, ROR4 = 0x124, SHL4 = 0x104, SHR4 = 0x114, BCAST = 0x150;

__device__ __forceinline__ float dot2(unsigned k, unsigned q, float acc) {
  return __builtin_amdgcn_fdot2_f32_bf16(__builtin_bit_cast(bf16x2, k), __builtin_bit_cast(bf16x2, q), acc, false);
}
__device__ __forceinline__ unsigned pk_bf16(float lo, float hi) {
  bf16x2 v; v[0] = (__bf16)lo; v[1] = (__bf16)hi; return __builtin_bit_cast(unsigned, v);
}
__device__ __forceinline__ float rbf(float f) { return __uint_as_float(pk_bf16(f, f) & 0xffff0000u); }
__device__ __forceinline__ f32x4 mfma(u32x4 a, u32x4 b, f32x4 c) {
  return __builtin_amdgcn_mfma_f32_16x16x32_bf16(__builtin_bit_cast(bf16x8, a), __builtin_bit_cast(bf16x8, b), c, 0, 0, 0);
}
__device__ __forceinline__ float x16_sum(float x) { auto s = __builtin_amdgcn_permlane16_swap(__float_as_uint(x), __float_as_uint(x), false, false); return __uint_as_float(s[0]) + __uint_as_float(s[1]); }
__device__ __forceinline__ float x32_sum(float x) { auto s = __builtin_amdgcn_permlane32_swap(__float_as_uint(x), __float_as_uint(x), false, false); return __uint_as_float(s[0]) + __uint_as_float(s[1]); }
__device__ __forceinline__ float x16_max(float x) { auto s = __builtin_amdgcn_permlane16_swap(__float_as_uint(x), __float_as_uint(x), false, false); return fmaxf(__uint_as_float(s[0]), __uint_as_float(s[1])); }
__device__ __forceinline__ float x32_max(float x) { auto s = __builtin_amdgcn_permlane32_swap(__float_as_uint(x), __float_as_uint(x), false, false); return fmaxf(__uint_as_float(s[0]), __uint_as_float(s[1])); }
__device__ __forceinline__ float row_sum(float x) { x += dppf<XOR1>(x); x += dppf<XOR2>(x); x += dppf<ROR4>(x); x += dppf<ROR8>(x); return x; }
__device__ __forceinline__ float row_max(float x) { x = fmaxf(x, dppf<XOR1>(x)); x = fmaxf(x, dppf<XOR2>(x)); x = fmaxf(x, dppf<ROR4>(x)); x = fmaxf(x, dppf<ROR8>(x)); return x; }
template <int C0, int C1> __device__ __forceinline__ float trsel(float lo, float hi, bool bit) {
  const float t0 = lo + dppf<C0>(lo), t1 = hi + dppf<C1>(hi);
  return bit ? t1 : t0;
}
__device__ __forceinline__ bool gok(u32x4 g, unsigned tag) { return (g[1] == tag) & (g[3] == tag); }
__device__ __forceinline__ u32x4 gran2(float a, float b, unsigned tag) { u32x4 g; g[0] = __float_as_uint(a); g[1] = tag; g[2] = __float_as_uint(b); g[3] = tag; return g; }
__device__ __forceinline__ void wg_barrier() { asm volatile("s_waitcnt lgkmcnt(0)\n\ts_barrier" ::: "memory"); }

constexpr float LOG2E = 1.4426950408889634f;

#define POLLDELAY() do { if (PDELAY) __builtin_amdgcn_s_sleep(PDELAY); } while (0)
#define NPACE 16
#define MAPX 0
#define KVORD 0
#define ABL_NOX0 0
#define ABL_NOB0 0
#define OSHARED 1
#define ABL 0
#define X0MODE 0
#define ARGPIN 1
#define W2LATE 1
#define ACKDELAY 48
#define STATS_EARLY 0
#define CODEPF_W 7
#define CODEPF_AUX 0
#define CODE_BACK 0
#define VMWAIT(n) asm volatile("s_waitcnt vmcnt(%0)" :: "i"(n) : "memory")

__device__ __forceinline__ f32x2 sum8(const float* p) {
  f32x2 a[8];
#pragma unroll
  for (int i = 0; i < 8; ++i) a[i] = *(const f32x2*)(p + i * 256);
  return ((a[0] + a[1]) + (a[2] + a[3])) + ((a[4] + a[5]) + (a[6] + a[7]));
}

__global__ void __launch_bounds__(640, 3)
fused_decode(const unsigned short* x, const unsigned short* kc, const unsigned short* vc,
             const unsigned short* wq, const unsigned short* wo, const unsigned short* w1,
             const unsigned short* w2, unsigned short* out, unsigned char* ws) {
  unsigned long long pc_entry;
  asm volatile("s_getpc_b64 %0" : "=s"(pc_entry));
  asm volatile("" : "+s"(x), "+s"(kc), "+s"(vc), "+s"(wq), "+s"(wo), "+s"(w1), "+s"(w2), "+s"(out), "+s"(ws));
  extern __shared__ __attribute__((aligned(16))) unsigned char smem[];
  f32x4* red = (f32x4*)smem;
  unsigned* qs = (unsigned*)(smem + 24576);
  unsigned* hs = (unsigned*)(smem + 24576 + 256);
  float* st = (float*)(smem + 24576 + 768);
  unsigned* flg = (unsigned*)(smem + 24576 + 832);
  float* gw = (float*)(smem + 24576 + 896);
  unsigned* wpl = (unsigned*)(smem + 24576 + 848);
  unsigned* os = (unsigned*)(smem + 24576 + 4096);
  unsigned* xl = (unsigned*)(smem + 24576 + 1024);
  const int blk = blockIdx.x, tid = threadIdx.x, lane = tid & 63, w = __builtin_amdgcn_readfirstlane(tid >> 6);
  STAMP_DECL
  const unsigned tstart_ = (unsigned)__builtin_amdgcn_s_memrealtime();
  STAMP(0);
  const int j = (blk >> 3) & 7, h = (blk & 7) + 8 * (blk >> 6), b = j >> 2, s = j & 3;
  const int r = lane >> 4, c = lane & 15;
  const bool ffn = h < 27;
  const __amdgpu_buffer_rsrc_t rws = RSRC(ws, WS_END);
  const __amdgpu_buffer_rsrc_t rx = RSRC(x, 2560);
  u32x4 mask;
#pragma unroll
  for (int i = 0; i < 4; ++i) mask[i] = (i == ((c & 7) >> 1)) ? ((c & 1) ? 0xffff0000u : 0xffffu) : 0u;
  auto sum_publish = [&](const f32x4* rr, unsigned box, int cbase, unsigned tag) {
    const int cr = cbase + (lane >> 3), e = lane & 7;
    const float* p = (const float*)rr + ((e >> 1) * 16 + (cr & 15)) * 4 + 2 * (e & 1);
    const f32x2 v = sum8(p);
    const float v0 = v[0], v1 = v[1];
    ST128(gran2(v0, v1, tag), rws, cr < 10 ? (unsigned)((h * 8 + j) * 1280 + cr * 128 + e * 16) : OOB, box, AUX_SC1);
  };

  if (w >= 2) {
    const int sw = w - 2;
    const size_t kvbase = ((size_t)(b * 32 + h) * 8192 + 6144 + s * 512) * 128;
    const __amdgpu_buffer_rsrc_t rk = RSRC(kc + kvbase, 131072), rv = RSRC(vc + kvbase, 131072);
    const __amdgpu_buffer_rsrc_t ro = RSRC(wo + (size_t)h * 128 * 640 + j * 80, 128 * 1280 - j * 160);
    const __amdgpu_buffer_rsrc_t r2 = RSRC(w2 + (size_t)h * 64 * 640 + j * 80, ffn ? 64 * 1280 - j * 160 : 0);
    const int rot = sw * 4 + ((blk * 37) & 127);
    const int voff = lane * 16;
#define KVOFF(t) ((((((t) >> 2) * 32 + ((t) & 3) + rot) & 127) << 10))
    u32x4 K[16], V[16], WO[4], W2[2];
    float sc = 0.f;
    auto scores = [&]() {
      CBAR();
      const u32x4 q4 = *(const u32x4*)(qs + 4 * c);
      float sb[4];
#pragma unroll
      for (int be = 0; be < 4; ++be) {
        float d[4];
#pragma unroll
        for (int tt = 0; tt < 4; ++tt) {
          const u32x4 kk = K[4 * be + tt];
          d[tt] = dot2(kk[3], q4[3], dot2(kk[2], q4[2], dot2(kk[1], q4[1], dot2(kk[0], q4[0], 0.f))));
        }
        const float e0 = trsel<XOR1, XOR1>(d[0], d[1], c & 1);
        const float e1 = trsel<XOR1, XOR1>(d[2], d[3], c & 1);
        sb[be] = trsel<XOR2, XOR2>(e0, e1, c & 2);
      }
      const float f0 = trsel<SHL4, SHR4>(sb[0], sb[1], c & 4);
      const float f1 = trsel<SHL4, SHR4>(sb[2], sb[3], c & 4);
      const float dotv = trsel<ROR8, ROR8>(f0, f1, c & 8);
      sc = rbf(rbf(dotv) * 0.08838834764831845f);
      const float mw = x32_max(x16_max(row_max(sc)));
      const float ew = __builtin_amdgcn_exp2f((sc - mw) * LOG2E);
      const float lw = x32_sum(x16_sum(row_sum(ew)));
      if (lane == 0) LDS_ST64(st + 2 * sw, (unsigned long long)__float_as_uint(mw) | ((unsigned long long)__float_as_uint(lw) << 32));
    };
    unsigned cp0 = 0, cp1 = 0;
    if (sw == CODEPF_W && (CODEPF == 1 || (blk >> 3) < 4)) {
      const __amdgpu_buffer_rsrc_t rc = RSRC((void*)(pc_entry & ~255ull), 15360);
      cp0 = __builtin_amdgcn_raw_buffer_load_b32(rc, lane * 128, 0, CODEPF_AUX);
      cp1 = __builtin_amdgcn_raw_buffer_load_b32(rc, lane * 128, 8192, CODEPF_AUX);
    }
    if (PRIO) __builtin_amdgcn_s_setprio(PRIO == 3 ? 3 : 2);
    wg_barrier();
#pragma unroll
    for (int i = 0; i < 24; ++i) {
      if (PRIO == 1 && i == 16) __builtin_amdgcn_s_setprio(1);
      if (PRIO == 3 && i == 16) __builtin_amdgcn_s_setprio(2);
      if (PRIO == 2 && i == 8) __builtin_amdgcn_s_setprio(1);
      if (PRIO == 2 && i == 16) __builtin_amdgcn_s_setprio(0);
      if (i >= WK) VMWAIT(WK - 1);
      if (i < 16) K[i] = LD128(rk, voff, KVOFF(i), AUX_NT); else V[i - 16] = LD128(rv, voff, KVOFF(i - 16), AUX_NT);
      SBAR();
      if (i == WK - 1) STAMP(1);
    }
    STAMP(4);
    const bool early = __builtin_amdgcn_readfirstlane(LDS_LD32(flg + 1)) != 0u;
    if (early) { scores(); STAMP(5); }
    SBAR();
    if (PRIO == 1) __builtin_amdgcn_s_setprio(0);
    if (PRIO == 3) __builtin_amdgcn_s_setprio(1);
#pragma unroll
    for (int t = 8; t < 16; ++t) {
      VMWAIT(WK2 - 1);
      V[t] = LD128(rv, voff, KVOFF(t), AUX_NT);
      SBAR();
    }
    if (!early) {
      for (unsigned spins = 0; spins < (SPIN_CAP << 4); ++spins) {
        if (__builtin_amdgcn_readfirstlane(LDS_LD32(flg + 1)) != 0u) break;
        __builtin_amdgcn_s_sleep(1);
      }
      scores();
      STAMP(6);
    }
    SBAR();
    {
      if (PRIO == 3) __builtin_amdgcn_s_setprio(0);
      const unsigned vo4 = c < 10 ? (unsigned)(r * 4 * 1280 + c * 16) : OOB;
#pragma unroll
      for (int i = 0; i < 4; ++i) { VMWAIT(WK2 - 1); WO[i] = LD128(ro, vo4, (16 * sw + i) * 1280, AUX_NT); SBAR(); }
    }
    {
      const unsigned vo2 = c < 10 ? (unsigned)(r * 2 * 1280 + c * 16) : OOB;
#pragma unroll
      for (int i = 0; i < 2; ++i) { VMWAIT(WK2 - 1); W2[i] = LD128(r2, vo2, (8 * sw + i) * 1280, AUX_NT); SBAR(); }
    }
    asm volatile("" :: "v"(cp0), "v"(cp1));
    STAMP(7);
    unsigned pp;
    {
      unsigned long long t_ = LDS_LD64(st + 2 * (lane & 7));
      for (unsigned spins = 0; !__all((unsigned)(t_ >> 32) != 0u) && spins < (SPIN_CAP << 4); ++spins) { __builtin_amdgcn_s_sleep(1); t_ = LDS_LD64(st + 2 * (lane & 7)); }
      float M = __uint_as_float((unsigned)t_);
      M = fmaxf(M, dppf<XOR1>(M)); M = fmaxf(M, dppf<XOR2>(M)); M = fmaxf(M, dppf<XOR7>(M));
      const float pv = __builtin_amdgcn_exp2f((sc - M) * LOG2E);
      pp = pk_bf16(pv, pv);
    }
    const unsigned tag = __builtin_amdgcn_readfirstlane(LDS_LD32(flg)) ^ 0x80000000u;
    f32x4 oacc = {0.f, 0.f, 0.f, 0.f};
#define PVSTEP(t) oacc = mfma(mask & dppu<BCAST + (t)>(pp), V[t], oacc);
    PVSTEP(0) PVSTEP(1) PVSTEP(2) PVSTEP(3) PVSTEP(4) PVSTEP(5) PVSTEP(6) PVSTEP(7)
    PVSTEP(8) PVSTEP(9) PVSTEP(10) PVSTEP(11) PVSTEP(12) PVSTEP(13) PVSTEP(14) PVSTEP(15)
#undef PVSTEP
    red[sw * 64 + lane] = oacc;
    asm volatile("s_waitcnt lgkmcnt(0)" ::: "memory");
    if (lane == 0) LDS_ADD(flg + 4);
    STAMP(8);
    if (ffn) {
      for (unsigned spins = 0; spins < (SPIN_CAP << 4); ++spins) {
        if (__builtin_amdgcn_readfirstlane(LDS_LD32(flg + 2)) != 0u) break;
        __builtin_amdgcn_s_sleep(1);
      }
      CBAR();
      const u32x2 hv = *(const u32x2*)(hs + (c >> 3) * 64 + 8 * sw + 2 * r);
      f32x4 facc = {0.f, 0.f, 0.f, 0.f};
      facc = mfma(mask & hv[0], W2[0], facc);
      facc = mfma(mask & hv[1], W2[1], facc);
      red[(8 + sw) * 64 + lane] = facc;
      asm volatile("s_waitcnt lgkmcnt(0)" ::: "memory");
      if (lane == 0) LDS_ADD(flg + 3);
    }
    VMWAIT(0);
    STAMP(15);
    wg_barrier();
    STAMP(10);
    {
      const u32x4 ov = *(const u32x4*)(os + (c >> 3) * 128 + 16 * sw + 4 * r);
      f32x4 y = {0.f, 0.f, 0.f, 0.f}, y2 = {0.f, 0.f, 0.f, 0.f};
      y = mfma(mask & ov[0], WO[0], y);
      y2 = mfma(mask & ov[1], WO[1], y2);
      y = mfma(mask & ov[2], WO[2], y);
      y2 = mfma(mask & ov[3], WO[3], y2);
      red[(16 + sw) * 64 + lane] = y + y2;
    }
    STAMP(11);
    wg_barrier();
    if (sw == 0) sum_publish(red + 1024, WS_YBOX, 8, tag);
  } else {
    __builtin_amdgcn_s_setprio(3);
    const bool isq = (w == 0);
    const unsigned epoch = __builtin_amdgcn_raw_buffer_load_b32(rws, 0, WS_EPOCH, AUX_SC1);
    unsigned pt = 0;
    u32x2 xs[5];
#pragma unroll
    for (int i = 0; i < 5; ++i) xs[i] = __builtin_amdgcn_raw_buffer_load_b64(rx, (lane >> 5) * 1280 + (lane & 31) * 8, i * 256, 0);
    const int row0 = isq ? 160 * (j >> 1) : 80 * j;
    const int nrow = isq ? 160 : 80;
    unsigned short xr[5];
#pragma unroll
    for (int i = 0; i < 5; ++i) xr[i] = __builtin_amdgcn_raw_buffer_load_b16(rx, (lane >> 5) * 1280 + (lane & 31) * 2, (row0 + 32 * i) * 2, 0);
    u32x4 wa[20];
    {
      const __amdgpu_buffer_rsrc_t rw = isq ? RSRC(wq + h * 128 + (j & 1) * 64, 640 * 4096 * 2 - h * 256 - (j & 1) * 128) : RSRC(w1 + h * 64, ffn ? 640 * 1728 * 2 - h * 128 : 0);
      const int rstride = isq ? 8192 : 3456;
      const unsigned vo = (unsigned)((lane >> 3) * rstride + (lane & 7) * 16);
#pragma unroll
      for (int i = 0; i < 10; ++i) wa[i] = LD128(rw, vo, (row0 + 8 * i) * rstride, AUX_NT);
      if (isq) {
#pragma unroll
        for (int i = 10; i < 20; ++i) wa[i] = LD128(rw, vo, (row0 + 8 * i) * rstride, AUX_NT);
      }
    }
    if (isq && lane < 28) LDS_ST32(st + lane, 0u);
    STAMP(1);
    wg_barrier();
    float xf[20];
#pragma unroll
    for (int i = 0; i < 5; ++i) {
      xf[4 * i + 0] = __uint_as_float(xs[i][0] << 16); xf[4 * i + 1] = __uint_as_float(xs[i][0] & 0xffff0000u);
      xf[4 * i + 2] = __uint_as_float(xs[i][1] << 16); xf[4 * i + 3] = __uint_as_float(xs[i][1] & 0xffff0000u);
    }
    float sum = 0.f;
#pragma unroll
    for (int i = 0; i < 20; ++i) sum += xf[i];
    sum = x16_sum(row_sum(sum));
    const float mean = sum * (1.f / 640.f);
    float vs = 0.f;
#pragma unroll
    for (int i = 0; i < 20; ++i) { const float d = xf[i] - mean; vs = fmaf(d, d, vs); }
    vs = x16_sum(row_sum(vs));
    const float mb = rbf(mean), den = rbf(sqrtf(rbf(rbf(vs * (1.f / 640.f)) + 1e-5f)));
    unsigned* xw_ = xl + w * 320;
#pragma unroll
    for (int i = 0; i < 5; ++i) {
      const int row = (lane & 31) + 32 * i;
      const float xn = rbf(__uint_as_float((unsigned)xr[i] << 16) - mb) / den;
      if (row < nrow) xw_[(lane >> 5) * 160 + ((row >> 1) & 3) * 40 + (row >> 3) * 2 + (row & 1)] = pk_bf16(xn, xn);
    }
    const unsigned tag = __builtin_amdgcn_readfirstlane(epoch) + 0x9E3779B9u, tagB = tag ^ 0x80000000u;
    if (isq && lane == 0) LDS_ST32(flg, tag);
    asm volatile("s_waitcnt lgkmcnt(0)" ::: "memory");
    asm volatile("" :: "v"(pt));
    STAMP(2);
    f32x4 acc0 = {0.f, 0.f, 0.f, 0.f}, acc1 = {0.f, 0.f, 0.f, 0.f};
    const unsigned* xrd = xw_ + (c >> 3) * 160 + r * 40;
#pragma unroll
    for (int i = 0; i < 10; i += 2) {
      const u32x4 xa = *(const u32x4*)(xrd + 2 * i);
      acc0 = mfma(mask & xa[0], wa[i], acc0); acc1 = mfma(mask & xa[1], wa[i], acc1);
      acc0 = mfma(mask & xa[2], wa[i + 1], acc0); acc1 = mfma(mask & xa[3], wa[i + 1], acc1);
    }
    if (isq) {
#pragma unroll
      for (int i = 10; i < 20; i += 2) {
        const u32x4 xa = *(const u32x4*)(xrd + 2 * i);
        acc0 = mfma(mask & xa[0], wa[i], acc0); acc1 = mfma(mask & xa[1], wa[i], acc1);
        acc0 = mfma(mask & xa[2], wa[i + 1], acc0); acc1 = mfma(mask & xa[3], wa[i + 1], acc1);
      }
    }
    if (isq || ffn) {
      f32x4 v;
#pragma unroll
      for (int i = 0; i < 4; ++i) { const float t_ = c < 8 ? acc0[i] : acc1[i]; v[i] = t_ + dppf<ROR8>(t_); }
      const u32x4 pub = gran2(c < 8 ? v[0] : v[2], c < 8 ? v[1] : v[3], tag);
      const unsigned cu = (unsigned)(h * 8 + j);
      if (isq) {
        if (X0MODE != 1 && X0MODE != 3) ST128(pub, rws, cu * 1024 + lane * 16, WS_Q1, AUX_SC1);
        STAMP(3);
        POLLDELAY();
        const unsigned goff = (unsigned)((h * 8 + (lane >> 5)) * 1024 + (b * 32 + (lane & 31)) * 16);
        u32x4 G[4];
#pragma unroll
        for (int p = 0; p < 4; ++p) G[p] = (X0MODE == 1 || X0MODE == 2) ? pub : LD128(rws, goff, WS_Q1 + p * 2048, AUX_POLL);
        for (unsigned spins = 0; X0MODE == 0 || X0MODE == 4; ++spins) {
          bool allok = true;
          CBAR();
#pragma unroll
          for (int p = 0; p < 4; ++p) if (!__all(gok(G[p], tag))) { allok = false; G[p] = LD128(rws, goff, WS_Q1 + p * 2048, AUX_POLL); }
          if (spins == 0) STAMP(14);
          if (allok || spins > SPIN_CAP) break;
        }
        const float a0 = (__uint_as_float(G[0][0]) + __uint_as_float(G[1][0])) + (__uint_as_float(G[2][0]) + __uint_as_float(G[3][0]));
        const float a1 = (__uint_as_float(G[0][2]) + __uint_as_float(G[1][2])) + (__uint_as_float(G[2][2]) + __uint_as_float(G[3][2]));
        qs[(lane >> 5) * 32 + (c & 7) * 4 + (r & 1) * 2 + (c >> 3)] = pk_bf16(a0, a1);
        ST128(gran2(0.f, 0.f, tag), rws, lane == 0 ? cu * 128 + 16 : OOB, WS_SBOX, AUX_SC1);
      } else {
        if (X0MODE == 0 || X0MODE == 2) ST128(pub, rws, (unsigned)((h * 8 + (c & 7)) * 1280 + j * 128 + (c >> 3) * 64 + r * 16), WS_H1, AUX_SC1);
        STAMP(3);
        POLLDELAY(); POLLDELAY();
        const unsigned goff = cu * 1280 + lane * 16;
        u32x4 G0 = (X0MODE == 0 || X0MODE == 3) ? LD128(rws, goff, WS_H1, AUX_POLL) : pub;
        for (unsigned spins = 0; X0MODE == 0 && !__all(gok(G0, tag)) && spins < SPIN_CAP; ++spins) { CBAR(); G0 = LD128(rws, goff, WS_H1, AUX_POLL); }
        STAMP(14);
        float a0 = __uint_as_float(G0[0]), a1 = __uint_as_float(G0[2]);
        a0 += dppf<ROR8>(a0); a1 += dppf<ROR8>(a1);
        a0 = fmaxf(x32_sum(x16_sum(a0)), 0.f); a1 = fmaxf(x32_sum(x16_sum(a1)), 0.f);
        u32x2 g2; g2[0] = pk_bf16(a0, a1); g2[1] = tag;
        if (X0MODE == 0 || X0MODE == 2) __builtin_amdgcn_raw_buffer_store_b64(g2, rws, lane < 8 ? cu * 128 + lane * 8 : OOB, WS_H2, AUX_SC1);
        const unsigned g2off = (unsigned)((h * 8 + (lane >> 3)) * 128 + (lane & 7) * 8);
        POLLDELAY(); POLLDELAY(); POLLDELAY();
        u32x2 q2 = (X0MODE == 0 || X0MODE == 3) ? __builtin_amdgcn_raw_buffer_load_b64(rws, g2off, WS_H2, AUX_POLL) : g2;
        for (unsigned spins = 0; X0MODE == 0 && !__all(q2[1] == tag) && spins < SPIN_CAP; ++spins) { CBAR(); q2 = __builtin_amdgcn_raw_buffer_load_b64(rws, g2off, WS_H2, AUX_POLL); }
        u32x2 hv; hv[0] = (q2[0] & 0xffffu) | (q2[0] << 16); hv[1] = (q2[0] >> 16) | (q2[0] & 0xffff0000u);
        *(u32x2*)(hs + ((lane >> 1) & 1) * 64 + (lane >> 3) * 8 + (lane & 1) * 4 + ((lane >> 2) & 1) * 2) = hv;
      }
      asm volatile("s_waitcnt lgkmcnt(0)" ::: "memory");
      if (lane == 0) LDS_ST32(flg + 1 + w, 1u);
    }
    STAMP(4);
    const unsigned soff = WS_SBOX + (unsigned)((h * 8 + (lane & 7)) * 128 + (lane & 8) * 2);
    u32x4 gs;
    if (isq) {
      __builtin_amdgcn_s_sleep(ACKDELAY);
      gs = LD128(rws, soff, 0, AUX_POLL);
      u32x2 ml;
      for (unsigned spins = 0;; ++spins) {
        { const unsigned long long t_ = LDS_LD64(st + 2 * (lane & 7)); ml[0] = (unsigned)t_; ml[1] = (unsigned)(t_ >> 32); }
        if (__all(ml[1] != 0u) || spins > (SPIN_CAP << 4)) break;
        __builtin_amdgcn_s_sleep(2);
      }
      const float m_ = __uint_as_float(ml[0]), l_ = __uint_as_float(ml[1]);
      float M = fmaxf(m_, dppf<XOR1>(m_)); M = fmaxf(M, dppf<XOR2>(M)); M = fmaxf(M, dppf<XOR7>(M));
      const float f_ = __builtin_amdgcn_exp2f((m_ - M) * LOG2E);
      float L = f_ * l_; L += dppf<XOR1>(L); L += dppf<XOR2>(L); L += dppf<XOR7>(L);
      ST128(gran2(M, L, tag), rws, lane == 0 ? WS_SBOX + (unsigned)((h * 8 + j) * 128) : OOB, 0, AUX_SC1);
      STAMP(5);
      STAMP(7);
    }
    if (isq) {
      if (!__all(gok(gs, tag) | !(lane & 8))) {
        for (unsigned spins = 0; spins < SPIN_CAP; ++spins) {
          CBAR();
          gs = LD128(rws, soff, 0, AUX_POLL);
          if (__all(gok(gs, tag) | !(lane & 8))) break;
        }
      }
      STAMP(9);
      CBAR();
      if (STATS_EARLY) { __builtin_amdgcn_s_sleep(STATS_EARLY); gs = LD128(rws, soff, 0, AUX_POLL); }
      for (unsigned spins = 0; spins < (SPIN_CAP << 6); ++spins) { if (__builtin_amdgcn_readfirstlane(LDS_LD32(flg + 4)) == 8u) break; }
      CBAR();
    }
    STAMP(8);
    if (!isq && ffn) {
      for (unsigned spins = 0; spins < (SPIN_CAP << 4); ++spins) { if (__builtin_amdgcn_readfirstlane(LDS_LD32(flg + 3)) == 8u) break; __builtin_amdgcn_s_sleep(1); }
      CBAR();
      sum_publish(red + 512, WS_FBOX, 0, tag);
      sum_publish(red + 512, WS_FBOX, 8, tag);
    }
    if (isq) {
      const unsigned ooff = (unsigned)(h * 8 * 1024 + lane * 16);
      const f32x2 own = sum8((const float*)red + (((lane >> 1) & 1) * 16 + (lane >> 2)) * 4 + 2 * (lane & 1));
      const u32x4 ownG = gran2(own[0], own[1], tagB);
      ST128(ownG, rws, ooff, WS_OBOX + j * 1024, AUX_SC1);
      STAMP(6);
      if (!STATS_EARLY) gs = LD128(rws, soff, 0, AUX_POLL);
      if (H1DELAY) __builtin_amdgcn_s_sleep(H1DELAY);
      u32x4 G[8];
#pragma unroll
      for (int p = 0; p < 8; ++p) G[p] = LD128(rws, p == j ? OOB : ooff, WS_OBOX + p * 1024, AUX_POLL);
#pragma unroll
      for (int p = 0; p < 8; ++p) if (p == j) G[p] = ownG;
      for (unsigned spins = 0; !__all(gok(gs, tag)) && spins < SPIN_CAP; ++spins) { CBAR(); __builtin_amdgcn_s_sleep(2); gs = LD128(rws, soff, 0, AUX_POLL); }
      float wp[8];
      {
        const float ms = __uint_as_float(gs[0]), ls = __uint_as_float(gs[2]);
        float Mg = fmaxf(ms, dppf<XOR1>(ms)); Mg = fmaxf(Mg, dppf<XOR2>(Mg));
        const float e_ = __builtin_amdgcn_exp2f((ms - Mg) * LOG2E);
        float Lg = e_ * ls; Lg += dppf<XOR1>(Lg); Lg += dppf<XOR2>(Lg);
        const float wgt = e_ / Lg;
#pragma unroll
        for (int p = 0; p < 8; ++p) wp[p] = __uint_as_float(__builtin_amdgcn_readlane(__float_as_uint(wgt), p));
      }
      for (unsigned spins = 0;; ++spins) {
        bool allok = true;
        CBAR();
#pragma unroll
        for (int p = 0; p < 8; ++p) {
          if (!__all(gok(G[p], tagB))) { if (allok && HSLEEP) __builtin_amdgcn_s_sleep(HSLEEP); allok = false; G[p] = LD128(rws, ooff, WS_OBOX + p * 1024, AUX_POLL); }
        }
        if (allok || spins > SPIN_CAP) break;
      }
      STAMP(10);
#pragma unroll
      for (int bb = 0; bb < 2; ++bb) {
        float o0 = 0.f, o1 = 0.f;
#pragma unroll
        for (int sp = 0; sp < 4; ++sp) { o0 = fmaf(wp[4 * bb + sp], __uint_as_float(G[4 * bb + sp][0]), o0); o1 = fmaf(wp[4 * bb + sp], __uint_as_float(G[4 * bb + sp][2]), o1); }
        u32x2 ov; ov[0] = pk_bf16(o0, o0); ov[1] = pk_bf16(o1, o1);
        *(u32x2*)(os + bb * 128 + 2 * lane) = ov;
      }
    }
    wg_barrier();
    const int e = lane & 7, ob = e >> 2, ocol = j * 80 + 8 * h + 4 * ((e >> 1) & 1) + 2 * (e & 1);
    const bool fin = !isq && h < 10;
    const unsigned yo = (unsigned)(((lane >> 3) * 8 + j) * 1280 + h * 128 + e * 16);
    const bool f3v = (lane >> 3) < 3;
    unsigned xw = 0; float fa = 0.f, fb = 0.f;
    if (fin) {
      xw = __builtin_amdgcn_raw_buffer_load_b32(rx, (ob * 640 + ocol) * 2, 0, 0);
    }
    wg_barrier();
    STAMP(11);
    if (isq) {
      sum_publish(red + 1024, WS_YBOX, 0, tagB);
      STAMP(12);
    } else if (fin) {
      u32x4 g[4], y[4];
#pragma unroll
      for (int i = 0; i < 4; ++i) g[i] = LD128(rws, (i < 3 || f3v) ? yo : OOB, WS_FBOX + i * 64 * 1280, AUX_POLL);
#pragma unroll
      for (int i = 0; i < 4; ++i) y[i] = LD128(rws, yo, WS_YBOX + i * 64 * 1280, AUX_POLL);
      for (unsigned spins = 0;; ++spins) {
        bool allok = true;
        CBAR();
#pragma unroll
        for (int i = 0; i < 4; ++i) if (!__all(gok(g[i], tag) | (i == 3 && !f3v))) { allok = false; g[i] = LD128(rws, (i < 3 || f3v) ? yo : OOB, WS_FBOX + i * 64 * 1280, AUX_POLL); }
        if (allok || spins > SPIN_CAP) break;
      }
      fa = (__uint_as_float(g[0][0]) + __uint_as_float(g[1][0])) + (__uint_as_float(g[2][0]) + __uint_as_float(g[3][0]));
      fb = (__uint_as_float(g[0][2]) + __uint_as_float(g[1][2])) + (__uint_as_float(g[2][2]) + __uint_as_float(g[3][2]));
      fa += dppf<ROR8>(fa); fb += dppf<ROR8>(fb);
      fa = rbf(x32_sum(x16_sum(fa))); fb = rbf(x32_sum(x16_sum(fb)));
      const float ga = rbf(fa / (1.f + __expf(-fa))), gb = rbf(fb / (1.f + __expf(-fb)));
      STAMP(10);
      for (unsigned spins = 0;; ++spins) {
        bool allok = true;
        CBAR();
#pragma unroll
        for (int i = 0; i < 4; ++i) if (!__all(gok(y[i], tagB))) { allok = false; y[i] = LD128(rws, yo, WS_YBOX + i * 64 * 1280, AUX_POLL); }
        if (allok || spins > SPIN_CAP) break;
      }
      STAMP(13);
      float ya = (__uint_as_float(y[0][0]) + __uint_as_float(y[1][0])) + (__uint_as_float(y[2][0]) + __uint_as_float(y[3][0]));
      float yb = (__uint_as_float(y[0][2]) + __uint_as_float(y[1][2])) + (__uint_as_float(y[2][2]) + __uint_as_float(y[3][2]));
      ya += dppf<ROR8>(ya); yb += dppf<ROR8>(yb);
      ya = rbf(x32_sum(x16_sum(ya))); yb = rbf(x32_sum(x16_sum(yb)));
      const float oa = __uint_as_float(xw << 16) + rbf(ya * ga), obv = __uint_as_float(xw & 0xffff0000u) + rbf(yb * gb);
      __builtin_amdgcn_raw_buffer_store_b32(pk_bf16(oa, obv), RSRC(out, 2560), lane < 8 ? (unsigned)((ob * 640 + ocol) * 2) : OOB, 0, 0);
      if (blk == 0 && lane == 0) __builtin_amdgcn_raw_buffer_store_b32(tag, rws, 0, WS_EPOCH, AUX_SC1);
      STAMP(12);
    }
  }
  if (w == 1 && h < 10) { asm volatile("s_waitcnt vmcnt(0)" ::: "memory"); unsigned t_ = (unsigned)__builtin_amdgcn_s_memrealtime(); unsigned d_ = (t_ - tstart_) * 10u; if (d_ > 60000u) d_ = 60000u; while ((unsigned)__builtin_amdgcn_s_memrealtime() - t_ < d_) __builtin_amdgcn_s_sleep(8); }
}

extern "C" void kernel_launch(void* const* d_in, const int* in_sizes, int n_in, void* d_out, int out_size, void* d_ws, size_t ws_size, hipStream_t stream) {
  constexpr int LDS = 96 * 1024;
  (void)hipFuncSetAttribute((const void*)fused_decode, hipFuncAttributeMaxDynamicSharedMemorySize, LDS);
  fused_decode<<<256, 640, LDS, stream>>>((const unsigned short*)d_in[0], (const unsigned short*)d_in[1], (const unsigned short*)d_in[2],
      (const unsigned short*)d_in[3], (const unsigned short*)d_in[4], (const unsigned short*)d_in[6], (const unsigned short*)d_in[7],
      (unsigned short*)d_out, (unsigned char*)d_ws + (WSEND ? ((ws_size - WS_END) & ~(size_t)4095) : 0));
}
```

```cpp
#define PRIO 3
#define WK 24
#define WK2 16
#define WSEND 0
#define HSLEEP 0
#define H1DELAY 0
#define PRETOUCH 0
#define CODEPF 2
#define PDELAY 32
#define STAUX 17
#define POLLAUX 17
#define REDW_ 1
#include <hip/hip_runtime.h>
#include <hip/hip_bf16.h>
#include <stdint.h>

#define STAMP(i) do {} while (0)
#define STAMP_DECL
#define STAMP_FLUSH() do {} while (0)

typedef unsigned u32x4 __attribute__((ext_vector_type(4)));
typedef unsigned u32x2 __attribute__((ext_vector_type(2)));
typedef float f32x4 __attribute__((ext_vector_type(4)));
typedef float f32x2 __attribute__((ext_vector_type(2)));
typedef __bf16 bf16x8 __attribute__((ext_vector_type(8)));
typedef __bf16 bf16x2 __attribute__((ext_vector_type(2)));

constexpr int AUX_NT = 2, AUX_SC1 = STAUX, AUX_POLL = POLLAUX;
constexpr unsigned OOB = 0x7ffffff0u;
constexpr unsigned WS_EPOCH = 0;
constexpr unsigned WS_Q1 = 4096;
constexpr unsigned WS_H1 = WS_Q1 + 256 * 1024;
constexpr unsigned WS_H2 = WS_H1 + 256 * 1280;
constexpr unsigned WS_SBOX = WS_H2 + 256 * 128;
constexpr unsigned WS_FBOX = WS_SBOX + 256 * 128;
constexpr unsigned WS_END = WS_FBOX + 256 * 1280;
constexpr unsigned WS_OBOX = WS_Q1, WS_YBOX = WS_H1;
constexpr unsigned SPIN_CAP = 1u << 16;

#define RSRC(p, n) __builtin_amdgcn_make_buffer_rsrc((void*)(p), 0, (n), 0x00020000)
#define LD128(rs, vo, so, aux) __builtin_amdgcn_raw_buffer_load_b128((rs), (vo), (so), (aux))
#define ST128(v, rs, vo, so, aux) __builtin_amdgcn_raw_buffer_store_b128((v), (rs), (vo), (so), (aux))
#define SBAR() __builtin_amdgcn_sched_barrier(0)
#define LAS __attribute__((address_space(3)))
#define LDS_LD32(p) __hip_atomic_load((LAS unsigned*)(p), __ATOMIC_RELAXED, __HIP_MEMORY_SCOPE_WORKGROUP)
#define LDS_ST32(p, v) __hip_atomic_store((LAS unsigned*)(p), (v), __ATOMIC_RELAXED, __HIP_MEMORY_SCOPE_WORKGROUP)
#define LDS_LD64(p) __hip_atomic_load((LAS unsigned long long*)(p), __ATOMIC_RELAXED, __HIP_MEMORY_SCOPE_WORKGROUP)
#define LDS_ST64(p, v) __hip_atomic_store((LAS unsigned long long*)(p), (v), __ATOMIC_RELAXED, __HIP_MEMORY_SCOPE_WORKGROUP)
#define LDS_ADD(p) asm volatile("ds_add_u32 %0, %1" :: "v"((unsigned)(size_t)(LAS unsigned*)(p)), "v"(1u) : "memory")
#define CBAR() asm volatile("" ::: "memory")

template <int CTRL> __device__ __forceinline__ unsigned dppu(unsigned x) { return (unsigned)__builtin_amdgcn_mov_dpp((int)x, CTRL, 0xf, 0xf, true); }
template <int CTRL> __device__ __forceinline__ float dppf(float x) { return __uint_as_float(dppu<CTRL>(__float_as_uint(x))); }
constexpr int XOR1 = 0xB1, XOR2 = 0x4E, XOR7 = 0x141, ROR8 = 0x128, ROR4 = 0x124, SHL4 = 0x104, SHR4 = 0x114, BCAST = 0x150;

__device__ __forceinline__ float dot2(unsigned k, unsigned q, float acc) {
  return __builtin_amdgcn_fdot2_f32_bf16(__builtin_bit_cast(bf16x2, k), __builtin_bit_cast(bf16x2, q), acc, false);
}
__device__ __forceinline__ unsigned pk_bf16(float lo, float hi) {
  bf16x2 v; v[0] = (__bf16)lo; v[1] = (__bf16)hi; return __builtin_bit_cast(unsigned, v);
}
__device__ __forceinline__ float rbf(float f) { return __uint_as_float(pk_bf16(f, f) & 0xffff0000u); }
__device__ __forceinline__ f32x4 mfma(u32x4 a, u32x4 b, f32x4 c) {
  return __builtin_amdgcn_mfma_f32_16x16x32_bf16(__builtin_bit_cast(bf16x8, a), __builtin_bit_cast(bf16x8, b), c, 0, 0, 0);
}
__device__ __forceinline__ float x16_sum(float x) { auto s = __builtin_amdgcn_permlane16_swap(__float_as_uint(x), __float_as_uint(x), false, false); return __uint_as_float(s[0]) + __uint_as_float(s[1]); }
__device__ __forceinline__ float x32_sum(float x) { auto s = __builtin_amdgcn_permlane32_swap(__float_as_uint(x), __float_as_uint(x), false, false); return __uint_as_float(s[0]) + __uint_as_float(s[1]); }
__device__ __forceinline__ float x16_max(float x) { auto s = __builtin_amdgcn_permlane16_swap(__float_as_uint(x), __float_as_uint(x), false, false); return fmaxf(__uint_as_float(s[0]), __uint_as_float(s[1])); }
__device__ __forceinline__ float x32_max(float x) { auto s = __builtin_amdgcn_permlane32_swap(__float_as_uint(x), __float_as_uint(x), false, false); return fmaxf(__uint_as_float(s[0]), __uint_as_float(s[1])); }
__device__ __forceinline__ float row_sum(float x) { x += dppf<XOR1>(x); x += dppf<XOR2>(x); x += dppf<ROR4>(x); x += dppf<ROR8>(x); return x; }
__device__ __forceinline__ float row_max(float x) { x = fmaxf(x, dppf<XOR1>(x)); x = fmaxf(x, dppf<XOR2>(x)); x = fmaxf(x, dppf<ROR4>(x)); x = fmaxf(x, dppf<ROR8>(x)); return x; }
template <int C0, int C1> __device__ __forceinline__ float trsel(float lo, float hi, bool bit) {
  const float t0 = lo + dppf<C0>(lo), t1 = hi + dppf<C1>(hi);
  return bit ? t1 : t0;
}
__device__ __forceinline__ bool gok(u32x4 g, unsigned tag) { return (g[1] == tag) & (g[3] == tag); }
__device__ __forceinline__ u32x4 gran2(float a, float b, unsigned tag) { u32x4 g; g[0] = __float_as_uint(a); g[1] = tag; g[2] = __float_as_uint(b); g[3] = tag; return g; }
__device__ __forceinline__ void wg_barrier() { asm volatile("s_waitcnt lgkmcnt(0)\n\ts_barrier" ::: "memory"); }

constexpr float LOG2E = 1.4426950408889634f;

#define POLLDELAY() do { if (PDELAY) __builtin_amdgcn_s_sleep(PDELAY); } while (0)
#define NPACE 16
#define MAPX 0
#define KVORD 0
#define ABL_NOX0 0
#define ABL_NOB0 0
#define OSHARED 1
#define ABL 0
#define X0MODE 0
#define ARGPIN 1
#define W2LATE 1
#define ACKDELAY 48
#define STATS_EARLY 0
#define CODEPF_W 7
#define CODEPF_AUX 0
#define CODE_BACK 0
#define VMWAIT(n) asm volatile("s_waitcnt vmcnt(%0)" :: "i"(n) : "memory")

__device__ __forceinline__ f32x2 sum8(const float* p) {
  f32x2 a[8];
#pragma unroll
  for (int i = 0; i < 8; ++i) a[i] = *(const f32x2*)(p + i * 256);
  return ((a[0] + a[1]) + (a[2] + a[3])) + ((a[4] + a[5]) + (a[6] + a[7]));
}

__global__ void __launch_bounds__(640, 3)
fused_decode(const unsigned short* x, const unsigned short* kc, const unsigned short* vc,
             const unsigned short* wq, const unsigned short* wo, const unsigned short* w1,
             unsigned char* ws, const unsigned short* w2, unsigned short* out) {
  unsigned long long pc_entry;
  asm volatile("s_getpc_b64 %0" : "=s"(pc_entry));
  asm volatile("" : "+s"(x), "+s"(kc), "+s"(vc), "+s"(wq), "+s"(wo), "+s"(w1), "+s"(ws));
  extern __shared__ __attribute__((aligned(16))) unsigned char smem[];
  f32x4* red = (f32x4*)smem;
  unsigned* qs = (unsigned*)(smem + 24576);
  unsigned* hs = (unsigned*)(smem + 24576 + 256);
  float* st = (float*)(smem + 24576 + 768);
  unsigned* flg = (unsigned*)(smem + 24576 + 832);
  float* gw = (float*)(smem + 24576 + 896);
  unsigned* wpl = (unsigned*)(smem + 24576 + 848);
  unsigned* os = (unsigned*)(smem + 24576 + 4096);
  unsigned* xl = (unsigned*)(smem + 24576 + 1024);
  const int blk = blockIdx.x, tid = threadIdx.x, lane = tid & 63, w = __builtin_amdgcn_readfirstlane(tid >> 6);
  STAMP_DECL
  const unsigned tstart_ = (unsigned)__builtin_amdgcn_s_memrealtime();
  STAMP(0);
  const int j = (blk >> 3) & 7, h = (blk & 7) + 8 * (blk >> 6), b = j >> 2, s = j & 3;
  const int r = lane >> 4, c = lane & 15;
  const bool ffn = h < 27;
  const __amdgpu_buffer_rsrc_t rws = RSRC(ws, WS_END);
  const __amdgpu_buffer_rsrc_t rx = RSRC(x, 2560);
  u32x4 mask;
#pragma unroll
  for (int i = 0; i < 4; ++i) mask[i] = (i == ((c & 7) >> 1)) ? ((c & 1) ? 0xffff0000u : 0xffffu) : 0u;
  auto sum_publish = [&](const f32x4* rr, unsigned box, int cbase, unsigned tag) {
    const int cr = cbase + (lane >> 3), e = lane & 7;
    const float* p = (const float*)rr + ((e >> 1) * 16 + (cr & 15)) * 4 + 2 * (e & 1);
    const f32x2 v = sum8(p);
    const float v0 = v[0], v1 = v[1];
    ST128(gran2(v0, v1, tag), rws, cr < 10 ? (unsigned)((h * 8 + j) * 1280 + cr * 128 + e * 16) : OOB, box, AUX_SC1);
  };

  if (w >= 2) {
    const int sw = w - 2;
    const size_t kvbase = ((size_t)(b * 32 + h) * 8192 + 6144 + s * 512) * 128;
    const __amdgpu_buffer_rsrc_t rk = RSRC(kc + kvbase, 131072), rv = RSRC(vc + kvbase, 131072);
    const __amdgpu_buffer_rsrc_t ro = RSRC(wo + (size_t)h * 128 * 640 + j * 80, 128 * 1280 - j * 160);
    const int rot = sw * 4 + ((blk * 37) & 127);
    const int voff = lane * 16;
#define KVOFF(t) ((((((t) >> 2) * 32 + ((t) & 3) + rot) & 127) << 10))
    u32x4 K[16], V[16], WO[4], W2[2];
    float sc = 0.f;
    auto scores = [&]() {
      CBAR();
      const u32x4 q4 = *(const u32x4*)(qs + 4 * c);
      float sb[4];
#pragma unroll
      for (int be = 0; be < 4; ++be) {
        float d[4];
#pragma unroll
        for (int tt = 0; tt < 4; ++tt) {
          const u32x4 kk = K[4 * be + tt];
          d[tt] = dot2(kk[3], q4[3], dot2(kk[2], q4[2], dot2(kk[1], q4[1], dot2(kk[0], q4[0], 0.f))));
        }
        const float e0 = trsel<XOR1, XOR1>(d[0], d[1], c & 1);
        const float e1 = trsel<XOR1, XOR1>(d[2], d[3], c & 1);
        sb[be] = trsel<XOR2, XOR2>(e0, e1, c & 2);
      }
      const float f0 = trsel<SHL4, SHR4>(sb[0], sb[1], c & 4);
      const float f1 = trsel<SHL4, SHR4>(sb[2], sb[3], c & 4);
      const float dotv = trsel<ROR8, ROR8>(f0, f1, c & 8);
      sc = rbf(rbf(dotv) * 0.08838834764831845f);
      const float mw = x32_max(x16_max(row_max(sc)));
      const float ew = __builtin_amdgcn_exp2f((sc - mw) * LOG2E);
      const float lw = x32_sum(x16_sum(row_sum(ew)));
      if (lane == 0) LDS_ST64(st + 2 * sw, (unsigned long long)__float_as_uint(mw) | ((unsigned long long)__float_as_uint(lw) << 32));
    };
    unsigned cp0 = 0, cp1 = 0;
    if (sw == CODEPF_W && (CODEPF == 1 || (blk >> 3) < 4)) {
      const __amdgpu_buffer_rsrc_t rc = RSRC((void*)(pc_entry & ~255ull), 15360);
      cp0 = __builtin_amdgcn_raw_buffer_load_b32(rc, lane * 128, 0, CODEPF_AUX);
      cp1 = __builtin_amdgcn_raw_buffer_load_b32(rc, lane * 128, 8192, CODEPF_AUX);
    }
    if (PRIO) __builtin_amdgcn_s_setprio(PRIO == 3 ? 3 : 2);
    wg_barrier();
#pragma unroll
    for (int i = 0; i < 24; ++i) {
      if (i == 8) asm volatile("" : "+s"(w2));
      if (PRIO == 1 && i == 16) __builtin_amdgcn_s_setprio(1);
      if (PRIO == 3 && i == 16) __builtin_amdgcn_s_setprio(2);
      if (PRIO == 2 && i == 8) __builtin_amdgcn_s_setprio(1);
      if (PRIO == 2 && i == 16) __builtin_amdgcn_s_setprio(0);
      if (i >= WK) VMWAIT(WK - 1);
      if (i < 16) K[i] = LD128(rk, voff, KVOFF(i), AUX_NT); else V[i - 16] = LD128(rv, voff, KVOFF(i - 16), AUX_NT);
      SBAR();
      if (i == WK - 1) STAMP(1);
    }
    STAMP(4);
    const bool early = __builtin_amdgcn_readfirstlane(LDS_LD32(flg + 1)) != 0u;
    if (early) { scores(); STAMP(5); }
    SBAR();
    if (PRIO == 1) __builtin_amdgcn_s_setprio(0);
    if (PRIO == 3) __builtin_amdgcn_s_setprio(1);
#pragma unroll
    for (int t = 8; t < 16; ++t) {
      VMWAIT(WK2 - 1);
      V[t] = LD128(rv, voff, KVOFF(t), AUX_NT);
      SBAR();
    }
    if (!early) {
      for (unsigned spins = 0; spins < (SPIN_CAP << 4); ++spins) {
        if (__builtin_amdgcn_readfirstlane(LDS_LD32(flg + 1)) != 0u) break;
        __builtin_amdgcn_s_sleep(1);
      }
      scores();
      STAMP(6);
    }
    SBAR();
    {
      if (PRIO == 3) __builtin_amdgcn_s_setprio(0);
      const unsigned vo4 = c < 10 ? (unsigned)(r * 4 * 1280 + c * 16) : OOB;
#pragma unroll
      for (int i = 0; i < 4; ++i) { VMWAIT(WK2 - 1); WO[i] = LD128(ro, vo4, (16 * sw + i) * 1280, AUX_NT); SBAR(); }
    }
    {
      const __amdgpu_buffer_rsrc_t r2 = RSRC(w2 + (size_t)h * 64 * 640 + j * 80, ffn ? 64 * 1280 - j * 160 : 0);
      const unsigned vo2 = c < 10 ? (unsigned)(r * 2 * 1280 + c * 16) : OOB;
#pragma unroll
      for (int i = 0; i < 2; ++i) { VMWAIT(WK2 - 1); W2[i] = LD128(r2, vo2, (8 * sw + i) * 1280, AUX_NT); SBAR(); }
    }
    asm volatile("" :: "v"(cp0), "v"(cp1));
    STAMP(7);
    unsigned pp;
    {
      unsigned long long t_ = LDS_LD64(st + 2 * (lane & 7));
      for (unsigned spins = 0; !__all((unsigned)(t_ >> 32) != 0u) && spins < (SPIN_CAP << 4); ++spins) { __builtin_amdgcn_s_sleep(1); t_ = LDS_LD64(st + 2 * (lane & 7)); }
      float M = __uint_as_float((unsigned)t_);
      M = fmaxf(M, dppf<XOR1>(M)); M = fmaxf(M, dppf<XOR2>(M)); M = fmaxf(M, dppf<XOR7>(M));
      const float pv = __builtin_amdgcn_exp2f((sc - M) * LOG2E);
      pp = pk_bf16(pv, pv);
    }
    const unsigned tag = __builtin_amdgcn_readfirstlane(LDS_LD32(flg)) ^ 0x80000000u;
    f32x4 oacc = {0.f, 0.f, 0.f, 0.f};
#define PVSTEP(t) oacc = mfma(mask & dppu<BCAST + (t)>(pp), V[t], oacc);
    PVSTEP(0) PVSTEP(1) PVSTEP(2) PVSTEP(3) PVSTEP(4) PVSTEP(5) PVSTEP(6) PVSTEP(7)
    PVSTEP(8) PVSTEP(9) PVSTEP(10) PVSTEP(11) PVSTEP(12) PVSTEP(13) PVSTEP(14) PVSTEP(15)
#undef PVSTEP
    red[sw * 64 + lane] = oacc;
    asm volatile("s_waitcnt lgkmcnt(0)" ::: "memory");
    if (lane == 0) LDS_ADD(flg + 4);
    STAMP(8);
    if (ffn) {
      for (unsigned spins = 0; spins < (SPIN_CAP << 4); ++spins) {
        if (__builtin_amdgcn_readfirstlane(LDS_LD32(flg + 2)) != 0u) break;
        __builtin_amdgcn_s_sleep(1);
      }
      CBAR();
      const u32x2 hv = *(const u32x2*)(hs + (c >> 3) * 64 + 8 * sw + 2 * r);
      f32x4 facc = {0.f, 0.f, 0.f, 0.f};
      facc = mfma(mask & hv[0], W2[0], facc);
      facc = mfma(mask & hv[1], W2[1], facc);
      red[(8 + sw) * 64 + lane] = facc;
      asm volatile("s_waitcnt lgkmcnt(0)" ::: "memory");
      if (lane == 0) LDS_ADD(flg + 3);
    }
    VMWAIT(0);
    STAMP(15);
    wg_barrier();
    STAMP(10);
    {
      const u32x4 ov = *(const u32x4*)(os + (c >> 3) * 128 + 16 * sw + 4 * r);
      f32x4 y = {0.f, 0.f, 0.f, 0.f}, y2 = {0.f, 0.f, 0.f, 0.f};
      y = mfma(mask & ov[0], WO[0], y);
      y2 = mfma(mask & ov[1], WO[1], y2);
      y = mfma(mask & ov[2], WO[2], y);
      y2 = mfma(mask & ov[3], WO[3], y2);
      red[(16 + sw) * 64 + lane] = y + y2;
    }
    STAMP(11);
    wg_barrier();
    if (sw == 0) sum_publish(red + 1024, WS_YBOX, 8, tag);
  } else {
    __builtin_amdgcn_s_setprio(3);
    const bool isq = (w == 0);
    const unsigned epoch = __builtin_amdgcn_raw_buffer_load_b32(rws, 0, WS_EPOCH, AUX_SC1);
    unsigned pt = 0;
    u32x2 xs[5];
#pragma unroll
    for (int i = 0; i < 5; ++i) xs[i] = __builtin_amdgcn_raw_buffer_load_b64(rx, (lane >> 5) * 1280 + (lane & 31) * 8, i * 256, 0);
    const int row0 = isq ? 160 * (j >> 1) : 80 * j;
    const int nrow = isq ? 160 : 80;
    unsigned short xr[5];
#pragma unroll
    for (int i = 0; i < 5; ++i) xr[i] = __builtin_amdgcn_raw_buffer_load_b16(rx, (lane >> 5) * 1280 + (lane & 31) * 2, (row0 + 32 * i) * 2, 0);
    u32x4 wa[20];
    {
      const __amdgpu_buffer_rsrc_t rw = isq ? RSRC(wq + h * 128 + (j & 1) * 64, 640 * 4096 * 2 - h * 256 - (j & 1) * 128) : RSRC(w1 + h * 64, ffn ? 640 * 1728 * 2 - h * 128 : 0);
      const int rstride = isq ? 8192 : 3456;
      const unsigned vo = (unsigned)((lane >> 3) * rstride + (lane & 7) * 16);
#pragma unroll
      for (int i = 0; i < 10; ++i) wa[i] = LD128(rw, vo, (row0 + 8 * i) * rstride, AUX_NT);
      if (isq) {
#pragma unroll
        for (int i = 10; i < 20; ++i) wa[i] = LD128(rw, vo, (row0 + 8 * i) * rstride, AUX_NT);
      }
    }
    if (isq && lane < 28) LDS_ST32(st + lane, 0u);
    STAMP(1);
    wg_barrier();
    asm volatile("" : "+s"(out));
    float xf[20];
#pragma unroll
    for (int i = 0; i < 5; ++i) {
      xf[4 * i + 0] = __uint_as_float(xs[i][0] << 16); xf[4 * i + 1] = __uint_as_float(xs[i][0] & 0xffff0000u);
      xf[4 * i + 2] = __uint_as_float(xs[i][1] << 16); xf[4 * i + 3] = __uint_as_float(xs[i][1] & 0xffff0000u);
    }
    float sum = 0.f;
#pragma unroll
    for (int i = 0; i < 20; ++i) sum += xf[i];
    sum = x16_sum(row_sum(sum));
    const float mean = sum * (1.f / 640.f);
    float vs = 0.f;
#pragma unroll
    for (int i = 0; i < 20; ++i) { const float d = xf[i] - mean; vs = fmaf(d, d, vs); }
    vs = x16_sum(row_sum(vs));
    const float mb = rbf(mean), den = rbf(sqrtf(rbf(rbf(vs * (1.f / 640.f)) + 1e-5f)));
    unsigned* xw_ = xl + w * 320;
#pragma unroll
    for (int i = 0; i < 5; ++i) {
      const int row = (lane & 31) + 32 * i;
      const float xn = rbf(__uint_as_float((unsigned)xr[i] << 16) - mb) / den;
      if (row < nrow) xw_[(lane >> 5) * 160 + ((row >> 1) & 3) * 40 + (row >> 3) * 2 + (row & 1)] = pk_bf16(xn, xn);
    }
    const unsigned tag = __builtin_amdgcn_readfirstlane(epoch) + 0x9E3779B9u, tagB = tag ^ 0x80000000u;
    if (isq && lane == 0) LDS_ST32(flg, tag);
    asm volatile("s_waitcnt lgkmcnt(0)" ::: "memory");
    asm volatile("" :: "v"(pt));
    STAMP(2);
    f32x4 acc0 = {0.f, 0.f, 0.f, 0.f}, acc1 = {0.f, 0.f, 0.f, 0.f};
    const unsigned* xrd = xw_ + (c >> 3) * 160 + r * 40;
#pragma unroll
    for (int i = 0; i < 10; i += 2) {
      const u32x4 xa = *(const u32x4*)(xrd + 2 * i);
      acc0 = mfma(mask & xa[0], wa[i], acc0); acc1 = mfma(mask & xa[1], wa[i], acc1);
      acc0 = mfma(mask & xa[2], wa[i + 1], acc0); acc1 = mfma(mask & xa[3], wa[i + 1], acc1);
    }
    if (isq) {
#pragma unroll
      for (int i = 10; i < 20; i += 2) {
        const u32x4 xa = *(const u32x4*)(xrd + 2 * i);
        acc0 = mfma(mask & xa[0], wa[i], acc0); acc1 = mfma(mask & xa[1], wa[i], acc1);
        acc0 = mfma(mask & xa[2], wa[i + 1], acc0); acc1 = mfma(mask & xa[3], wa[i + 1], acc1);
      }
    }
    if (isq || ffn) {
      f32x4 v;
#pragma unroll
      for (int i = 0; i < 4; ++i) { const float t_ = c < 8 ? acc0[i] : acc1[i]; v[i] = t_ + dppf<ROR8>(t_); }
      const u32x4 pub = gran2(c < 8 ? v[0] : v[2], c < 8 ? v[1] : v[3], tag);
      const unsigned cu = (unsigned)(h * 8 + j);
      if (isq) {
        if (X0MODE != 1 && X0MODE != 3) ST128(pub, rws, cu * 1024 + lane * 16, WS_Q1, AUX_SC1);
        STAMP(3);
        POLLDELAY();
        const unsigned goff = (unsigned)((h * 8 + (lane >> 5)) * 1024 + (b * 32 + (lane & 31)) * 16);
        u32x4 G[4];
#pragma unroll
        for (int p = 0; p < 4; ++p) G[p] = (X0MODE == 1 || X0MODE == 2) ? pub : LD128(rws, goff, WS_Q1 + p * 2048, AUX_POLL);
        for (unsigned spins = 0; X0MODE == 0 || X0MODE == 4; ++spins) {
          bool allok = true;
          CBAR();
#pragma unroll
          for (int p = 0; p < 4; ++p) if (!__all(gok(G[p], tag))) { allok = false; G[p] = LD128(rws, goff, WS_Q1 + p * 2048, AUX_POLL); }
          if (spins == 0) STAMP(14);
          if (allok || spins > SPIN_CAP) break;
        }
        const float a0 = (__uint_as_float(G[0][0]) + __uint_as_float(G[1][0])) + (__uint_as_float(G[2][0]) + __uint_as_float(G[3][0]));
        const float a1 = (__uint_as_float(G[0][2]) + __uint_as_float(G[1][2])) + (__uint_as_float(G[2][2]) + __uint_as_float(G[3][2]));
        qs[(lane >> 5) * 32 + (c & 7) * 4 + (r & 1) * 2 + (c >> 3)] = pk_bf16(a0, a1);
        ST128(gran2(0.f, 0.f, tag), rws, lane == 0 ? cu * 128 + 16 : OOB, WS_SBOX, AUX_SC1);
      } else {
        if (X0MODE == 0 || X0MODE == 2) ST128(pub, rws, (unsigned)((h * 8 + (c & 7)) * 1280 + j * 128 + (c >> 3) * 64 + r * 16), WS_H1, AUX_SC1);
        STAMP(3);
        POLLDELAY(); POLLDELAY();
        const unsigned goff = cu * 1280 + lane * 16;
        u32x4 G0 = (X0MODE == 0 || X0MODE == 3) ? LD128(rws, goff, WS_H1, AUX_POLL) : pub;
        for (unsigned spins = 0; X0MODE == 0 && !__all(gok(G0, tag)) && spins < SPIN_CAP; ++spins) { CBAR(); G0 = LD128(rws, goff, WS_H1, AUX_POLL); }
        STAMP(14);
        float a0 = __uint_as_float(G0[0]), a1 = __uint_as_float(G0[2]);
        a0 += dppf<ROR8>(a0); a1 += dppf<ROR8>(a1);
        a0 = fmaxf(x32_sum(x16_sum(a0)), 0.f); a1 = fmaxf(x32_sum(x16_sum(a1)), 0.f);
        u32x2 g2; g2[0] = pk_bf16(a0, a1); g2[1] = tag;
        if (X0MODE == 0 || X0MODE == 2) __builtin_amdgcn_raw_buffer_store_b64(g2, rws, lane < 8 ? cu * 128 + lane * 8 : OOB, WS_H2, AUX_SC1);
        const unsigned g2off = (unsigned)((h * 8 + (lane >> 3)) * 128 + (lane & 7) * 8);
        POLLDELAY(); POLLDELAY(); POLLDELAY();
        u32x2 q2 = (X0MODE == 0 || X0MODE == 3) ? __builtin_amdgcn_raw_buffer_load_b64(rws, g2off, WS_H2, AUX_POLL) : g2;
        for (unsigned spins = 0; X0MODE == 0 && !__all(q2[1] == tag) && spins < SPIN_CAP; ++spins) { CBAR(); q2 = __builtin_amdgcn_raw_buffer_load_b64(rws, g2off, WS_H2, AUX_POLL); }
        u32x2 hv; hv[0] = (q2[0] & 0xffffu) | (q2[0] << 16); hv[1] = (q2[0] >> 16) | (q2[0] & 0xffff0000u);
        *(u32x2*)(hs + ((lane >> 1) & 1) * 64 + (lane >> 3) * 8 + (lane & 1) * 4 + ((lane >> 2) & 1) * 2) = hv;
      }
      asm volatile("s_waitcnt lgkmcnt(0)" ::: "memory");
      if (lane == 0) LDS_ST32(flg + 1 + w, 1u);
    }
    STAMP(4);
    const unsigned soff = WS_SBOX + (unsigned)((h * 8 + (lane & 7)) * 128 + (lane & 8) * 2);
    u32x4 gs;
    if (isq) {
      __builtin_amdgcn_s_sleep(ACKDELAY);
      gs = LD128(rws, soff, 0, AUX_POLL);
      u32x2 ml;
      for (unsigned spins = 0;; ++spins) {
        { const unsigned long long t_ = LDS_LD64(st + 2 * (lane & 7)); ml[0] = (unsigned)t_; ml[1] = (unsigned)(t_ >> 32); }
        if (__all(ml[1] != 0u) || spins > (SPIN_CAP << 4)) break;
        __builtin_amdgcn_s_sleep(2);
      }
      const float m_ = __uint_as_float(ml[0]), l_ = __uint_as_float(ml[1]);
      float M = fmaxf(m_, dppf<XOR1>(m_)); M = fmaxf(M, dppf<XOR2>(M)); M = fmaxf(M, dppf<XOR7>(M));
      const float f_ = __builtin_amdgcn_exp2f((m_ - M) * LOG2E);
      float L = f_ * l_; L += dppf<XOR1>(L); L += dppf<XOR2>(L); L += dppf<XOR7>(L);
      ST128(gran2(M, L, tag), rws, lane == 0 ? WS_SBOX + (unsigned)((h * 8 + j) * 128) : OOB, 0, AUX_SC1);
      STAMP(5);
      STAMP(7);
    }
    if (isq) {
      if (!__all(gok(gs, tag) | !(lane & 8))) {
        for (unsigned spins = 0; spins < SPIN_CAP; ++spins) {
          CBAR();
          gs = LD128(rws, soff, 0, AUX_POLL);
          if (__all(gok(gs, tag) | !(lane & 8))) break;
        }
      }
      STAMP(9);
      CBAR();
      if (STATS_EARLY) { __builtin_amdgcn_s_sleep(STATS_EARLY); gs = LD128(rws, soff, 0, AUX_POLL); }
      for (unsigned spins = 0; spins < (SPIN_CAP << 6); ++spins) { if (__builtin_amdgcn_readfirstlane(LDS_LD32(flg + 4)) == 8u) break; }
      CBAR();
    }
    STAMP(8);
    if (!isq && ffn) {
      for (unsigned spins = 0; spins < (SPIN_CAP << 4); ++spins) { if (__builtin_amdgcn_readfirstlane(LDS_LD32(flg + 3)) == 8u) break; __builtin_amdgcn_s_sleep(1); }
      CBAR();
      sum_publish(red + 512, WS_FBOX, 0, tag);
      sum_publish(red + 512, WS_FBOX, 8, tag);
    }
    if (isq) {
      const unsigned ooff = (unsigned)(h * 8 * 1024 + lane * 16);
      const f32x2 own = sum8((const float*)red + (((lane >> 1) & 1) * 16 + (lane >> 2)) * 4 + 2 * (lane & 1));
      const u32x4 ownG = gran2(own[0], own[1], tagB);
      ST128(ownG, rws, ooff, WS_OBOX + j * 1024, AUX_SC1);
      STAMP(6);
      if (!STATS_EARLY) gs = LD128(rws, soff, 0, AUX_POLL);
      if (H1DELAY) __builtin_amdgcn_s_sleep(H1DELAY);
      u32x4 G[8];
#pragma unroll
      for (int p = 0; p < 8; ++p) G[p] = LD128(rws, p == j ? OOB : ooff, WS_OBOX + p * 1024, AUX_POLL);
#pragma unroll
      for (int p = 0; p < 8; ++p) if (p == j) G[p] = ownG;
      for (unsigned spins = 0; !__all(gok(gs, tag)) && spins < SPIN_CAP; ++spins) { CBAR(); __builtin_amdgcn_s_sleep(2); gs = LD128(rws, soff, 0, AUX_POLL); }
      float wp[8];
      {
        const float ms = __uint_as_float(gs[0]), ls = __uint_as_float(gs[2]);
        float Mg = fmaxf(ms, dppf<XOR1>(ms)); Mg = fmaxf(Mg, dppf<XOR2>(Mg));
        const float e_ = __builtin_amdgcn_exp2f((ms - Mg) * LOG2E);
        float Lg = e_ * ls; Lg += dppf<XOR1>(Lg); Lg += dppf<XOR2>(Lg);
        const float wgt = e_ / Lg;
#pragma unroll
        for (int p = 0; p < 8; ++p) wp[p] = __uint_as_float(__builtin_amdgcn_readlane(__float_as_uint(wgt), p));
      }
      for (unsigned spins = 0;; ++spins) {
        bool allok = true;
        CBAR();
#pragma unroll
        for (int p = 0; p < 8; ++p) {
          if (!__all(gok(G[p], tagB))) { if (allok && HSLEEP) __builtin_amdgcn_s_sleep(HSLEEP); allok = false; G[p] = LD128(rws, ooff, WS_OBOX + p * 1024, AUX_POLL); }
        }
        if (allok || spins > SPIN_CAP) break;
      }
      STAMP(10);
#pragma unroll
      for (int bb = 0; bb < 2; ++bb) {
        float o0 = 0.f, o1 = 0.f;
#pragma unroll
        for (int sp = 0; sp < 4; ++sp) { o0 = fmaf(wp[4 * bb + sp], __uint_as_float(G[4 * bb + sp][0]), o0); o1 = fmaf(wp[4 * bb + sp], __uint_as_float(G[4 * bb + sp][2]), o1); }
        u32x2 ov; ov[0] = pk_bf16(o0, o0); ov[1] = pk_bf16(o1, o1);
        *(u32x2*)(os + bb * 128 + 2 * lane) = ov;
      }
    }
    wg_barrier();
    const int e = lane & 7, ob = e >> 2, ocol = j * 80 + 8 * h + 4 * ((e >> 1) & 1) + 2 * (e & 1);
    const bool fin = !isq && h < 10;
    const unsigned yo = (unsigned)(((lane >> 3) * 8 + j) * 1280 + h * 128 + e * 16);
    const bool f3v = (lane >> 3) < 3;
    unsigned xw = 0; float fa = 0.f, fb = 0.f;
    if (fin) {
      xw = __builtin_amdgcn_raw_buffer_load_b32(rx, (ob * 640 + ocol) * 2, 0, 0);
    }
    wg_barrier();
    STAMP(11);
    if (isq) {
      sum_publish(red + 1024, WS_YBOX, 0, tagB);
      STAMP(12);
    } else if (fin) {
      u32x4 g[4], y[4];
#pragma unroll
      for (int i = 0; i < 4; ++i) g[i] = LD128(rws, (i < 3 || f3v) ? yo : OOB, WS_FBOX + i * 64 * 1280, AUX_POLL);
#pragma unroll
      for (int i = 0; i < 4; ++i) y[i] = LD128(rws, yo, WS_YBOX + i * 64 * 1280, AUX_POLL);
      for (unsigned spins = 0;; ++spins) {
        bool allok = true;
        CBAR();
#pragma unroll
        for (int i = 0; i < 4; ++i) if (!__all(gok(g[i], tag) | (i == 3 && !f3v))) { allok = false; g[i] = LD128(rws, (i < 3 || f3v) ? yo : OOB, WS_FBOX + i * 64 * 1280, AUX_POLL); }
        if (allok || spins > SPIN_CAP) break;
      }
      fa = (__uint_as_float(g[0][0]) + __uint_as_float(g[1][0])) + (__uint_as_float(g[2][0]) + __uint_as_float(g[3][0]));
      fb = (__uint_as_float(g[0][2]) + __uint_as_float(g[1][2])) + (__uint_as_float(g[2][2]) + __uint_as_float(g[3][2]));
      fa += dppf<ROR8>(fa); fb += dppf<ROR8>(fb);
      fa = rbf(x32_sum(x16_sum(fa))); fb = rbf(x32_sum(x16_sum(fb)));
      const float ga = rbf(fa / (1.f + __expf(-fa))), gb = rbf(fb / (1.f + __expf(-fb)));
      STAMP(10);
      for (unsigned spins = 0;; ++spins) {
        bool allok = true;
        CBAR();
#pragma unroll
        for (int i = 0; i < 4; ++i) if (!__all(gok(y[i], tagB))) { allok = false; y[i] = LD128(rws, yo, WS_YBOX + i * 64 * 1280, AUX_POLL); }
        if (allok || spins > SPIN_CAP) break;
      }
      STAMP(13);
      float ya = (__uint_as_float(y[0][0]) + __uint_as_float(y[1][0])) + (__uint_as_float(y[2][0]) + __uint_as_float(y[3][0]));
      float yb = (__uint_as_float(y[0][2]) + __uint_as_float(y[1][2])) + (__uint_as_float(y[2][2]) + __uint_as_float(y[3][2]));
      ya += dppf<ROR8>(ya); yb += dppf<ROR8>(yb);
      ya = rbf(x32_sum(x16_sum(ya))); yb = rbf(x32_sum(x16_sum(yb)));
      const float oa = __uint_as_float(xw << 16) + rbf(ya * ga), obv = __uint_as_float(xw & 0xffff0000u) + rbf(yb * gb);
      __builtin_amdgcn_raw_buffer_store_b32(pk_bf16(oa, obv), RSRC(out, 2560), lane < 8 ? (unsigned)((ob * 640 + ocol) * 2) : OOB, 0, 0);
      if (blk == 0 && lane == 0) __builtin_amdgcn_raw_buffer_store_b32(tag, rws, 0, WS_EPOCH, AUX_SC1);
      STAMP(12);
    }
  }
  if (w == REDW_ && h < 10) { asm volatile("s_waitcnt vmcnt(0)" ::: "memory"); unsigned t_ = (unsigned)__builtin_amdgcn_s_memrealtime(); unsigned d_ = (t_ - tstart_) * 10u; if (d_ > 60000u) d_ = 60000u; while ((unsigned)__builtin_amdgcn_s_memrealtime() - t_ < d_) __builtin_amdgcn_s_sleep(8); }
}

extern "C" void kernel_launch(void* const* d_in, const int* in_sizes, int n_in, void* d_out, int out_size, void* d_ws, size_t ws_size, hipStream_t stream) {
  constexpr int LDS = 96 * 1024;
  (void)hipFuncSetAttribute((const void*)fused_decode, hipFuncAttributeMaxDynamicSharedMemorySize, LDS);
  fused_decode<<<256, 640, LDS, stream>>>((const unsigned short*)d_in[0], (const unsigned short*)d_in[1], (const unsigned short*)d_in[2],
      (const unsigned short*)d_in[3], (const unsigned short*)d_in[4], (const unsigned short*)d_in[6],
      (unsigned char*)d_ws + (WSEND ? ((ws_size - WS_END) & ~(size_t)4095) : 0), (const unsigned short*)d_in[7], (unsigned short*)d_out);
}
```

```cpp
#define PRIO 3
#define WK 24
#define WK2 16
#define WSEND 0
#define HSLEEP 0
#define H1DELAY 0
#define PRETOUCH 0
#define CODEPF 2
#define PDELAY 32
#define REDW_ 1
#include <hip/hip_runtime.h>
#include <hip/hip_bf16.h>
#include <stdint.h>

#define STAMP(i) do {} while (0)
#define STAMP_DECL
#define STAMP_FLUSH() do {} while (0)

typedef unsigned u32x4 __attribute__((ext_vector_type(4)));
typedef unsigned u32x2 __attribute__((ext_vector_type(2)));
typedef float f32x4 __attribute__((ext_vector_type(4)));
typedef float f32x2 __attribute__((ext_vector_type(2)));
typedef __bf16 bf16x8 __attribute__((ext_vector_type(8)));
typedef __bf16 bf16x2 __attribute__((ext_vector_type(2)));

#define POLLAUX 16
#define STAUX 16
constexpr int AUX_NT = 2, AUX_SC1 = STAUX, AUX_POLL = POLLAUX;
constexpr unsigned OOB = 0x7ffffff0u;
constexpr unsigned WS_EPOCH = 0;
constexpr unsigned WS_Q1 = 4096;
constexpr unsigned WS_H1 = WS_Q1 + 256 * 1024;
constexpr unsigned WS_H2 = WS_H1 + 256 * 1280;
constexpr unsigned WS_SBOX = WS_H2 + 256 * 128;
constexpr unsigned WS_FBOX = WS_SBOX + 256 * 128;
constexpr unsigned WS_END = WS_FBOX + 256 * 1280;
constexpr unsigned WS_OBOX = WS_Q1, WS_YBOX = WS_H1;
constexpr unsigned SPIN_CAP = 1u << 16;

#define RSRC(p, n) __builtin_amdgcn_make_buffer_rsrc((void*)(p), 0, (n), 0x00020000)
#define LD128(rs, vo, so, aux) __builtin_amdgcn_raw_buffer_load_b128((rs), (vo), (so), (aux))
#define ST128(v, rs, vo, so, aux) __builtin_amdgcn_raw_buffer_store_b128((v), (rs), (vo), (so), (aux))
#define SBAR() __builtin_amdgcn_sched_barrier(0)
#define LAS __attribute__((address_space(3)))
#define LDS_LD32(p) __hip_atomic_load((LAS unsigned*)(p), __ATOMIC_RELAXED, __HIP_MEMORY_SCOPE_WORKGROUP)
#define LDS_ST32(p, v) __hip_atomic_store((LAS unsigned*)(p), (v), __ATOMIC_RELAXED, __HIP_MEMORY_SCOPE_WORKGROUP)
#define LDS_LD64(p) __hip_atomic_load((LAS unsigned long long*)(p), __ATOMIC_RELAXED, __HIP_MEMORY_SCOPE_WORKGROUP)
#define LDS_ST64(p, v) __hip_atomic_store((LAS unsigned long long*)(p), (v), __ATOMIC_RELAXED, __HIP_MEMORY_SCOPE_WORKGROUP)
#define LDS_ADD(p) asm volatile("ds_add_u32 %0, %1" :: "v"((unsigned)(size_t)(LAS unsigned*)(p)), "v"(1u) : "memory")
#define CBAR() asm volatile("" ::: "memory")

template <int CTRL> __device__ __forceinline__ unsigned dppu(unsigned x) { return (unsigned)__builtin_amdgcn_mov_dpp((int)x, CTRL, 0xf, 0xf, true); }
template <int CTRL> __device__ __forceinline__ float dppf(float x) { return __uint_as_float(dppu<CTRL>(__float_as_uint(x))); }
constexpr int XOR1 = 0xB1, XOR2 = 0x4E, XOR7 = 0x141, ROR8 = 0x128, ROR4 = 0x124, SHL4 = 0x104, SHR4 = 0x114, BCAST = 0x150;

__device__ __forceinline__ float dot2(unsigned k, unsigned q, float acc) {
  return __builtin_amdgcn_fdot2_f32_bf16(__builtin_bit_cast(bf16x2, k), __builtin_bit_cast(bf16x2, q), acc, false);
}
__device__ __forceinline__ unsigned pk_bf16(float lo, float hi) {
  bf16x2 v; v[0] = (__bf16)lo; v[1] = (__bf16)hi; return __builtin_bit_cast(unsigned, v);
}
__device__ __forceinline__ float rbf(float f) { return __uint_as_float(pk_bf16(f, f) & 0xffff0000u); }
__device__ __forceinline__ f32x4 mfma(u32x4 a, u32x4 b, f32x4 c) {
  return __builtin_amdgcn_mfma_f32_16x16x32_bf16(__builtin_bit_cast(bf16x8, a), __builtin_bit_cast(bf16x8, b), c, 0, 0, 0);
}
__device__ __forceinline__ float x16_sum(float x) { auto s = __builtin_amdgcn_permlane16_swap(__float_as_uint(x), __float_as_uint(x), false, false); return __uint_as_float(s[0]) + __uint_as_float(s[1]); }
__device__ __forceinline__ float x32_sum(float x) { auto s = __builtin_amdgcn_permlane32_swap(__float_as_uint(x), __float_as_uint(x), false, false); return __uint_as_float(s[0]) + __uint_as_float(s[1]); }
__device__ __forceinline__ float x16_max(float x) { auto s = __builtin_amdgcn_permlane16_swap(__float_as_uint(x), __float_as_uint(x), false, false); return fmaxf(__uint_as_float(s[0]), __uint_as_float(s[1])); }
__device__ __forceinline__ float x32_max(float x) { auto s = __builtin_amdgcn_permlane32_swap(__float_as_uint(x), __float_as_uint(x), false, false); return fmaxf(__uint_as_float(s[0]), __uint_as_float(s[1])); }
__device__ __forceinline__ float row_sum(float x) { x += dppf<XOR1>(x); x += dppf<XOR2>(x); x += dppf<ROR4>(x); x += dppf<ROR8>(x); return x; }
__device__ __forceinline__ float row_max(float x) { x = fmaxf(x, dppf<XOR1>(x)); x = fmaxf(x, dppf<XOR2>(x)); x = fmaxf(x, dppf<ROR4>(x)); x = fmaxf(x, dppf<ROR8>(x)); return x; }
template <int C0, int C1> __device__ __forceinline__ float trsel(float lo, float hi, bool bit) {
  const float t0 = lo + dppf<C0>(lo), t1 = hi + dppf<C1>(hi);
  return bit ? t1 : t0;
}
__device__ __forceinline__ bool gok(u32x4 g, unsigned tag) { return (g[1] == tag) & (g[3] == tag); }
__device__ __forceinline__ u32x4 gran2(float a, float b, unsigned tag) { u32x4 g; g[0] = __float_as_uint(a); g[1] = tag; g[2] = __float_as_uint(b); g[3] = tag; return g; }
__device__ __forceinline__ void wg_barrier() { asm volatile("s_waitcnt lgkmcnt(0)\n\ts_barrier" ::: "memory"); }

constexpr float LOG2E = 1.4426950408889634f;

#define POLLDELAY() do { if (PDELAY) __builtin_amdgcn_s_sleep(PDELAY); } while (0)
#define NPACE 16
#define MAPX 0
#define KVORD 0
#define ABL_NOX0 0
#define ABL_NOB0 0
#define OSHARED 1
#define ABL 0
#define X0MODE 0
#define ARGPIN 1
#define W2LATE 1
#define ACKDELAY 48
#define STATS_EARLY 0
#define CODEPF_W 7
#define CODEPF_AUX 0
#define CODE_BACK 0
#define VMWAIT(n) asm volatile("s_waitcnt vmcnt(%0)" :: "i"(n) : "memory")

__device__ __forceinline__ f32x2 sum8(const float* p) {
  f32x2 a[8];
#pragma unroll
  for (int i = 0; i < 8; ++i) a[i] = *(const f32x2*)(p + i * 256);
  return ((a[0] + a[1]) + (a[2] + a[3])) + ((a[4] + a[5]) + (a[6] + a[7]));
}

__global__ void __launch_bounds__(640, 3)
fused_decode(const unsigned short* x, const unsigned short* kc, const unsigned short* vc,
             const unsigned short* wq, const unsigned short* wo, const unsigned short* w1,
             unsigned char* ws, const unsigned short* w2, unsigned short* out) {
  unsigned long long pc_entry;
  asm volatile("s_getpc_b64 %0" : "=s"(pc_entry));
  asm volatile("" : "+s"(x), "+s"(kc), "+s"(vc), "+s"(wq), "+s"(wo), "+s"(w1), "+s"(ws));
  extern __shared__ __attribute__((aligned(16))) unsigned char smem[];
  f32x4* red = (f32x4*)smem;
  unsigned* qs = (unsigned*)(smem + 24576);
  unsigned* hs = (unsigned*)(smem + 24576 + 256);
  float* st = (float*)(smem + 24576 + 768);
  unsigned* flg = (unsigned*)(smem + 24576 + 832);
  float* gw = (float*)(smem + 24576 + 896);
  unsigned* wpl = (unsigned*)(smem + 24576 + 848);
  unsigned* os = (unsigned*)(smem + 24576 + 4096);
  unsigned* xl = (unsigned*)(smem + 24576 + 1024);
  const int blk = blockIdx.x, tid = threadIdx.x, lane = tid & 63, w = __builtin_amdgcn_readfirstlane(tid >> 6);
  STAMP_DECL
  STAMP(0);
  const int j = (blk >> 3) & 7, h = (blk & 7) + 8 * (blk >> 6), b = j >> 2, s = j & 3;
  const int r = lane >> 4, c = lane & 15;
  const bool ffn = h < 27;
  const __amdgpu_buffer_rsrc_t rws = RSRC(ws, WS_END);
  const __amdgpu_buffer_rsrc_t rx = RSRC(x, 2560);
  u32x4 mask;
#pragma unroll
  for (int i = 0; i < 4; ++i) mask[i] = (i == ((c & 7) >> 1)) ? ((c & 1) ? 0xffff0000u : 0xffffu) : 0u;
  auto sum_publish = [&](const f32x4* rr, unsigned box, int cbase, unsigned tag) {
    const int cr = cbase + (lane >> 3), e = lane & 7;
    const float* p = (const float*)rr + ((e >> 1) * 16 + (cr & 15)) * 4 + 2 * (e & 1);
    const f32x2 v = sum8(p);
    const float v0 = v[0], v1 = v[1];
    ST128(gran2(v0, v1, tag), rws, cr < 10 ? (unsigned)((h * 8 + j) * 1280 + cr * 128 + e * 16) : OOB, box, AUX_SC1);
  };

  if (w >= 2) {
    const int sw = w - 2;
    const size_t kvbase = ((size_t)(b * 32 + h) * 8192 + 6144 + s * 512) * 128;
    const __amdgpu_buffer_rsrc_t rk = RSRC(kc + kvbase, 131072), rv = RSRC(vc + kvbase, 131072);
    const __amdgpu_buffer_rsrc_t ro = RSRC(wo + (size_t)h * 128 * 640 + j * 80, 128 * 1280 - j * 160);
    const int rot = sw * 4 + ((blk * 37) & 127);
    const int voff = lane * 16;
#define KVOFF(t) ((((((t) >> 2) * 32 + ((t) & 3) + rot) & 127) << 10))
    u32x4 K[16], V[16], WO[4], W2[2];
    float sc = 0.f;
    auto scores = [&]() {
      CBAR();
      const u32x4 q4 = *(const u32x4*)(qs + 4 * c);
      float sb[4];
#pragma unroll
      for (int be = 0; be < 4; ++be) {
        float d[4];
#pragma unroll
        for (int tt = 0; tt < 4; ++tt) {
          const u32x4 kk = K[4 * be + tt];
          d[tt] = dot2(kk[3], q4[3], dot2(kk[2], q4[2], dot2(kk[1], q4[1], dot2(kk[0], q4[0], 0.f))));
        }
        const float e0 = trsel<XOR1, XOR1>(d[0], d[1], c & 1);
        const float e1 = trsel<XOR1, XOR1>(d[2], d[3], c & 1);
        sb[be] = trsel<XOR2, XOR2>(e0, e1, c & 2);
      }
      const float f0 = trsel<SHL4, SHR4>(sb[0], sb[1], c & 4);
      const float f1 = trsel<SHL4, SHR4>(sb[2], sb[3], c & 4);
      const float dotv = trsel<ROR8, ROR8>(f0, f1, c & 8);
      sc = rbf(rbf(dotv) * 0.08838834764831845f);
      const float mw = x32_max(x16_max(row_max(sc)));
      const float ew = __builtin_amdgcn_exp2f((sc - mw) * LOG2E);
      const float lw = x32_sum(x16_sum(row_sum(ew)));
      if (lane == 0) LDS_ST64(st + 2 * sw, (unsigned long long)__float_as_uint(mw) | ((unsigned long long)__float_as_uint(lw) << 32));
    };
    unsigned cp0 = 0, cp1 = 0;
    if (sw == CODEPF_W && (CODEPF == 1 || (blk >> 3) < 4)) {
      const __amdgpu_buffer_rsrc_t rc = RSRC((void*)(pc_entry & ~255ull), 15360);
      cp0 = __builtin_amdgcn_raw_buffer_load_b32(rc, lane * 128, 0, CODEPF_AUX);
      cp1 = __builtin_amdgcn_raw_buffer_load_b32(rc, lane * 128, 8192, CODEPF_AUX);
    }
    if (PRIO) __builtin_amdgcn_s_setprio(PRIO == 3 ? 3 : 2);
    wg_barrier();
#pragma unroll
    for (int i = 0; i < 24; ++i) {
      if (i == 8) asm volatile("" : "+s"(w2));
      if (PRIO == 1 && i == 16) __builtin_amdgcn_s_setprio(1);
      if (PRIO == 3 && i == 16) __builtin_amdgcn_s_setprio(2);
      if (PRIO == 2 && i == 8) __builtin_amdgcn_s_setprio(1);
      if (PRIO == 2 && i == 16) __builtin_amdgcn_s_setprio(0);
      if (i >= WK) VMWAIT(WK - 1);
      if (i < 16) K[i] = LD128(rk, voff, KVOFF(i), AUX_NT); else V[i - 16] = LD128(rv, voff, KVOFF(i - 16), AUX_NT);
      SBAR();
      if (i == WK - 1) STAMP(1);
    }
    STAMP(4);
    const bool early = __builtin_amdgcn_readfirstlane(LDS_LD32(flg + 1)) != 0u;
    if (early) { scores(); STAMP(5); }
    SBAR();
    if (PRIO == 1) __builtin_amdgcn_s_setprio(0);
    if (PRIO == 3) __builtin_amdgcn_s_setprio(1);
#pragma unroll
    for (int t = 8; t < 16; ++t) {
      VMWAIT(WK2 - 1);
      V[t] = LD128(rv, voff, KVOFF(t), AUX_NT);
      SBAR();
    }
    if (!early) {
      for (unsigned spins = 0; spins < (SPIN_CAP << 4); ++spins) {
        if (__builtin_amdgcn_readfirstlane(LDS_LD32(flg + 1)) != 0u) break;
        __builtin_amdgcn_s_sleep(1);
      }
      scores();
      STAMP(6);
    }
    SBAR();
    {
      if (PRIO == 3) __builtin_amdgcn_s_setprio(0);
      const unsigned vo4 = c < 10 ? (unsigned)(r * 4 * 1280 + c * 16) : OOB;
#pragma unroll
      for (int i = 0; i < 4; ++i) { VMWAIT(WK2 - 1); WO[i] = LD128(ro, vo4, (16 * sw + i) * 1280, AUX_NT); SBAR(); }
    }
    {
      const __amdgpu_buffer_rsrc_t r2 = RSRC(w2 + (size_t)h * 64 * 640 + j * 80, ffn ? 64 * 1280 - j * 160 : 0);
      const unsigned vo2 = c < 10 ? (unsigned)(r * 2 * 1280 + c * 16) : OOB;
#pragma unroll
      for (int i = 0; i < 2; ++i) { VMWAIT(WK2 - 1); W2[i] = LD128(r2, vo2, (8 * sw + i) * 1280, AUX_NT); SBAR(); }
    }
    asm volatile("" :: "v"(cp0), "v"(cp1));
    STAMP(7);
    unsigned pp;
    {
      unsigned long long t_ = LDS_LD64(st + 2 * (lane & 7));
      for (unsigned spins = 0; !__all((unsigned)(t_ >> 32) != 0u) && spins < (SPIN_CAP << 4); ++spins) { __builtin_amdgcn_s_sleep(1); t_ = LDS_LD64(st + 2 * (lane & 7)); }
      float M = __uint_as_float((unsigned)t_);
      M = fmaxf(M, dppf<XOR1>(M)); M = fmaxf(M, dppf<XOR2>(M)); M = fmaxf(M, dppf<XOR7>(M));
      const float pv = __builtin_amdgcn_exp2f((sc - M) * LOG2E);
      pp = pk_bf16(pv, pv);
    }
    const unsigned tag = __builtin_amdgcn_readfirstlane(LDS_LD32(flg)) ^ 0x80000000u;
    f32x4 oacc = {0.f, 0.f, 0.f, 0.f};
#define PVSTEP(t) oacc = mfma(mask & dppu<BCAST + (t)>(pp), V[t], oacc);
    PVSTEP(0) PVSTEP(1) PVSTEP(2) PVSTEP(3) PVSTEP(4) PVSTEP(5) PVSTEP(6) PVSTEP(7)
    PVSTEP(8) PVSTEP(9) PVSTEP(10) PVSTEP(11) PVSTEP(12) PVSTEP(13) PVSTEP(14) PVSTEP(15)
#undef PVSTEP
    red[sw * 64 + lane] = oacc;
    asm volatile("s_waitcnt lgkmcnt(0)" ::: "memory");
    if (lane == 0) LDS_ADD(flg + 4);
    STAMP(8);
    if (ffn) {
      for (unsigned spins = 0; spins < (SPIN_CAP << 4); ++spins) {
        if (__builtin_amdgcn_readfirstlane(LDS_LD32(flg + 2)) != 0u) break;
        __builtin_amdgcn_s_sleep(1);
      }
      CBAR();
      const u32x2 hv = *(const u32x2*)(hs + (c >> 3) * 64 + 8 * sw + 2 * r);
      f32x4 facc = {0.f, 0.f, 0.f, 0.f};
      facc = mfma(mask & hv[0], W2[0], facc);
      facc = mfma(mask & hv[1], W2[1], facc);
      red[(8 + sw) * 64 + lane] = facc;
      asm volatile("s_waitcnt lgkmcnt(0)" ::: "memory");
      if (lane == 0) LDS_ADD(flg + 3);
    }
    VMWAIT(0);
    STAMP(15);
    wg_barrier();
    STAMP(10);
    {
      const u32x4 ov = *(const u32x4*)(os + (c >> 3) * 128 + 16 * sw + 4 * r);
      f32x4 y = {0.f, 0.f, 0.f, 0.f}, y2 = {0.f, 0.f, 0.f, 0.f};
      y = mfma(mask & ov[0], WO[0], y);
      y2 = mfma(mask & ov[1], WO[1], y2);
      y = mfma(mask & ov[2], WO[2], y);
      y2 = mfma(mask & ov[3], WO[3], y2);
      red[(16 + sw) * 64 + lane] = y + y2;
    }
    STAMP(11);
    wg_barrier();
    if (sw == 0) sum_publish(red + 1024, WS_YBOX, 8, tag);
  } else {
    __builtin_amdgcn_s_setprio(3);
    const bool isq = (w == 0);
    const unsigned epoch = __builtin_amdgcn_raw_buffer_load_b32(rws, 0, WS_EPOCH, AUX_SC1);
    unsigned pt = 0;
    u32x2 xs[5];
#pragma unroll
    for (int i = 0; i < 5; ++i) xs[i] = __builtin_amdgcn_raw_buffer_load_b64(rx, (lane >> 5) * 1280 + (lane & 31) * 8, i * 256, 0);
    const int row0 = isq ? 160 * (j >> 1) : 80 * j;
    const int nrow = isq ? 160 : 80;
    unsigned short xr[5];
#pragma unroll
    for (int i = 0; i < 5; ++i) xr[i] = __builtin_amdgcn_raw_buffer_load_b16(rx, (lane >> 5) * 1280 + (lane & 31) * 2, (row0 + 32 * i) * 2, 0);
    u32x4 wa[20];
    {
      const __amdgpu_buffer_rsrc_t rw = isq ? RSRC(wq + h * 128 + (j & 1) * 64, 640 * 4096 * 2 - h * 256 - (j & 1) * 128) : RSRC(w1 + h * 64, ffn ? 640 * 1728 * 2 - h * 128 : 0);
      const int rstride = isq ? 8192 : 3456;
      const unsigned vo = (unsigned)((lane >> 3) * rstride + (lane & 7) * 16);
#pragma unroll
      for (int i = 0; i < 10; ++i) wa[i] = LD128(rw, vo, (row0 + 8 * i) * rstride, AUX_NT);
      if (isq) {
#pragma unroll
        for (int i = 10; i < 20; ++i) wa[i] = LD128(rw, vo, (row0 + 8 * i) * rstride, AUX_NT);
      }
    }
    if (isq && lane < 28) LDS_ST32(st + lane, 0u);
    STAMP(1);
    wg_barrier();
    asm volatile("" : "+s"(out));
    float xf[20];
#pragma unroll
    for (int i = 0; i < 5; ++i) {
      xf[4 * i + 0] = __uint_as_float(xs[i][0] << 16); xf[4 * i + 1] = __uint_as_float(xs[i][0] & 0xffff0000u);
      xf[4 * i + 2] = __uint_as_float(xs[i][1] << 16); xf[4 * i + 3] = __uint_as_float(xs[i][1] & 0xffff0000u);
    }
    float sum = 0.f;
#pragma unroll
    for (int i = 0; i < 20; ++i) sum += xf[i];
    sum = x16_sum(row_sum(sum));
    const float mean = sum * (1.f / 640.f);
    float vs = 0.f;
#pragma unroll
    for (int i = 0; i < 20; ++i) { const float d = xf[i] - mean; vs = fmaf(d, d, vs); }
    vs = x16_sum(row_sum(vs));
    const float mb = rbf(mean), den = rbf(sqrtf(rbf(rbf(vs * (1.f / 640.f)) + 1e-5f)));
    unsigned* xw_ = xl + w * 320;
#pragma unroll
    for (int i = 0; i < 5; ++i) {
      const int row = (lane & 31) + 32 * i;
      const float xn = rbf(__uint_as_float((unsigned)xr[i] << 16) - mb) / den;
      if (row < nrow) xw_[(lane >> 5) * 160 + ((row >> 1) & 3) * 40 + (row >> 3) * 2 + (row & 1)] = pk_bf16(xn, xn);
    }
    const unsigned tag = __builtin_amdgcn_readfirstlane(epoch) + 0x9E3779B9u, tagB = tag ^ 0x80000000u;
    if (isq && lane == 0) LDS_ST32(flg, tag);
    asm volatile("s_waitcnt lgkmcnt(0)" ::: "memory");
    asm volatile("" :: "v"(pt));
    STAMP(2);
    f32x4 acc0 = {0.f, 0.f, 0.f, 0.f}, acc1 = {0.f, 0.f, 0.f, 0.f};
    const unsigned* xrd = xw_ + (c >> 3) * 160 + r * 40;
#pragma unroll
    for (int i = 0; i < 10; i += 2) {
      const u32x4 xa = *(const u32x4*)(xrd + 2 * i);
      acc0 = mfma(mask & xa[0], wa[i], acc0); acc1 = mfma(mask & xa[1], wa[i], acc1);
      acc0 = mfma(mask & xa[2], wa[i + 1], acc0); acc1 = mfma(mask & xa[3], wa[i + 1], acc1);
    }
    if (isq) {
#pragma unroll
      for (int i = 10; i < 20; i += 2) {
        const u32x4 xa = *(const u32x4*)(xrd + 2 * i);
        acc0 = mfma(mask & xa[0], wa[i], acc0); acc1 = mfma(mask & xa[1], wa[i], acc1);
        acc0 = mfma(mask & xa[2], wa[i + 1], acc0); acc1 = mfma(mask & xa[3], wa[i + 1], acc1);
      }
    }
    if (isq || ffn) {
      f32x4 v;
#pragma unroll
      for (int i = 0; i < 4; ++i) { const float t_ = c < 8 ? acc0[i] : acc1[i]; v[i] = t_ + dppf<ROR8>(t_); }
      const u32x4 pub = gran2(c < 8 ? v[0] : v[2], c < 8 ? v[1] : v[3], tag);
      const unsigned cu = (unsigned)(h * 8 + j);
      if (isq) {
        if (X0MODE != 1 && X0MODE != 3) ST128(pub, rws, cu * 1024 + lane * 16, WS_Q1, AUX_SC1);
        STAMP(3);
        POLLDELAY();
        const unsigned goff = (unsigned)((h * 8 + (lane >> 5)) * 1024 + (b * 32 + (lane & 31)) * 16);
        u32x4 G[4];
#pragma unroll
        for (int p = 0; p < 4; ++p) G[p] = (X0MODE == 1 || X0MODE == 2) ? pub : LD128(rws, goff, WS_Q1 + p * 2048, AUX_POLL);
        for (unsigned spins = 0; X0MODE == 0 || X0MODE == 4; ++spins) {
          bool allok = true;
          CBAR();
#pragma unroll
          for (int p = 0; p < 4; ++p) if (!__all(gok(G[p], tag))) { allok = false; G[p] = LD128(rws, goff, WS_Q1 + p * 2048, AUX_POLL); }
          if (spins == 0) STAMP(14);
          if (allok || spins > SPIN_CAP) break;
        }
        const float a0 = (__uint_as_float(G[0][0]) + __uint_as_float(G[1][0])) + (__uint_as_float(G[2][0]) + __uint_as_float(G[3][0]));
        const float a1 = (__uint_as_float(G[0][2]) + __uint_as_float(G[1][2])) + (__uint_as_float(G[2][2]) + __uint_as_float(G[3][2]));
        qs[(lane >> 5) * 32 + (c & 7) * 4 + (r & 1) * 2 + (c >> 3)] = pk_bf16(a0, a1);
        ST128(gran2(0.f, 0.f, tag), rws, lane == 0 ? cu * 128 + 16 : OOB, WS_SBOX, AUX_SC1);
      } else {
        if (X0MODE == 0 || X0MODE == 2) ST128(pub, rws, (unsigned)((h * 8 + (c & 7)) * 1280 + j * 128 + (c >> 3) * 64 + r * 16), WS_H1, AUX_SC1);
        STAMP(3);
        POLLDELAY(); POLLDELAY();
        const unsigned goff = cu * 1280 + lane * 16;
        u32x4 G0 = (X0MODE == 0 || X0MODE == 3) ? LD128(rws, goff, WS_H1, AUX_POLL) : pub;
        for (unsigned spins = 0; X0MODE == 0 && !__all(gok(G0, tag)) && spins < SPIN_CAP; ++spins) { CBAR(); G0 = LD128(rws, goff, WS_H1, AUX_POLL); }
        STAMP(14);
        float a0 = __uint_as_float(G0[0]), a1 = __uint_as_float(G0[2]);
        a0 += dppf<ROR8>(a0); a1 += dppf<ROR8>(a1);
        a0 = fmaxf(x32_sum(x16_sum(a0)), 0.f); a1 = fmaxf(x32_sum(x16_sum(a1)), 0.f);
        u32x2 g2; g2[0] = pk_bf16(a0, a1); g2[1] = tag;
        if (X0MODE == 0 || X0MODE == 2) __builtin_amdgcn_raw_buffer_store_b64(g2, rws, lane < 8 ? cu * 128 + lane * 8 : OOB, WS_H2, AUX_SC1);
        const unsigned g2off = (unsigned)((h * 8 + (lane >> 3)) * 128 + (lane & 7) * 8);
        POLLDELAY(); POLLDELAY(); POLLDELAY();
        u32x2 q2 = (X0MODE == 0 || X0MODE == 3) ? __builtin_amdgcn_raw_buffer_load_b64(rws, g2off, WS_H2, AUX_POLL) : g2;
        for (unsigned spins = 0; X0MODE == 0 && !__all(q2[1] == tag) && spins < SPIN_CAP; ++spins) { CBAR(); q2 = __builtin_amdgcn_raw_buffer_load_b64(rws, g2off, WS_H2, AUX_POLL); }
        u32x2 hv; hv[0] = (q2[0] & 0xffffu) | (q2[0] << 16); hv[1] = (q2[0] >> 16) | (q2[0] & 0xffff0000u);
        *(u32x2*)(hs + ((lane >> 1) & 1) * 64 + (lane >> 3) * 8 + (lane & 1) * 4 + ((lane >> 2) & 1) * 2) = hv;
      }
      asm volatile("s_waitcnt lgkmcnt(0)" ::: "memory");
      if (lane == 0) LDS_ST32(flg + 1 + w, 1u);
    }
    STAMP(4);
    const unsigned soff = WS_SBOX + (unsigned)((h * 8 + (lane & 7)) * 128 + (lane & 8) * 2);
    u32x4 gs;
    if (isq) {
      __builtin_amdgcn_s_sleep(ACKDELAY);
      gs = LD128(rws, soff, 0, AUX_POLL);
      u32x2 ml;
      for (unsigned spins = 0;; ++spins) {
        { const unsigned long long t_ = LDS_LD64(st + 2 * (lane & 7)); ml[0] = (unsigned)t_; ml[1] = (unsigned)(t_ >> 32); }
        if (__all(ml[1] != 0u) || spins > (SPIN_CAP << 4)) break;
        __builtin_amdgcn_s_sleep(2);
      }
      const float m_ = __uint_as_float(ml[0]), l_ = __uint_as_float(ml[1]);
      float M = fmaxf(m_, dppf<XOR1>(m_)); M = fmaxf(M, dppf<XOR2>(M)); M = fmaxf(M, dppf<XOR7>(M));
      const float f_ = __builtin_amdgcn_exp2f((m_ - M) * LOG2E);
      float L = f_ * l_; L += dppf<XOR1>(L); L += dppf<XOR2>(L); L += dppf<XOR7>(L);
      ST128(gran2(M, L, tag), rws, lane == 0 ? WS_SBOX + (unsigned)((h * 8 + j) * 128) : OOB, 0, AUX_SC1);
      STAMP(5);
      STAMP(7);
    }
    if (isq) {
      if (!__all(gok(gs, tag) | !(lane & 8))) {
        for (unsigned spins = 0; spins < SPIN_CAP; ++spins) {
          CBAR();
          gs = LD128(rws, soff, 0, AUX_POLL);
          if (__all(gok(gs, tag) | !(lane & 8))) break;
        }
      }
      STAMP(9);
      CBAR();
      if (STATS_EARLY) { __builtin_amdgcn_s_sleep(STATS_EARLY); gs = LD128(rws, soff, 0, AUX_POLL); }
      for (unsigned spins = 0; spins < (SPIN_CAP << 6); ++spins) { if (__builtin_amdgcn_readfirstlane(LDS_LD32(flg + 4)) == 8u) break; }
      CBAR();
    }
    STAMP(8);
    if (!isq && ffn) {
      for (unsigned spins = 0; spins < (SPIN_CAP << 4); ++spins) { if (__builtin_amdgcn_readfirstlane(LDS_LD32(flg + 3)) == 8u) break; __builtin_amdgcn_s_sleep(1); }
      CBAR();
      sum_publish(red + 512, WS_FBOX, 0, tag);
      sum_publish(red + 512, WS_FBOX, 8, tag);
    }
    if (isq) {
      const unsigned ooff = (unsigned)(h * 8 * 1024 + lane * 16);
      const f32x2 own = sum8((const float*)red + (((lane >> 1) & 1) * 16 + (lane >> 2)) * 4 + 2 * (lane & 1));
      const u32x4 ownG = gran2(own[0], own[1], tagB);
      ST128(ownG, rws, ooff, WS_OBOX + j * 1024, AUX_SC1);
      STAMP(6);
      if (!STATS_EARLY) gs = LD128(rws, soff, 0, AUX_POLL);
      if (H1DELAY) __builtin_amdgcn_s_sleep(H1DELAY);
      u32x4 G[8];
#pragma unroll
      for (int p = 0; p < 8; ++p) G[p] = LD128(rws, p == j ? OOB : ooff, WS_OBOX + p * 1024, AUX_POLL);
#pragma unroll
      for (int p = 0; p < 8; ++p) if (p == j) G[p] = ownG;
      for (unsigned spins = 0; !__all(gok(gs, tag)) && spins < SPIN_CAP; ++spins) { CBAR(); __builtin_amdgcn_s_sleep(2); gs = LD128(rws, soff, 0, AUX_POLL); }
      float wp[8];
      {
        const float ms = __uint_as_float(gs[0]), ls = __uint_as_float(gs[2]);
        float Mg = fmaxf(ms, dppf<XOR1>(ms)); Mg = fmaxf(Mg, dppf<XOR2>(Mg));
        const float e_ = __builtin_amdgcn_exp2f((ms - Mg) * LOG2E);
        float Lg = e_ * ls; Lg += dppf<XOR1>(Lg); Lg += dppf<XOR2>(Lg);
        const float wgt = e_ / Lg;
#pragma unroll
        for (int p = 0; p < 8; ++p) wp[p] = __uint_as_float(__builtin_amdgcn_readlane(__float_as_uint(wgt), p));
      }
      for (unsigned spins = 0;; ++spins) {
        bool allok = true;
        CBAR();
#pragma unroll
        for (int p = 0; p < 8; ++p) {
          if (!__all(gok(G[p], tagB))) { if (allok && HSLEEP) __builtin_amdgcn_s_sleep(HSLEEP); allok = false; G[p] = LD128(rws, ooff, WS_OBOX + p * 1024, AUX_POLL); }
        }
        if (allok || spins > SPIN_CAP) break;
      }
      STAMP(10);
#pragma unroll
      for (int bb = 0; bb < 2; ++bb) {
        float o0 = 0.f, o1 = 0.f;
#pragma unroll
        for (int sp = 0; sp < 4; ++sp) { o0 = fmaf(wp[4 * bb + sp], __uint_as_float(G[4 * bb + sp][0]), o0); o1 = fmaf(wp[4 * bb + sp], __uint_as_float(G[4 * bb + sp][2]), o1); }
        u32x2 ov; ov[0] = pk_bf16(o0, o0); ov[1] = pk_bf16(o1, o1);
        *(u32x2*)(os + bb * 128 + 2 * lane) = ov;
      }
    }
    wg_barrier();
    const int e = lane & 7, ob = e >> 2, ocol = j * 80 + 8 * h + 4 * ((e >> 1) & 1) + 2 * (e & 1);
    const bool fin = !isq && h < 10;
    const unsigned yo = (unsigned)(((lane >> 3) * 8 + j) * 1280 + h * 128 + e * 16);
    const bool f3v = (lane >> 3) < 3;
    unsigned xw = 0; float fa = 0.f, fb = 0.f;
    if (fin) {
      xw = __builtin_amdgcn_raw_buffer_load_b32(rx, (ob * 640 + ocol) * 2, 0, 0);
    }
    wg_barrier();
    STAMP(11);
    if (isq) {
      sum_publish(red + 1024, WS_YBOX, 0, tagB);
      STAMP(12);
    } else if (fin) {
      u32x4 g[4], y[4];
#pragma unroll
      for (int i = 0; i < 4; ++i) g[i] = LD128(rws, (i < 3 || f3v) ? yo : OOB, WS_FBOX + i * 64 * 1280, AUX_POLL);
#pragma unroll
      for (int i = 0; i < 4; ++i) y[i] = LD128(rws, yo, WS_YBOX + i * 64 * 1280, AUX_POLL);
      for (unsigned spins = 0;; ++spins) {
        bool allok = true;
        CBAR();
#pragma unroll
        for (int i = 0; i < 4; ++i) if (!__all(gok(g[i], tag) | (i == 3 && !f3v))) { allok = false; g[i] = LD128(rws, (i < 3 || f3v) ? yo : OOB, WS_FBOX + i * 64 * 1280, AUX_POLL); }
        if (allok || spins > SPIN_CAP) break;
      }
      fa = (__uint_as_float(g[0][0]) + __uint_as_float(g[1][0])) + (__uint_as_float(g[2][0]) + __uint_as_float(g[3][0]));
      fb = (__uint_as_float(g[0][2]) + __uint_as_float(g[1][2])) + (__uint_as_float(g[2][2]) + __uint_as_float(g[3][2]));
      fa += dppf<ROR8>(fa); fb += dppf<ROR8>(fb);
      fa = rbf(x32_sum(x16_sum(fa))); fb = rbf(x32_sum(x16_sum(fb)));
      const float ga = rbf(fa / (1.f + __expf(-fa))), gb = rbf(fb / (1.f + __expf(-fb)));
      STAMP(10);
      for (unsigned spins = 0;; ++spins) {
        bool allok = true;
        CBAR();
#pragma unroll
        for (int i = 0; i < 4; ++i) if (!__all(gok(y[i], tagB))) { allok = false; y[i] = LD128(rws, yo, WS_YBOX + i * 64 * 1280, AUX_POLL); }
        if (allok || spins > SPIN_CAP) break;
      }
      STAMP(13);
      float ya = (__uint_as_float(y[0][0]) + __uint_as_float(y[1][0])) + (__uint_as_float(y[2][0]) + __uint_as_float(y[3][0]));
      float yb = (__uint_as_float(y[0][2]) + __uint_as_float(y[1][2])) + (__uint_as_float(y[2][2]) + __uint_as_float(y[3][2]));
      ya += dppf<ROR8>(ya); yb += dppf<ROR8>(yb);
      ya = rbf(x32_sum(x16_sum(ya))); yb = rbf(x32_sum(x16_sum(yb)));
      const float oa = __uint_as_float(xw << 16) + rbf(ya * ga), obv = __uint_as_float(xw & 0xffff0000u) + rbf(yb * gb);
      __builtin_amdgcn_raw_buffer_store_b32(pk_bf16(oa, obv), RSRC(out, 2560), lane < 8 ? (unsigned)((ob * 640 + ocol) * 2) : OOB, 0, 0);
      if (blk == 0 && lane == 0) __builtin_amdgcn_raw_buffer_store_b32(tag, rws, 0, WS_EPOCH, AUX_SC1);
      STAMP(12);
    }
  }
  STAMP_FLUSH();
}

extern "C" void kernel_launch(void* const* d_in, const int* in_sizes, int n_in, void* d_out, int out_size, void* d_ws, size_t ws_size, hipStream_t stream) {
  constexpr int LDS = 96 * 1024;
  (void)hipFuncSetAttribute((const void*)fused_decode, hipFuncAttributeMaxDynamicSharedMemorySize, LDS);
  fused_decode<<<256, 640, LDS, stream>>>((const unsigned short*)d_in[0], (const unsigned short*)d_in[1], (const unsigned short*)d_in[2],
      (const unsigned short*)d_in[3], (const unsigned short*)d_in[4], (const unsigned short*)d_in[6],
      (unsigned char*)d_ws + (WSEND ? ((ws_size - WS_END) & ~(size_t)4095) : 0), (const unsigned short*)d_in[7], (unsigned short*)d_out);
}
```

```cpp
#define ROTM 13
#define REDW_ 1
#include <hip/hip_runtime.h>
#include <hip/hip_bf16.h>
#include <stdint.h>

#define STAMP(i) do {} while (0)
#define STAMP_DECL
#define STAMP_FLUSH() do {} while (0)

typedef unsigned u32x4 __attribute__((ext_vector_type(4)));
typedef unsigned u32x2 __attribute__((ext_vector_type(2)));
typedef float f32x4 __attribute__((ext_vector_type(4)));
typedef float f32x2 __attribute__((ext_vector_type(2)));
typedef __bf16 bf16x8 __attribute__((ext_vector_type(8)));
typedef __bf16 bf16x2 __attribute__((ext_vector_type(2)));

#define PDELAY 32
#define ACKDELAY 48
#define YSLEEP 0
#define FEARLY 0
constexpr int AUX_NT = 2, AUX_SC1 = 16;
constexpr unsigned OOB = 0x7ffffff0u;
constexpr unsigned WS_EPOCH = 0;
constexpr unsigned WS_Q1 = 4096;
constexpr unsigned WS_H1 = WS_Q1 + 256 * 1024;
constexpr unsigned WS_H2 = WS_H1 + 256 * 1280;
constexpr unsigned WS_S = WS_H2 + 256 * 128;
constexpr unsigned WS_F = WS_S + 256 * 128;
constexpr unsigned WS_END = WS_F + 256 * 1280;
constexpr unsigned WS_O = WS_Q1, WS_Y = WS_H1;
constexpr unsigned SPIN_CAP = 1u << 16, LSPIN_CAP = 1u << 22;
constexpr int CODE_BYTES = 14336;
constexpr int L_RED = 0, L_QS = 24576, L_HS = L_QS + 256, L_ST = L_HS + 512, L_FLG = L_ST + 64, L_XL = L_FLG + 64, L_OS = L_XL + 2560, L_END = L_OS + 1024;

#define RSRC(p, n) __builtin_amdgcn_make_buffer_rsrc((void*)(p), 0, (n), 0x00020000)
#define LD128(rs, vo, so, aux) __builtin_amdgcn_raw_buffer_load_b128((rs), (vo), (so), (aux))
#define ST128(v, rs, vo, so, aux) __builtin_amdgcn_raw_buffer_store_b128((v), (rs), (vo), (so), (aux))
#define SBAR() __builtin_amdgcn_sched_barrier(0)
#define CBAR() asm volatile("" ::: "memory")
#define LGKM0() asm volatile("s_waitcnt lgkmcnt(0)" ::: "memory")
#define LAS __attribute__((address_space(3)))
#define LDS_LD32(p) __hip_atomic_load((LAS unsigned*)(p), __ATOMIC_RELAXED, __HIP_MEMORY_SCOPE_WORKGROUP)
#define LDS_ST32(p, v) __hip_atomic_store((LAS unsigned*)(p), (v), __ATOMIC_RELAXED, __HIP_MEMORY_SCOPE_WORKGROUP)
#define LDS_LD64(p) __hip_atomic_load((LAS unsigned long long*)(p), __ATOMIC_RELAXED, __HIP_MEMORY_SCOPE_WORKGROUP)
#define LDS_ST64(p, v) __hip_atomic_store((LAS unsigned long long*)(p), (v), __ATOMIC_RELAXED, __HIP_MEMORY_SCOPE_WORKGROUP)
#define LDS_INC(p) asm volatile("ds_add_u32 %0, %1" :: "v"((unsigned)(size_t)(LAS unsigned*)(p)), "v"(1u) : "memory")

template <int CTRL> __device__ __forceinline__ unsigned dppu(unsigned x) { return (unsigned)__builtin_amdgcn_mov_dpp((int)x, CTRL, 0xf, 0xf, true); }
template <int CTRL> __device__ __forceinline__ float dppf(float x) { return __uint_as_float(dppu<CTRL>(__float_as_uint(x))); }
constexpr int XOR1 = 0xB1, XOR2 = 0x4E, XOR7 = 0x141, ROR8 = 0x128, ROR4 = 0x124, SHL4 = 0x104, SHR4 = 0x114, BCAST = 0x150;

__device__ __forceinline__ float dot2(unsigned k, unsigned q, float acc) {
  return __builtin_amdgcn_fdot2_f32_bf16(__builtin_bit_cast(bf16x2, k), __builtin_bit_cast(bf16x2, q), acc, false);
}
__device__ __forceinline__ unsigned pk_bf16(float lo, float hi) { bf16x2 v; v[0] = (__bf16)lo; v[1] = (__bf16)hi; return __builtin_bit_cast(unsigned, v); }
__device__ __forceinline__ float rbf(float f) { return __uint_as_float(pk_bf16(f, f) & 0xffff0000u); }
__device__ __forceinline__ float bflo(unsigned u) { return __uint_as_float(u << 16); }
__device__ __forceinline__ float bfhi(unsigned u) { return __uint_as_float(u & 0xffff0000u); }
__device__ __forceinline__ f32x4 mfma(u32x4 a, u32x4 b, f32x4 c) {
  return __builtin_amdgcn_mfma_f32_16x16x32_bf16(__builtin_bit_cast(bf16x8, a), __builtin_bit_cast(bf16x8, b), c, 0, 0, 0);
}
__device__ __forceinline__ float x16_sum(float x) { auto s = __builtin_amdgcn_permlane16_swap(__float_as_uint(x), __float_as_uint(x), false, false); return __uint_as_float(s[0]) + __uint_as_float(s[1]); }
__device__ __forceinline__ float x32_sum(float x) { auto s = __builtin_amdgcn_permlane32_swap(__float_as_uint(x), __float_as_uint(x), false, false); return __uint_as_float(s[0]) + __uint_as_float(s[1]); }
__device__ __forceinline__ float x16_max(float x) { auto s = __builtin_amdgcn_permlane16_swap(__float_as_uint(x), __float_as_uint(x), false, false); return fmaxf(__uint_as_float(s[0]), __uint_as_float(s[1])); }
__device__ __forceinline__ float x32_max(float x) { auto s = __builtin_amdgcn_permlane32_swap(__float_as_uint(x), __float_as_uint(x), false, false); return fmaxf(__uint_as_float(s[0]), __uint_as_float(s[1])); }
__device__ __forceinline__ float row_sum(float x) { x += dppf<XOR1>(x); x += dppf<XOR2>(x); x += dppf<ROR4>(x); x += dppf<ROR8>(x); return x; }
__device__ __forceinline__ float row_max(float x) { x = fmaxf(x, dppf<XOR1>(x)); x = fmaxf(x, dppf<XOR2>(x)); x = fmaxf(x, dppf<ROR4>(x)); x = fmaxf(x, dppf<ROR8>(x)); return x; }
__device__ __forceinline__ float oct_max(float x) { x = fmaxf(x, dppf<XOR1>(x)); x = fmaxf(x, dppf<XOR2>(x)); return fmaxf(x, dppf<XOR7>(x)); }
template <int C0, int C1> __device__ __forceinline__ float trsel(float lo, float hi, bool bit) {
  const float t0 = lo + dppf<C0>(lo), t1 = hi + dppf<C1>(hi);
  return bit ? t1 : t0;
}
__device__ __forceinline__ bool gok(u32x4 g, unsigned tag) { return (g[1] == tag) & (g[3] == tag); }
__device__ __forceinline__ u32x4 gran2(float a, float b, unsigned tag) { u32x4 g; g[0] = __float_as_uint(a); g[1] = tag; g[2] = __float_as_uint(b); g[3] = tag; return g; }
__device__ __forceinline__ float gv0(u32x4 g) { return __uint_as_float(g[0]); }
__device__ __forceinline__ float gv1(u32x4 g) { return __uint_as_float(g[2]); }
__device__ __forceinline__ void wg_barrier() { asm volatile("s_waitcnt lgkmcnt(0)\n\ts_barrier" ::: "memory"); }
__device__ __forceinline__ unsigned lds_flag(unsigned* p) { return __builtin_amdgcn_readfirstlane(LDS_LD32(p)); }
__device__ __forceinline__ f32x2 sum8(const float* p) {
  f32x2 a[8];
#pragma unroll
  for (int i = 0; i < 8; ++i) a[i] = *(const f32x2*)(p + i * 256);
  return ((a[0] + a[1]) + (a[2] + a[3])) + ((a[4] + a[5]) + (a[6] + a[7]));
}
constexpr float LOG2E = 1.4426950408889634f;

__global__ void __launch_bounds__(640, 3)
fused_decode(const unsigned short* x, const unsigned short* kc, const unsigned short* vc,
             const unsigned short* wq, const unsigned short* wo, const unsigned short* w1,
             unsigned char* ws, const unsigned short* w2, unsigned short* out) {
  unsigned long long pc_entry;
  asm volatile("s_getpc_b64 %0" : "=s"(pc_entry));
  asm volatile("" : "+s"(x), "+s"(kc), "+s"(vc), "+s"(wq), "+s"(wo), "+s"(w1), "+s"(ws), "+s"(w2), "+s"(out));
  __shared__ __attribute__((aligned(16))) unsigned char smem[L_END];
  f32x4* red = (f32x4*)(smem + L_RED);
  unsigned* qs = (unsigned*)(smem + L_QS);
  unsigned* hs = (unsigned*)(smem + L_HS);
  float* st = (float*)(smem + L_ST);
  unsigned* flg = (unsigned*)(smem + L_FLG);
  unsigned* xl = (unsigned*)(smem + L_XL);
  unsigned* os = (unsigned*)(smem + L_OS);
  const int blk = blockIdx.x, tid = threadIdx.x, lane = tid & 63, w = __builtin_amdgcn_readfirstlane(tid >> 6);
  STAMP_DECL
  const unsigned tstart_ = (unsigned)__builtin_amdgcn_s_memrealtime();
  const int j = (blk >> 3) & 7, h = (blk & 7) + 8 * (blk >> 6), b = j >> 2, s = j & 3;
  const int r = lane >> 4, c = lane & 15;
  const bool ffn = h < 27;
  const unsigned cu = (unsigned)(h * 8 + j);
  const __amdgpu_buffer_rsrc_t rws = RSRC(ws, WS_END);
  const __amdgpu_buffer_rsrc_t rx = RSRC(x, 2560);
  u32x4 mask;
#pragma unroll
  for (int i = 0; i < 4; ++i) mask[i] = (i == ((c & 7) >> 1)) ? ((c & 1) ? 0xffff0000u : 0xffffu) : 0u;
  auto sum_publish = [&](const f32x4* rr, unsigned box, int cbase, unsigned tg) {
    const int cr = cbase + (lane >> 3), e = lane & 7;
    const f32x2 v = sum8((const float*)rr + ((e >> 1) * 16 + (cr & 15)) * 4 + 2 * (e & 1));
    ST128(gran2(v[0], v[1], tg), rws, cr < 10 ? cu * 1280 + (unsigned)(cr * 128 + e * 16) : OOB, box, AUX_SC1);
  };

  if (w >= 2) {
    const int sw = w - 2;
    const size_t kvbase = ((size_t)(b * 32 + h) * 8192 + 6144 + s * 512) * 128;
    const __amdgpu_buffer_rsrc_t rk = RSRC(kc + kvbase, 131072), rv = RSRC(vc + kvbase, 131072);
    const int rot = sw * 4 + ((blk * ROTM) & 127);
    const int voff = lane * 16;
#define KVOFF(t) ((((((t) >> 2) * 32 + ((t) & 3) + rot) & 127) << 10))
    u32x4 K[16], V[16], WO[4], W2[2];
    float sc = 0.f;
    auto scores = [&]() {
      CBAR();
      const u32x4 q4 = *(const u32x4*)(qs + 4 * c);
      float sb[4];
#pragma unroll
      for (int be = 0; be < 4; ++be) {
        float d[4];
#pragma unroll
        for (int tt = 0; tt < 4; ++tt) {
          const u32x4 kk = K[4 * be + tt];
          d[tt] = dot2(kk[3], q4[3], dot2(kk[2], q4[2], dot2(kk[1], q4[1], dot2(kk[0], q4[0], 0.f))));
        }
        const float e0 = trsel<XOR1, XOR1>(d[0], d[1], c & 1);
        const float e1 = trsel<XOR1, XOR1>(d[2], d[3], c & 1);
        sb[be] = trsel<XOR2, XOR2>(e0, e1, c & 2);
      }
      const float f0 = trsel<SHL4, SHR4>(sb[0], sb[1], c & 4);
      const float f1 = trsel<SHL4, SHR4>(sb[2], sb[3], c & 4);
      const float dotv = trsel<ROR8, ROR8>(f0, f1, c & 8);
      sc = rbf(rbf(dotv) * 0.08838834764831845f);
      const float mw = x32_max(x16_max(row_max(sc)));
      const float lw = x32_sum(x16_sum(row_sum(__builtin_amdgcn_exp2f((sc - mw) * LOG2E))));
      if (lane == 0) LDS_ST64(st + 2 * sw, (unsigned long long)__float_as_uint(mw) | ((unsigned long long)__float_as_uint(lw) << 32));
    };
    unsigned cp0 = 0, cp1 = 0;
    if (sw == 7 && (blk >> 3) < 4) {
      const __amdgpu_buffer_rsrc_t rc = RSRC((void*)(pc_entry & ~255ull), CODE_BYTES);
      cp0 = __builtin_amdgcn_raw_buffer_load_b32(rc, lane * 128, 0, 0);
      cp1 = __builtin_amdgcn_raw_buffer_load_b32(rc, lane * 128, 8192, 0);
    }
    __builtin_amdgcn_s_setprio(3);
    wg_barrier();
#pragma unroll
    for (int i = 0; i < 24; ++i) {
      if (i == 16) __builtin_amdgcn_s_setprio(2);
      if (i < 16) K[i] = LD128(rk, voff, KVOFF(i), AUX_NT); else V[i - 16] = LD128(rv, voff, KVOFF(i - 16), AUX_NT);
      SBAR();
    }
    const bool early = lds_flag(flg + 1) != 0u;
    if (early) scores();
    SBAR();
    __builtin_amdgcn_s_setprio(1);
#pragma unroll
    for (int t = 8; t < 16; ++t) { V[t] = LD128(rv, voff, KVOFF(t), AUX_NT); SBAR(); }
    if (!early) {
      for (unsigned sp = 0; sp < LSPIN_CAP && lds_flag(flg + 1) == 0u; ++sp) __builtin_amdgcn_s_sleep(1);
      scores();
    }
    SBAR();
    __builtin_amdgcn_s_setprio(0);
    {
      const __amdgpu_buffer_rsrc_t ro = RSRC(wo + (size_t)h * 128 * 640 + j * 80, 128 * 1280 - j * 160);
      const __amdgpu_buffer_rsrc_t r2 = RSRC(w2 + (size_t)h * 64 * 640 + j * 80, ffn ? 64 * 1280 - j * 160 : 0);
      const unsigned vo4 = c < 10 ? (unsigned)(r * 4 * 1280 + c * 16) : OOB;
      const unsigned vo2 = c < 10 ? (unsigned)(r * 2 * 1280 + c * 16) : OOB;
#pragma unroll
      for (int i = 0; i < 4; ++i) { WO[i] = LD128(ro, vo4, (16 * sw + i) * 1280, AUX_NT); SBAR(); }
#pragma unroll
      for (int i = 0; i < 2; ++i) { W2[i] = LD128(r2, vo2, (8 * sw + i) * 1280, AUX_NT); SBAR(); }
    }
    asm volatile("" :: "v"(cp0), "v"(cp1));
    unsigned pp;
    {
      unsigned long long t_ = LDS_LD64(st + 2 * (lane & 7));
      for (unsigned sp = 0; !__all((unsigned)(t_ >> 32) != 0u) && sp < LSPIN_CAP; ++sp) { __builtin_amdgcn_s_sleep(1); t_ = LDS_LD64(st + 2 * (lane & 7)); }
      const float M = oct_max(__uint_as_float((unsigned)t_));
      const float pv = __builtin_amdgcn_exp2f((sc - M) * LOG2E);
      pp = pk_bf16(pv, pv);
    }
    const unsigned tagB = lds_flag(flg) ^ 0x80000000u;
    f32x4 oacc = {0.f, 0.f, 0.f, 0.f};
#define PVSTEP(t) oacc = mfma(mask & dppu<BCAST + (t)>(pp), V[t], oacc);
    PVSTEP(0) PVSTEP(1) PVSTEP(2) PVSTEP(3) PVSTEP(4) PVSTEP(5) PVSTEP(6) PVSTEP(7)
    PVSTEP(8) PVSTEP(9) PVSTEP(10) PVSTEP(11) PVSTEP(12) PVSTEP(13) PVSTEP(14) PVSTEP(15)
#undef PVSTEP
    red[sw * 64 + lane] = oacc;
    LGKM0();
    if (lane == 0) LDS_INC(flg + 4);
    if (ffn) {
      for (unsigned sp = 0; sp < LSPIN_CAP && lds_flag(flg + 2) == 0u; ++sp) __builtin_amdgcn_s_sleep(1);
      CBAR();
      const u32x2 hv = *(const u32x2*)(hs + (c >> 3) * 64 + 8 * sw + 2 * r);
      f32x4 facc = {0.f, 0.f, 0.f, 0.f};
      facc = mfma(mask & hv[0], W2[0], facc);
      facc = mfma(mask & hv[1], W2[1], facc);
      red[(8 + sw) * 64 + lane] = facc;
      LGKM0();
      if (lane == 0) LDS_INC(flg + 3);
    }
    asm volatile("s_waitcnt vmcnt(0)" ::: "memory");
    wg_barrier();
    {
      const u32x4 ov = *(const u32x4*)(os + (c >> 3) * 128 + 16 * sw + 4 * r);
      f32x4 y = {0.f, 0.f, 0.f, 0.f}, y2 = {0.f, 0.f, 0.f, 0.f};
      y = mfma(mask & ov[0], WO[0], y);
      y2 = mfma(mask & ov[1], WO[1], y2);
      y = mfma(mask & ov[2], WO[2], y);
      y2 = mfma(mask & ov[3], WO[3], y2);
      red[(16 + sw) * 64 + lane] = y + y2;
    }
    wg_barrier();
    if (sw == 0) sum_publish(red + 1024, WS_Y, 8, tagB);
  } else {
    __builtin_amdgcn_s_setprio(3);
    const bool isq = (w == 0);
    const unsigned epoch = __builtin_amdgcn_raw_buffer_load_b32(rws, 0, WS_EPOCH, AUX_SC1);
    u32x2 xs[5];
#pragma unroll
    for (int i = 0; i < 5; ++i) xs[i] = __builtin_amdgcn_raw_buffer_load_b64(rx, (lane >> 5) * 1280 + (lane & 31) * 8, i * 256, 0);
    const int row0 = isq ? 160 * (j >> 1) : 80 * j;
    const int nrow = isq ? 160 : 80;
    unsigned short xr[5];
#pragma unroll
    for (int i = 0; i < 5; ++i) xr[i] = __builtin_amdgcn_raw_buffer_load_b16(rx, (lane >> 5) * 1280 + (lane & 31) * 2, (row0 + 32 * i) * 2, 0);
    u32x4 wa[20];
    {
      const __amdgpu_buffer_rsrc_t rw = isq ? RSRC(wq + h * 128 + (j & 1) * 64, 640 * 4096 * 2 - h * 256 - (j & 1) * 128) : RSRC(w1 + h * 64, ffn ? 640 * 1728 * 2 - h * 128 : 0);
      const int rstride = isq ? 8192 : 3456;
      const unsigned vo = (unsigned)((lane >> 3) * rstride + (lane & 7) * 16);
#pragma unroll
      for (int i = 0; i < 10; ++i) wa[i] = LD128(rw, vo, (row0 + 8 * i) * rstride, AUX_NT);
      if (isq) {
#pragma unroll
        for (int i = 10; i < 20; ++i) wa[i] = LD128(rw, vo, (row0 + 8 * i) * rstride, AUX_NT);
      }
    }
    if (isq && lane < 24) LDS_ST32(st + lane, 0u);
    wg_barrier();
    float xf[20];
#pragma unroll
    for (int i = 0; i < 5; ++i) { xf[4 * i] = bflo(xs[i][0]); xf[4 * i + 1] = bfhi(xs[i][0]); xf[4 * i + 2] = bflo(xs[i][1]); xf[4 * i + 3] = bfhi(xs[i][1]); }
    float sum = 0.f;
#pragma unroll
    for (int i = 0; i < 20; ++i) sum += xf[i];
    sum = x16_sum(row_sum(sum));
    const float mean = sum * (1.f / 640.f);
    float vs = 0.f;
#pragma unroll
    for (int i = 0; i < 20; ++i) { const float d = xf[i] - mean; vs = fmaf(d, d, vs); }
    vs = x16_sum(row_sum(vs));
    const float mb = rbf(mean), den = rbf(sqrtf(rbf(rbf(vs * (1.f / 640.f)) + 1e-5f)));
    unsigned* xw_ = xl + w * 320;
#pragma unroll
    for (int i = 0; i < 5; ++i) {
      const int row = (lane & 31) + 32 * i;
      const float xn = rbf(bflo(xr[i]) - mb) / den;
      if (row < nrow) xw_[(lane >> 5) * 160 + ((row >> 1) & 3) * 40 + (row >> 3) * 2 + (row & 1)] = pk_bf16(xn, xn);
    }
    const unsigned tag = __builtin_amdgcn_readfirstlane(epoch) + 0x9E3779B9u, tagB = tag ^ 0x80000000u;
    if (isq && lane == 0) LDS_ST32(flg, tag);
    LGKM0();
    f32x4 acc0 = {0.f, 0.f, 0.f, 0.f}, acc1 = {0.f, 0.f, 0.f, 0.f};
    const unsigned* xrd = xw_ + (c >> 3) * 160 + r * 40;
#pragma unroll
    for (int i = 0; i < 10; i += 2) {
      const u32x4 xa = *(const u32x4*)(xrd + 2 * i);
      acc0 = mfma(mask & xa[0], wa[i], acc0); acc1 = mfma(mask & xa[1], wa[i], acc1);
      acc0 = mfma(mask & xa[2], wa[i + 1], acc0); acc1 = mfma(mask & xa[3], wa[i + 1], acc1);
    }
    if (isq) {
#pragma unroll
      for (int i = 10; i < 20; i += 2) {
        const u32x4 xa = *(const u32x4*)(xrd + 2 * i);
        acc0 = mfma(mask & xa[0], wa[i], acc0); acc1 = mfma(mask & xa[1], wa[i], acc1);
        acc0 = mfma(mask & xa[2], wa[i + 1], acc0); acc1 = mfma(mask & xa[3], wa[i + 1], acc1);
      }
    }
    f32x4 v;
#pragma unroll
    for (int i = 0; i < 4; ++i) { const float t_ = c < 8 ? acc0[i] : acc1[i]; v[i] = t_ + dppf<ROR8>(t_); }
    const u32x4 pub = gran2(c < 8 ? v[0] : v[2], c < 8 ? v[1] : v[3], tag);
    if (isq) {
      ST128(pub, rws, cu * 1024 + lane * 16, WS_Q1, AUX_SC1);
      __builtin_amdgcn_s_sleep(PDELAY);
      const unsigned goff = (unsigned)((h * 8 + (lane >> 5)) * 1024 + (b * 32 + (lane & 31)) * 16);
      u32x4 G[8];
#pragma unroll
      for (int p = 0; p < 4; ++p) G[p] = LD128(rws, goff, WS_Q1 + p * 2048, AUX_SC1);
      for (unsigned sp = 0;; ++sp) {
        bool allok = true;
        CBAR();
#pragma unroll
        for (int p = 0; p < 4; ++p) if (!__all(gok(G[p], tag))) { allok = false; G[p] = LD128(rws, goff, WS_Q1 + p * 2048, AUX_SC1); }
        if (allok || sp > SPIN_CAP) break;
      }
      qs[(lane >> 5) * 32 + (c & 7) * 4 + (r & 1) * 2 + (c >> 3)] = pk_bf16((gv0(G[0]) + gv0(G[1])) + (gv0(G[2]) + gv0(G[3])), (gv1(G[0]) + gv1(G[1])) + (gv1(G[2]) + gv1(G[3])));
      ST128(gran2(0.f, 0.f, tag), rws, lane == 0 ? cu * 128 + 16 : OOB, WS_S, AUX_SC1);
      LGKM0();
      if (lane == 0) LDS_ST32(flg + 1, 1u);
      const unsigned soff = WS_S + (unsigned)((h * 8 + (lane & 7)) * 128 + (lane & 8) * 2);
      __builtin_amdgcn_s_sleep(ACKDELAY);
      u32x4 gs = LD128(rws, soff, 0, AUX_SC1);
      unsigned long long t_ = LDS_LD64(st + 2 * (lane & 7));
      for (unsigned sp = 0; !__all((unsigned)(t_ >> 32) != 0u) && sp < LSPIN_CAP; ++sp) { __builtin_amdgcn_s_sleep(2); t_ = LDS_LD64(st + 2 * (lane & 7)); }
      {
        const float m_ = __uint_as_float((unsigned)t_), l_ = __uint_as_float((unsigned)(t_ >> 32));
        const float M = oct_max(m_);
        float L = __builtin_amdgcn_exp2f((m_ - M) * LOG2E) * l_; L += dppf<XOR1>(L); L += dppf<XOR2>(L); L += dppf<XOR7>(L);
        ST128(gran2(M, L, tag), rws, lane == 0 ? cu * 128 : OOB, WS_S, AUX_SC1);
      }
      if (!__all(gok(gs, tag) | !(lane & 8))) {
        for (unsigned sp = 0; sp < SPIN_CAP; ++sp) {
          CBAR();
          gs = LD128(rws, soff, 0, AUX_SC1);
          if (__all(gok(gs, tag) | !(lane & 8))) break;
        }
      }
      for (unsigned sp = 0; sp < LSPIN_CAP && lds_flag(flg + 4) != 8u; ++sp) {}
      CBAR();
      const unsigned ooff = (unsigned)(h * 8 * 1024 + lane * 16);
      const f32x2 own = sum8((const float*)red + (((lane >> 1) & 1) * 16 + (lane >> 2)) * 4 + 2 * (lane & 1));
      const u32x4 ownG = gran2(own[0], own[1], tagB);
      ST128(ownG, rws, ooff, WS_O + j * 1024, AUX_SC1);
      gs = LD128(rws, soff, 0, AUX_SC1);
#pragma unroll
      for (int p = 0; p < 8; ++p) G[p] = LD128(rws, p == j ? OOB : ooff, WS_O + p * 1024, AUX_SC1);
#pragma unroll
      for (int p = 0; p < 8; ++p) if (p == j) G[p] = ownG;
      for (unsigned sp = 0; !__all(gok(gs, tag)) && sp < SPIN_CAP; ++sp) { CBAR(); __builtin_amdgcn_s_sleep(2); gs = LD128(rws, soff, 0, AUX_SC1); }
      float wp[8];
      {
        const float ms = gv0(gs), ls = gv1(gs);
        float Mg = fmaxf(ms, dppf<XOR1>(ms)); Mg = fmaxf(Mg, dppf<XOR2>(Mg));
        const float e_ = __builtin_amdgcn_exp2f((ms - Mg) * LOG2E);
        float Lg = e_ * ls; Lg += dppf<XOR1>(Lg); Lg += dppf<XOR2>(Lg);
        const float wgt = e_ / Lg;
#pragma unroll
        for (int p = 0; p < 8; ++p) wp[p] = __uint_as_float(__builtin_amdgcn_readlane(__float_as_uint(wgt), p));
      }
      for (unsigned sp = 0;; ++sp) {
        bool allok = true;
        CBAR();
#pragma unroll
        for (int p = 0; p < 8; ++p) if (!__all(gok(G[p], tagB))) { allok = false; G[p] = LD128(rws, ooff, WS_O + p * 1024, AUX_SC1); }
        if (allok || sp > SPIN_CAP) break;
      }
#pragma unroll
      for (int bb = 0; bb < 2; ++bb) {
        float o0 = 0.f, o1 = 0.f;
#pragma unroll
        for (int sp = 0; sp < 4; ++sp) { o0 = fmaf(wp[4 * bb + sp], gv0(G[4 * bb + sp]), o0); o1 = fmaf(wp[4 * bb + sp], gv1(G[4 * bb + sp]), o1); }
        u32x2 ov; ov[0] = pk_bf16(o0, o0); ov[1] = pk_bf16(o1, o1);
        *(u32x2*)(os + bb * 128 + 2 * lane) = ov;
      }
      wg_barrier();
      wg_barrier();
      sum_publish(red + 1024, WS_Y, 0, tagB);
    } else {
      if (ffn) {
        ST128(pub, rws, (unsigned)((h * 8 + (c & 7)) * 1280 + j * 128 + (c >> 3) * 64 + r * 16), WS_H1, AUX_SC1);
        __builtin_amdgcn_s_sleep(2 * PDELAY);
        const unsigned goff = cu * 1280 + lane * 16;
        u32x4 G0 = LD128(rws, goff, WS_H1, AUX_SC1);
        for (unsigned sp = 0; !__all(gok(G0, tag)) && sp < SPIN_CAP; ++sp) { CBAR(); G0 = LD128(rws, goff, WS_H1, AUX_SC1); }
        float a0 = gv0(G0), a1 = gv1(G0);
        a0 += dppf<ROR8>(a0); a1 += dppf<ROR8>(a1);
        a0 = fmaxf(x32_sum(x16_sum(a0)), 0.f); a1 = fmaxf(x32_sum(x16_sum(a1)), 0.f);
        u32x2 g2; g2[0] = pk_bf16(a0, a1); g2[1] = tag;
        __builtin_amdgcn_raw_buffer_store_b64(g2, rws, lane < 8 ? cu * 128 + lane * 8 : OOB, WS_H2, AUX_SC1);
        const unsigned g2off = (unsigned)((h * 8 + (lane >> 3)) * 128 + (lane & 7) * 8);
        __builtin_amdgcn_s_sleep(3 * PDELAY);
        u32x2 q2 = __builtin_amdgcn_raw_buffer_load_b64(rws, g2off, WS_H2, AUX_SC1);
        for (unsigned sp = 0; !__all(q2[1] == tag) && sp < SPIN_CAP; ++sp) { CBAR(); q2 = __builtin_amdgcn_raw_buffer_load_b64(rws, g2off, WS_H2, AUX_SC1); }
        u32x2 hv; hv[0] = (q2[0] & 0xffffu) | (q2[0] << 16); hv[1] = (q2[0] >> 16) | (q2[0] & 0xffff0000u);
        *(u32x2*)(hs + ((lane >> 1) & 1) * 64 + (lane >> 3) * 8 + (lane & 1) * 4 + ((lane >> 2) & 1) * 2) = hv;
        LGKM0();
        if (lane == 0) LDS_ST32(flg + 2, 1u);
        for (unsigned sp = 0; sp < LSPIN_CAP && lds_flag(flg + 3) != 8u; ++sp) __builtin_amdgcn_s_sleep(1);
        CBAR();
        sum_publish(red + 512, WS_F, 0, tag);
        sum_publish(red + 512, WS_F, 8, tag);
      }
      const int e = lane & 7, ob = e >> 2, ocol = j * 80 + 8 * h + 4 * ((e >> 1) & 1) + 2 * (e & 1);
      const bool fin = h < 10, f3v = (lane >> 3) < 3;
      const unsigned yo = (unsigned)(((lane >> 3) * 8 + j) * 1280 + h * 128 + e * 16);
      unsigned xw = 0;
      u32x4 g[4], y[4];
      if (fin) xw = __builtin_amdgcn_raw_buffer_load_b32(rx, (ob * 640 + ocol) * 2, 0, 0);
      wg_barrier();
      if (FEARLY && fin) {
#pragma unroll
        for (int i = 0; i < 4; ++i) g[i] = LD128(rws, (i < 3 || f3v) ? yo : OOB, WS_F + i * 64 * 1280, AUX_SC1);
      }
      wg_barrier();
      if (fin) {
        if (!FEARLY) {
#pragma unroll
          for (int i = 0; i < 4; ++i) g[i] = LD128(rws, (i < 3 || f3v) ? yo : OOB, WS_F + i * 64 * 1280, AUX_SC1);
        }
#pragma unroll
        for (int i = 0; i < 4; ++i) y[i] = LD128(rws, yo, WS_Y + i * 64 * 1280, AUX_SC1);
        for (unsigned sp = 0;; ++sp) {
          bool allok = true;
          CBAR();
#pragma unroll
          for (int i = 0; i < 4; ++i) if (!__all(gok(g[i], tag) | (i == 3 && !f3v))) { allok = false; g[i] = LD128(rws, (i < 3 || f3v) ? yo : OOB, WS_F + i * 64 * 1280, AUX_SC1); }
          if (allok || sp > SPIN_CAP) break;
        }
        float fa = (gv0(g[0]) + gv0(g[1])) + (gv0(g[2]) + gv0(g[3])), fb = (gv1(g[0]) + gv1(g[1])) + (gv1(g[2]) + gv1(g[3]));
        fa += dppf<ROR8>(fa); fb += dppf<ROR8>(fb);
        fa = rbf(x32_sum(x16_sum(fa))); fb = rbf(x32_sum(x16_sum(fb)));
        const float ga = rbf(fa / (1.f + __expf(-fa))), gb = rbf(fb / (1.f + __expf(-fb)));
        for (unsigned sp = 0;; ++sp) {
          bool allok = true;
          CBAR();
#pragma unroll
          for (int i = 0; i < 4; ++i) if (!__all(gok(y[i], tagB))) { if (YSLEEP && allok) __builtin_amdgcn_s_sleep(YSLEEP); allok = false; y[i] = LD128(rws, yo, WS_Y + i * 64 * 1280, AUX_SC1); }
          if (allok || sp > SPIN_CAP) break;
        }
        float ya = (gv0(y[0]) + gv0(y[1])) + (gv0(y[2]) + gv0(y[3])), yb = (gv1(y[0]) + gv1(y[1])) + (gv1(y[2]) + gv1(y[3]));
        ya += dppf<ROR8>(ya); yb += dppf<ROR8>(yb);
        ya = rbf(x32_sum(x16_sum(ya))); yb = rbf(x32_sum(x16_sum(yb)));
        __builtin_amdgcn_raw_buffer_store_b32(pk_bf16(bflo(xw) + rbf(ya * ga), bfhi(xw) + rbf(yb * gb)), RSRC(out, 2560), lane < 8 ? (unsigned)((ob * 640 + ocol) * 2) : OOB, 0, 0);
        if (blk == 0 && lane == 0) __builtin_amdgcn_raw_buffer_store_b32(tag, rws, 0, WS_EPOCH, AUX_SC1);
      }
    }
  }
  if (w == REDW_ && h < 10) { asm volatile("s_waitcnt vmcnt(0)" ::: "memory"); unsigned t_ = (unsigned)__builtin_amdgcn_s_memrealtime(); unsigned d_ = (t_ - tstart_) * 10u; if (d_ > 60000u) d_ = 60000u; while ((unsigned)__builtin_amdgcn_s_memrealtime() - t_ < d_) __builtin_amdgcn_s_sleep(8); }
}

extern "C" void kernel_launch(void* const* d_in, const int* in_sizes, int n_in, void* d_out, int out_size, void* d_ws, size_t ws_size, hipStream_t stream) {
  fused_decode<<<256, 640, 0, stream>>>((const unsigned short*)d_in[0], (const unsigned short*)d_in[1], (const unsigned short*)d_in[2],
      (const unsigned short*)d_in[3], (const unsigned short*)d_in[4], (const unsigned short*)d_in[6],
      (unsigned char*)d_ws, (const unsigned short*)d_in[7], (unsigned short*)d_out);
}
```

```cpp
#include <hip/hip_runtime.h>
#include <hip/hip_bf16.h>
#include <stdint.h>

#define STAMP(i) do {} while (0)
#define STAMP_DECL
#define STAMP_FLUSH() do {} while (0)

typedef unsigned u32x4 __attribute__((ext_vector_type(4)));
typedef unsigned u32x2 __attribute__((ext_vector_type(2)));
typedef float f32x4 __attribute__((ext_vector_type(4)));
typedef float f32x2 __attribute__((ext_vector_type(2)));
typedef __bf16 bf16x8 __attribute__((ext_vector_type(8)));
typedef __bf16 bf16x2 __attribute__((ext_vector_type(2)));
typedef short s16x4 __attribute__((ext_vector_type(4)));

#define PDELAY 32
#define ACKDELAY 48
#define YSLEEP 0
#define YDELAY 4
#define ROTM 37
constexpr int AUX_NT = 2, AUX_SC1 = 16;
constexpr unsigned OOB = 0x7ffffff0u;
constexpr unsigned WS_EPOCH = 0;
constexpr unsigned WS_Q1 = 4096;
constexpr unsigned WS_H1 = WS_Q1 + 256 * 1024;
constexpr unsigned WS_H2 = WS_H1 + 256 * 1280;
constexpr unsigned WS_S = WS_H2 + 256 * 128;
constexpr unsigned WS_F = WS_S + 256 * 128;
constexpr unsigned WS_END = WS_F + 256 * 1280;
constexpr unsigned WS_O = WS_Q1, WS_Y = WS_H1;
constexpr unsigned SPIN_CAP = 1u << 16, LSPIN_CAP = 1u << 22;
constexpr int CODE_BYTES = 13312;
constexpr int L_RED = 0, L_WL = 16384, L_QS = L_WL + 20480, L_HS = L_QS + 256, L_ST = L_HS + 512, L_FLG = L_ST + 64, L_XL = L_FLG + 64, L_OS = L_XL + 2560, L_END = L_OS + 512;

#define RSRC(p, n) __builtin_amdgcn_make_buffer_rsrc((void*)(p), 0, (n), 0x00020000)
#define LD128(rs, vo, so, aux) __builtin_amdgcn_raw_buffer_load_b128((rs), (vo), (so), (aux))
#define ST128(v, rs, vo, so, aux) __builtin_amdgcn_raw_buffer_store_b128((v), (rs), (vo), (so), (aux))
#define SBAR() __builtin_amdgcn_sched_barrier(0)
#define CBAR() asm volatile("" ::: "memory")
#define LGKM0() asm volatile("s_waitcnt lgkmcnt(0)" ::: "memory")
#define LAS __attribute__((address_space(3)))
#define LDS_LD32(p) __hip_atomic_load((LAS unsigned*)(p), __ATOMIC_RELAXED, __HIP_MEMORY_SCOPE_WORKGROUP)
#define LDS_ST32(p, v) __hip_atomic_store((LAS unsigned*)(p), (v), __ATOMIC_RELAXED, __HIP_MEMORY_SCOPE_WORKGROUP)
#define LDS_LD64(p) __hip_atomic_load((LAS unsigned long long*)(p), __ATOMIC_RELAXED, __HIP_MEMORY_SCOPE_WORKGROUP)
#define LDS_ST64(p, v) __hip_atomic_store((LAS unsigned long long*)(p), (v), __ATOMIC_RELAXED, __HIP_MEMORY_SCOPE_WORKGROUP)
#define LDS_INC(p) asm volatile("ds_add_u32 %0, %1" :: "v"((unsigned)(size_t)(LAS unsigned*)(p)), "v"(1u) : "memory")

template <int CTRL> __device__ __forceinline__ unsigned dppu(unsigned x) { return (unsigned)__builtin_amdgcn_mov_dpp((int)x, CTRL, 0xf, 0xf, true); }
template <int CTRL> __device__ __forceinline__ float dppf(float x) { return __uint_as_float(dppu<CTRL>(__float_as_uint(x))); }
constexpr int XOR1 = 0xB1, XOR2 = 0x4E, XOR7 = 0x141, ROR8 = 0x128, ROR4 = 0x124, SHL4 = 0x104, SHR4 = 0x114, BCAST = 0x150;

__device__ __forceinline__ float dot2(unsigned k, unsigned q, float acc) {
  return __builtin_amdgcn_fdot2_f32_bf16(__builtin_bit_cast(bf16x2, k), __builtin_bit_cast(bf16x2, q), acc, false);
}
__device__ __forceinline__ unsigned pk_bf16(float lo, float hi) { bf16x2 v; v[0] = (__bf16)lo; v[1] = (__bf16)hi; return __builtin_bit_cast(unsigned, v); }
__device__ __forceinline__ float rbf(float f) { return __uint_as_float(pk_bf16(f, f) & 0xffff0000u); }
__device__ __forceinline__ float bflo(unsigned u) { return __uint_as_float(u << 16); }
__device__ __forceinline__ float bfhi(unsigned u) { return __uint_as_float(u & 0xffff0000u); }
__device__ __forceinline__ f32x4 mfma(u32x4 a, u32x4 b, f32x4 c) {
  return __builtin_amdgcn_mfma_f32_16x16x32_bf16(__builtin_bit_cast(bf16x8, a), __builtin_bit_cast(bf16x8, b), c, 0, 0, 0);
}
__device__ __forceinline__ float x16_sum(float x) { auto s = __builtin_amdgcn_permlane16_swap(__float_as_uint(x), __float_as_uint(x), false, false); return __uint_as_float(s[0]) + __uint_as_float(s[1]); }
__device__ __forceinline__ float x32_sum(float x) { auto s = __builtin_amdgcn_permlane32_swap(__float_as_uint(x), __float_as_uint(x), false, false); return __uint_as_float(s[0]) + __uint_as_float(s[1]); }
__device__ __forceinline__ float x16_max(float x) { auto s = __builtin_amdgcn_permlane16_swap(__float_as_uint(x), __float_as_uint(x), false, false); return fmaxf(__uint_as_float(s[0]), __uint_as_float(s[1])); }
__device__ __forceinline__ float x32_max(float x) { auto s = __builtin_amdgcn_permlane32_swap(__float_as_uint(x), __float_as_uint(x), false, false); return fmaxf(__uint_as_float(s[0]), __uint_as_float(s[1])); }
__device__ __forceinline__ float row_sum(float x) { x += dppf<XOR1>(x); x += dppf<XOR2>(x); x += dppf<ROR4>(x); x += dppf<ROR8>(x); return x; }
__device__ __forceinline__ float row_max(float x) { x = fmaxf(x, dppf<XOR1>(x)); x = fmaxf(x, dppf<XOR2>(x)); x = fmaxf(x, dppf<ROR4>(x)); x = fmaxf(x, dppf<ROR8>(x)); return x; }
__device__ __forceinline__ float oct_max(float x) { x = fmaxf(x, dppf<XOR1>(x)); x = fmaxf(x, dppf<XOR2>(x)); return fmaxf(x, dppf<XOR7>(x)); }
template <int C0, int C1> __device__ __forceinline__ float trsel(float lo, float hi, bool bit) {
  const float t0 = lo + dppf<C0>(lo), t1 = hi + dppf<C1>(hi);
  return bit ? t1 : t0;
}
__device__ __forceinline__ bool gok(u32x4 g, unsigned tag) { return (g[1] == tag) & (g[3] == tag); }
__device__ __forceinline__ u32x4 gran2(float a, float b, unsigned tag) { u32x4 g; g[0] = __float_as_uint(a); g[1] = tag; g[2] = __float_as_uint(b); g[3] = tag; return g; }
__device__ __forceinline__ float gv0(u32x4 g) { return __uint_as_float(g[0]); }
__device__ __forceinline__ float gv1(u32x4 g) { return __uint_as_float(g[2]); }
__device__ __forceinline__ void wg_barrier() { asm volatile("s_waitcnt lgkmcnt(0)\n\ts_barrier" ::: "memory"); }
__device__ __forceinline__ unsigned lds_flag(unsigned* p) { return __builtin_amdgcn_readfirstlane(LDS_LD32(p)); }
__device__ __forceinline__ f32x2 sum8(const float* p) {
  f32x2 a[8];
#pragma unroll
  for (int i = 0; i < 8; ++i) a[i] = *(const f32x2*)(p + i * 256);
  return ((a[0] + a[1]) + (a[2] + a[3])) + ((a[4] + a[5]) + (a[6] + a[7]));
}
__device__ __forceinline__ u32x2 trread(unsigned char* p) { return __builtin_bit_cast(u32x2, __builtin_amdgcn_ds_read_tr16_b64_v4i16((LAS s16x4*)p)); }
constexpr float LOG2E = 1.4426950408889634f;

__global__ void __launch_bounds__(640, 3)
fused_decode(const unsigned short* x, const unsigned short* kc, const unsigned short* vc,
             const unsigned short* wq, const unsigned short* wo, const unsigned short* w1,
             unsigned char* ws, const unsigned short* w2, unsigned short* out) {
  unsigned long long pc_entry;
  asm volatile("s_getpc_b64 %0" : "=s"(pc_entry));
  asm volatile("" : "+s"(x), "+s"(kc), "+s"(vc), "+s"(wq), "+s"(wo), "+s"(w1), "+s"(ws), "+s"(w2), "+s"(out));
  __shared__ __attribute__((aligned(16))) unsigned char smem[L_END];
  f32x4* red = (f32x4*)(smem + L_RED);
  unsigned* qs = (unsigned*)(smem + L_QS);
  unsigned* hs = (unsigned*)(smem + L_HS);
  float* st = (float*)(smem + L_ST);
  unsigned* flg = (unsigned*)(smem + L_FLG);
  unsigned* xl = (unsigned*)(smem + L_XL);
  unsigned* os = (unsigned*)(smem + L_OS);
  const int blk = blockIdx.x, tid = threadIdx.x, lane = tid & 63, w = __builtin_amdgcn_readfirstlane(tid >> 6);
  STAMP_DECL
  const int j = (blk >> 3) & 7, h = (blk & 7) + 8 * (blk >> 6), b = j >> 2, s = j & 3;
  const int r = lane >> 4, c = lane & 15;
  const bool ffn = h < 27;
  const unsigned cu = (unsigned)(h * 8 + j);
  const __amdgpu_buffer_rsrc_t rws = RSRC(ws, WS_END);
  const __amdgpu_buffer_rsrc_t rx = RSRC(x, 2560);
  u32x4 mask;
#pragma unroll
  for (int i = 0; i < 4; ++i) mask[i] = (i == ((c & 7) >> 1)) ? ((c & 1) ? 0xffff0000u : 0xffffu) : 0u;
  auto f_publish = [&](int cbase, unsigned tg) {
    const int cr = cbase + (lane >> 3), e = lane & 7;
    const float* p = (const float*)(red + 512) + ((e >> 2) * 16 + (cr & 15)) * 4 + (e & 3);
    float a0[8], a1[8];
#pragma unroll
    for (int i = 0; i < 8; ++i) { a0[i] = p[i * 256]; a1[i] = p[i * 256 + 128]; }
    const float v0 = ((a0[0] + a0[1]) + (a0[2] + a0[3])) + ((a0[4] + a0[5]) + (a0[6] + a0[7]));
    const float v1 = ((a1[0] + a1[1]) + (a1[2] + a1[3])) + ((a1[4] + a1[5]) + (a1[6] + a1[7]));
    ST128(gran2(v0, v1, tg), rws, cr < 10 ? cu * 1280 + (unsigned)(cr * 128 + e * 16) : OOB, WS_F, AUX_SC1);
  };

  if (w >= 2) {
    const int sw = w - 2;
    const size_t kvbase = ((size_t)(b * 32 + h) * 8192 + 6144 + s * 512) * 128;
    const __amdgpu_buffer_rsrc_t rk = RSRC(kc + kvbase, 131072), rv = RSRC(vc + kvbase, 131072);
    const int rot = sw * 4 + ((blk * ROTM) & 127);
    const int voff = lane * 16;
#define KVOFF(t) ((((((t) >> 2) * 32 + ((t) & 3) + rot) & 127) << 10))
    u32x4 K[16], V[16], WO[4], W2[2];
    float sc = 0.f;
    auto scores = [&]() {
      CBAR();
      const u32x4 q4 = *(const u32x4*)(qs + 4 * c);
      float sb[4];
#pragma unroll
      for (int be = 0; be < 4; ++be) {
        float d[4];
#pragma unroll
        for (int tt = 0; tt < 4; ++tt) {
          const u32x4 kk = K[4 * be + tt];
          d[tt] = dot2(kk[3], q4[3], dot2(kk[2], q4[2], dot2(kk[1], q4[1], dot2(kk[0], q4[0], 0.f))));
        }
        const float e0 = trsel<XOR1, XOR1>(d[0], d[1], c & 1);
        const float e1 = trsel<XOR1, XOR1>(d[2], d[3], c & 1);
        sb[be] = trsel<XOR2, XOR2>(e0, e1, c & 2);
      }
      const float f0 = trsel<SHL4, SHR4>(sb[0], sb[1], c & 4);
      const float f1 = trsel<SHL4, SHR4>(sb[2], sb[3], c & 4);
      const float dotv = trsel<ROR8, ROR8>(f0, f1, c & 8);
      sc = rbf(rbf(dotv) * 0.08838834764831845f);
      const float mw = x32_max(x16_max(row_max(sc)));
      const float lw = x32_sum(x16_sum(row_sum(__builtin_amdgcn_exp2f((sc - mw) * LOG2E))));
      if (lane == 0) LDS_ST64(st + 2 * sw, (unsigned long long)__float_as_uint(mw) | ((unsigned long long)__float_as_uint(lw) << 32));
    };
    unsigned cp0 = 0, cp1 = 0;
    if (sw == 7 && (blk >> 3) < 4) {
      const __amdgpu_buffer_rsrc_t rc = RSRC((void*)(pc_entry & ~255ull), CODE_BYTES);
      cp0 = __builtin_amdgcn_raw_buffer_load_b32(rc, lane * 128, 0, 0);
      cp1 = __builtin_amdgcn_raw_buffer_load_b32(rc, lane * 128, 8192, 0);
    }
    __builtin_amdgcn_s_setprio(3);
    wg_barrier();
#pragma unroll
    for (int i = 0; i < 24; ++i) {
      if (i == 16) __builtin_amdgcn_s_setprio(2);
      if (i < 16) K[i] = LD128(rk, voff, KVOFF(i), AUX_NT); else V[i - 16] = LD128(rv, voff, KVOFF(i - 16), AUX_NT);
      SBAR();
    }
    const bool early = lds_flag(flg + 1) != 0u;
    if (early) scores();
    SBAR();
    __builtin_amdgcn_s_setprio(1);
#pragma unroll
    for (int t = 8; t < 16; ++t) { V[t] = LD128(rv, voff, KVOFF(t), AUX_NT); SBAR(); }
    if (!early) {
      for (unsigned sp = 0; sp < LSPIN_CAP && lds_flag(flg + 1) == 0u; ++sp) __builtin_amdgcn_s_sleep(1);
      scores();
    }
    SBAR();
    __builtin_amdgcn_s_setprio(0);
    {
      const __amdgpu_buffer_rsrc_t ro = RSRC(wo + (size_t)h * 128 * 640 + j * 80, 128 * 1280 - j * 160);
      const __amdgpu_buffer_rsrc_t r2 = RSRC(w2 + (size_t)h * 64 * 640 + j * 80, ffn ? 64 * 1280 - j * 160 : 0);
      const unsigned vo4 = c < 10 ? (unsigned)(r * 4 * 1280 + c * 16) : OOB;
      const unsigned vo2 = c < 10 ? (unsigned)(r * 2 * 1280 + c * 16) : OOB;
#pragma unroll
      for (int i = 0; i < 4; ++i) { WO[i] = LD128(ro, vo4, (16 * sw + i) * 1280, AUX_NT); SBAR(); }
#pragma unroll
      for (int i = 0; i < 2; ++i) { W2[i] = LD128(r2, vo2, (8 * sw + i) * 1280, AUX_NT); SBAR(); }
    }
    asm volatile("" :: "v"(cp0), "v"(cp1));
    unsigned pp;
    {
      unsigned long long t_ = LDS_LD64(st + 2 * (lane & 7));
      for (unsigned sp = 0; !__all((unsigned)(t_ >> 32) != 0u) && sp < LSPIN_CAP; ++sp) { __builtin_amdgcn_s_sleep(1); t_ = LDS_LD64(st + 2 * (lane & 7)); }
      const float M = oct_max(__uint_as_float((unsigned)t_));
      const float pv = __builtin_amdgcn_exp2f((sc - M) * LOG2E);
      pp = pk_bf16(pv, pv);
    }
    const unsigned tagB = lds_flag(flg) ^ 0x80000000u;
    f32x4 oacc = {0.f, 0.f, 0.f, 0.f};
#define PVSTEP(t) oacc = mfma(mask & dppu<BCAST + (t)>(pp), V[t], oacc);
    PVSTEP(0) PVSTEP(1) PVSTEP(2) PVSTEP(3) PVSTEP(4) PVSTEP(5) PVSTEP(6) PVSTEP(7)
    PVSTEP(8) PVSTEP(9) PVSTEP(10) PVSTEP(11) PVSTEP(12) PVSTEP(13) PVSTEP(14) PVSTEP(15)
#undef PVSTEP
    red[sw * 64 + lane] = oacc;
    LGKM0();
    if (lane == 0) LDS_INC(flg + 4);
    if (ffn) {
      for (unsigned sp = 0; sp < LSPIN_CAP && lds_flag(flg + 2) == 0u; ++sp) __builtin_amdgcn_s_sleep(1);
      CBAR();
      const u32x2 hv = *(const u32x2*)(hs + (c >> 3) * 64 + 8 * sw + 2 * r);
      f32x4 facc = {0.f, 0.f, 0.f, 0.f};
      facc = mfma(mask & hv[0], W2[0], facc);
      facc = mfma(mask & hv[1], W2[1], facc);
      red[(8 + sw) * 64 + lane] = facc;
      LGKM0();
      if (lane == 0) LDS_INC(flg + 3);
    }
    if (c < 10) {
#pragma unroll
      for (int i = 0; i < 4; ++i) *(u32x4*)(smem + L_WL + (16 * sw + 4 * r + i) * 160 + c * 16) = WO[i];
    }
    const int tro = L_WL + (8 * r + ((lane >> 2) & 3)) * 160 + sw * 32 + (lane & 3) * 8;
    const unsigned yoff = lane < 16 ? cu * 1280 + (unsigned)(sw * 256 + lane * 16) : OOB;
    wg_barrier();
    if (sw < 5) {
      u32x4 A[4], B[4];
#pragma unroll
      for (int t = 0; t < 4; ++t) {
        A[t] = *(const u32x4*)(os + (c & 1) * 64 + t * 16 + r * 4);
        const u32x2 lo = trread(smem + tro + t * 5120), hi = trread(smem + tro + t * 5120 + 640);
        B[t] = u32x4{lo[0], lo[1], hi[0], hi[1]};
      }
      f32x4 y = {0.f, 0.f, 0.f, 0.f}, y2 = {0.f, 0.f, 0.f, 0.f};
      y = mfma(A[0], B[0], y); y2 = mfma(A[1], B[1], y2);
      y = mfma(A[2], B[2], y); y2 = mfma(A[3], B[3], y2);
      ST128(gran2(y[0] + y2[0], y[1] + y2[1], tagB), rws, yoff, WS_Y, AUX_SC1);
    }
  } else {
    __builtin_amdgcn_s_setprio(3);
    const bool isq = (w == 0);
    const unsigned epoch = __builtin_amdgcn_raw_buffer_load_b32(rws, 0, WS_EPOCH, AUX_SC1);
    u32x2 xs[5];
#pragma unroll
    for (int i = 0; i < 5; ++i) xs[i] = __builtin_amdgcn_raw_buffer_load_b64(rx, (lane >> 5) * 1280 + (lane & 31) * 8, i * 256, 0);
    const int row0 = isq ? 160 * (j >> 1) : 80 * j;
    const int nrow = isq ? 160 : 80;
    unsigned short xr[5];
#pragma unroll
    for (int i = 0; i < 5; ++i) xr[i] = __builtin_amdgcn_raw_buffer_load_b16(rx, (lane >> 5) * 1280 + (lane & 31) * 2, (row0 + 32 * i) * 2, 0);
    u32x4 wa[20];
    {
      const __amdgpu_buffer_rsrc_t rw = isq ? RSRC(wq + h * 128 + (j & 1) * 64, 640 * 4096 * 2 - h * 256 - (j & 1) * 128) : RSRC(w1 + h * 64, ffn ? 640 * 1728 * 2 - h * 128 : 0);
      const int rstride = isq ? 8192 : 3456;
      const unsigned vo = (unsigned)((lane >> 3) * rstride + (lane & 7) * 16);
#pragma unroll
      for (int i = 0; i < 10; ++i) wa[i] = LD128(rw, vo, (row0 + 8 * i) * rstride, AUX_NT);
      if (isq) {
#pragma unroll
        for (int i = 10; i < 20; ++i) wa[i] = LD128(rw, vo, (row0 + 8 * i) * rstride, AUX_NT);
      }
    }
    if (isq && lane < 24) LDS_ST32(st + lane, 0u);
    wg_barrier();
    float xf[20];
#pragma unroll
    for (int i = 0; i < 5; ++i) { xf[4 * i] = bflo(xs[i][0]); xf[4 * i + 1] = bfhi(xs[i][0]); xf[4 * i + 2] = bflo(xs[i][1]); xf[4 * i + 3] = bfhi(xs[i][1]); }
    float sum = 0.f;
#pragma unroll
    for (int i = 0; i < 20; ++i) sum += xf[i];
    sum = x16_sum(row_sum(sum));
    const float mean = sum * (1.f / 640.f);
    float vs = 0.f;
#pragma unroll
    for (int i = 0; i < 20; ++i) { const float d = xf[i] - mean; vs = fmaf(d, d, vs); }
    vs = x16_sum(row_sum(vs));
    const float mb = rbf(mean), den = rbf(sqrtf(rbf(rbf(vs * (1.f / 640.f)) + 1e-5f)));
    unsigned* xw_ = xl + w * 320;
#pragma unroll
    for (int i = 0; i < 5; ++i) {
      const int row = (lane & 31) + 32 * i;
      const float xn = rbf(bflo(xr[i]) - mb) / den;
      if (row < nrow) xw_[(lane >> 5) * 160 + ((row >> 1) & 3) * 40 + (row >> 3) * 2 + (row & 1)] = pk_bf16(xn, xn);
    }
    const unsigned tag = __builtin_amdgcn_readfirstlane(epoch) + 0x9E3779B9u, tagB = tag ^ 0x80000000u;
    if (isq && lane == 0) LDS_ST32(flg, tag);
    LGKM0();
    f32x4 acc0 = {0.f, 0.f, 0.f, 0.f}, acc1 = {0.f, 0.f, 0.f, 0.f};
    const unsigned* xrd = xw_ + (c >> 3) * 160 + r * 40;
#pragma unroll
    for (int i = 0; i < 10; i += 2) {
      const u32x4 xa = *(const u32x4*)(xrd + 2 * i);
      acc0 = mfma(mask & xa[0], wa[i], acc0); acc1 = mfma(mask & xa[1], wa[i], acc1);
      acc0 = mfma(mask & xa[2], wa[i + 1], acc0); acc1 = mfma(mask & xa[3], wa[i + 1], acc1);
    }
    if (isq) {
#pragma unroll
      for (int i = 10; i < 20; i += 2) {
        const u32x4 xa = *(const u32x4*)(xrd + 2 * i);
        acc0 = mfma(mask & xa[0], wa[i], acc0); acc1 = mfma(mask & xa[1], wa[i], acc1);
        acc0 = mfma(mask & xa[2], wa[i + 1], acc0); acc1 = mfma(mask & xa[3], wa[i + 1], acc1);
      }
    }
    f32x4 v;
#pragma unroll
    for (int i = 0; i < 4; ++i) { const float t_ = c < 8 ? acc0[i] : acc1[i]; v[i] = t_ + dppf<ROR8>(t_); }
    const u32x4 pub = gran2(c < 8 ? v[0] : v[2], c < 8 ? v[1] : v[3], tag);
    if (isq) {
      ST128(pub, rws, cu * 1024 + lane * 16, WS_Q1, AUX_SC1);
      __builtin_amdgcn_s_sleep(PDELAY);
      const unsigned goff = (unsigned)((h * 8 + (lane >> 5)) * 1024 + (b * 32 + (lane & 31)) * 16);
      u32x4 G[8];
#pragma unroll
      for (int p = 0; p < 4; ++p) G[p] = LD128(rws, goff, WS_Q1 + p * 2048, AUX_SC1);
      for (unsigned sp = 0;; ++sp) {
        bool allok = true;
        CBAR();
#pragma unroll
        for (int p = 0; p < 4; ++p) if (!__all(gok(G[p], tag))) { allok = false; G[p] = LD128(rws, goff, WS_Q1 + p * 2048, AUX_SC1); }
        if (allok || sp > SPIN_CAP) break;
      }
      qs[(lane >> 5) * 32 + (c & 7) * 4 + (r & 1) * 2 + (c >> 3)] = pk_bf16((gv0(G[0]) + gv0(G[1])) + (gv0(G[2]) + gv0(G[3])), (gv1(G[0]) + gv1(G[1])) + (gv1(G[2]) + gv1(G[3])));
      ST128(gran2(0.f, 0.f, tag), rws, lane == 0 ? cu * 128 + 16 : OOB, WS_S, AUX_SC1);
      LGKM0();
      if (lane == 0) LDS_ST32(flg + 1, 1u);
      const unsigned soff = WS_S + (unsigned)((h * 8 + (lane & 7)) * 128 + (lane & 8) * 2);
      __builtin_amdgcn_s_sleep(ACKDELAY);
      u32x4 gs = LD128(rws, soff, 0, AUX_SC1);
      unsigned long long t_ = LDS_LD64(st + 2 * (lane & 7));
      for (unsigned sp = 0; !__all((unsigned)(t_ >> 32) != 0u) && sp < LSPIN_CAP; ++sp) { __builtin_amdgcn_s_sleep(2); t_ = LDS_LD64(st + 2 * (lane & 7)); }
      {
        const float m_ = __uint_as_float((unsigned)t_), l_ = __uint_as_float((unsigned)(t_ >> 32));
        const float M = oct_max(m_);
        float L = __builtin_amdgcn_exp2f((m_ - M) * LOG2E) * l_; L += dppf<XOR1>(L); L += dppf<XOR2>(L); L += dppf<XOR7>(L);
        ST128(gran2(M, L, tag), rws, lane == 0 ? cu * 128 : OOB, WS_S, AUX_SC1);
      }
      if (!__all(gok(gs, tag) | !(lane & 8))) {
        for (unsigned sp = 0; sp < SPIN_CAP; ++sp) {
          CBAR();
          gs = LD128(rws, soff, 0, AUX_SC1);
          if (__all(gok(gs, tag) | !(lane & 8))) break;
        }
      }
      for (unsigned sp = 0; sp < LSPIN_CAP && lds_flag(flg + 4) != 8u; ++sp) {}
      CBAR();
      const unsigned ooff = (unsigned)(h * 8 * 1024 + lane * 16);
      const f32x2 own = sum8((const float*)red + (((lane >> 1) & 1) * 16 + (lane >> 2)) * 4 + 2 * (lane & 1));
      const u32x4 ownG = gran2(own[0], own[1], tagB);
      ST128(ownG, rws, ooff, WS_O + j * 1024, AUX_SC1);
      gs = LD128(rws, soff, 0, AUX_SC1);
#pragma unroll
      for (int p = 0; p < 8; ++p) G[p] = LD128(rws, p == j ? OOB : ooff, WS_O + p * 1024, AUX_SC1);
#pragma unroll
      for (int p = 0; p < 8; ++p) if (p == j) G[p] = ownG;
      for (unsigned sp = 0; !__all(gok(gs, tag)) && sp < SPIN_CAP; ++sp) { CBAR(); __builtin_amdgcn_s_sleep(2); gs = LD128(rws, soff, 0, AUX_SC1); }
      float wp[8];
      {
        const float ms = gv0(gs), ls = gv1(gs);
        float Mg = fmaxf(ms, dppf<XOR1>(ms)); Mg = fmaxf(Mg, dppf<XOR2>(Mg));
        const float e_ = __builtin_amdgcn_exp2f((ms - Mg) * LOG2E);
        float Lg = e_ * ls; Lg += dppf<XOR1>(Lg); Lg += dppf<XOR2>(Lg);
        const float wgt = e_ / Lg;
#pragma unroll
        for (int p = 0; p < 8; ++p) wp[p] = __uint_as_float(__builtin_amdgcn_readlane(__float_as_uint(wgt), p));
      }
      for (unsigned sp = 0;; ++sp) {
        bool allok = true;
        CBAR();
#pragma unroll
        for (int p = 0; p < 8; ++p) if (!__all(gok(G[p], tagB))) { allok = false; G[p] = LD128(rws, ooff, WS_O + p * 1024, AUX_SC1); }
        if (allok || sp > SPIN_CAP) break;
      }
#pragma unroll
      for (int bb = 0; bb < 2; ++bb) {
        float o0 = 0.f, o1 = 0.f;
#pragma unroll
        for (int sp = 0; sp < 4; ++sp) { o0 = fmaf(wp[4 * bb + sp], gv0(G[4 * bb + sp]), o0); o1 = fmaf(wp[4 * bb + sp], gv1(G[4 * bb + sp]), o1); }
        os[bb * 64 + lane] = pk_bf16(o0, o1);
      }
      wg_barrier();
    } else {
      if (ffn) {
        ST128(pub, rws, (unsigned)((h * 8 + (c & 7)) * 1280 + j * 128 + (c >> 3) * 64 + r * 16), WS_H1, AUX_SC1);
        __builtin_amdgcn_s_sleep(2 * PDELAY);
        const unsigned goff = cu * 1280 + lane * 16;
        u32x4 G0 = LD128(rws, goff, WS_H1, AUX_SC1);
        for (unsigned sp = 0; !__all(gok(G0, tag)) && sp < SPIN_CAP; ++sp) { CBAR(); G0 = LD128(rws, goff, WS_H1, AUX_SC1); }
        float a0 = gv0(G0), a1 = gv1(G0);
        a0 += dppf<ROR8>(a0); a1 += dppf<ROR8>(a1);
        a0 = fmaxf(x32_sum(x16_sum(a0)), 0.f); a1 = fmaxf(x32_sum(x16_sum(a1)), 0.f);
        u32x2 g2; g2[0] = pk_bf16(a0, a1); g2[1] = tag;
        __builtin_amdgcn_raw_buffer_store_b64(g2, rws, lane < 8 ? cu * 128 + lane * 8 : OOB, WS_H2, AUX_SC1);
        const unsigned g2off = (unsigned)((h * 8 + (lane >> 3)) * 128 + (lane & 7) * 8);
        __builtin_amdgcn_s_sleep(3 * PDELAY);
        u32x2 q2 = __builtin_amdgcn_raw_buffer_load_b64(rws, g2off, WS_H2, AUX_SC1);
        for (unsigned sp = 0; !__all(q2[1] == tag) && sp < SPIN_CAP; ++sp) { CBAR(); q2 = __builtin_amdgcn_raw_buffer_load_b64(rws, g2off, WS_H2, AUX_SC1); }
        u32x2 hv; hv[0] = (q2[0] & 0xffffu) | (q2[0] << 16); hv[1] = (q2[0] >> 16) | (q2[0] & 0xffff0000u);
        *(u32x2*)(hs + ((lane >> 1) & 1) * 64 + (lane >> 3) * 8 + (lane & 1) * 4 + ((lane >> 2) & 1) * 2) = hv;
        LGKM0();
        if (lane == 0) LDS_ST32(flg + 2, 1u);
        for (unsigned sp = 0; sp < LSPIN_CAP && lds_flag(flg + 3) != 8u; ++sp) __builtin_amdgcn_s_sleep(1);
        CBAR();
        f_publish(0, tag);
        f_publish(8, tag);
      }
      const int e = lane & 7, ocol = j * 80 + 8 * h + e;
      const bool fin = h < 10, f3v = (lane >> 3) < 3;
      const unsigned yo = (unsigned)(((lane >> 3) * 8 + j) * 1280 + h * 128 + e * 16);
      unsigned short x0 = 0, x1 = 0;
      u32x4 g[4], y[4];
      if (fin) { x0 = __builtin_amdgcn_raw_buffer_load_b16(rx, ocol * 2, 0, 0); x1 = __builtin_amdgcn_raw_buffer_load_b16(rx, ocol * 2, 1280, 0); }
      wg_barrier();
      if (fin) {
#pragma unroll
        for (int i = 0; i < 4; ++i) g[i] = LD128(rws, (i < 3 || f3v) ? yo : OOB, WS_F + i * 64 * 1280, AUX_SC1);
        if (YDELAY) __builtin_amdgcn_s_sleep(YDELAY);
#pragma unroll
        for (int i = 0; i < 4; ++i) y[i] = LD128(rws, yo, WS_Y + i * 64 * 1280, AUX_SC1);
        for (unsigned sp = 0;; ++sp) {
          bool allok = true;
          CBAR();
#pragma unroll
          for (int i = 0; i < 4; ++i) if (!__all(gok(g[i], tag) | (i == 3 && !f3v))) { allok = false; g[i] = LD128(rws, (i < 3 || f3v) ? yo : OOB, WS_F + i * 64 * 1280, AUX_SC1); }
          if (allok || sp > SPIN_CAP) break;
        }
        float fa = (gv0(g[0]) + gv0(g[1])) + (gv0(g[2]) + gv0(g[3])), fb = (gv1(g[0]) + gv1(g[1])) + (gv1(g[2]) + gv1(g[3]));
        fa += dppf<ROR8>(fa); fb += dppf<ROR8>(fb);
        fa = rbf(x32_sum(x16_sum(fa))); fb = rbf(x32_sum(x16_sum(fb)));
        const float ga = rbf(fa / (1.f + __expf(-fa))), gb = rbf(fb / (1.f + __expf(-fb)));
        for (unsigned sp = 0;; ++sp) {
          bool allok = true;
          CBAR();
#pragma unroll
          for (int i = 0; i < 4; ++i) if (!__all(gok(y[i], tagB))) { if (YSLEEP && allok) __builtin_amdgcn_s_sleep(YSLEEP); allok = false; y[i] = LD128(rws, yo, WS_Y + i * 64 * 1280, AUX_SC1); }
          if (allok || sp > SPIN_CAP) break;
        }
        float ya = (gv0(y[0]) + gv0(y[1])) + (gv0(y[2]) + gv0(y[3])), yb = (gv1(y[0]) + gv1(y[1])) + (gv1(y[2]) + gv1(y[3]));
        ya += dppf<ROR8>(ya); yb += dppf<ROR8>(yb);
        ya = rbf(x32_sum(x16_sum(ya))); yb = rbf(x32_sum(x16_sum(yb)));
        const unsigned vv = pk_bf16(bflo(x0) + rbf(ya * ga), bflo(x1) + rbf(yb * gb)), nb = dppu<XOR1>(vv);
        const unsigned res = (e & 1) ? ((nb >> 16) | (vv & 0xffff0000u)) : ((vv & 0xffffu) | (nb << 16));
        __builtin_amdgcn_raw_buffer_store_b32(res, RSRC(out, 2560), lane < 8 ? (unsigned)(((e & 1) * 640 + (ocol & ~1)) * 2) : OOB, 0, 0);
        if (blk == 0 && lane == 0) __builtin_amdgcn_raw_buffer_store_b32(tag, rws, 0, WS_EPOCH, AUX_SC1);
      }
    }
  }
  STAMP_FLUSH();
}

extern "C" void kernel_launch(void* const* d_in, const int* in_sizes, int n_in, void* d_out, int out_size, void* d_ws, size_t ws_size, hipStream_t stream) {
  fused_decode<<<256, 640, 0, stream>>>((const unsigned short*)d_in[0], (const unsigned short*)d_in[1], (const unsigned short*)d_in[2],
      (const unsigned short*)d_in[3], (const unsigned short*)d_in[4], (const unsigned short*)d_in[6],
      (unsigned char*)d_ws, (const unsigned short*)d_in[7], (unsigned short*)d_out);
}
```

```cpp
#define ACKDELAY 32
#define ROTM 53
#define REDW_ 1
#include <hip/hip_runtime.h>
#include <hip/hip_bf16.h>
#include <stdint.h>

#define STAMP(i) do {} while (0)
#define STAMP_DECL
#define STAMP_FLUSH() do {} while (0)

typedef unsigned u32x4 __attribute__((ext_vector_type(4)));
typedef unsigned u32x2 __attribute__((ext_vector_type(2)));
typedef float f32x4 __attribute__((ext_vector_type(4)));
typedef float f32x2 __attribute__((ext_vector_type(2)));
typedef __bf16 bf16x8 __attribute__((ext_vector_type(8)));
typedef __bf16 bf16x2 __attribute__((ext_vector_type(2)));
typedef short s16x4 __attribute__((ext_vector_type(4)));

#define PDELAY 32
#define YSLEEP 0
#define YDELAY 4
constexpr int AUX_NT = 2, AUX_SC1 = 16;
constexpr unsigned OOB = 0x7ffffff0u;
constexpr unsigned WS_EPOCH = 0;
constexpr unsigned WS_Q1 = 4096;
constexpr unsigned WS_H1 = WS_Q1 + 256 * 1024;
constexpr unsigned WS_H2 = WS_H1 + 256 * 1280;
constexpr unsigned WS_S = WS_H2 + 256 * 128;
constexpr unsigned WS_F = WS_S + 256 * 128;
constexpr unsigned WS_END = WS_F + 256 * 1280;
constexpr unsigned WS_O = WS_Q1, WS_Y = WS_H1;
constexpr unsigned SPIN_CAP = 1u << 16, LSPIN_CAP = 1u << 22;
constexpr int CODE_BYTES = 13312;
constexpr int L_RED = 0, L_WL = 16384, L_QS = L_WL + 20480, L_HS = L_QS + 256, L_ST = L_HS + 512, L_FLG = L_ST + 64, L_XL = L_FLG + 64, L_OS = L_XL + 2560, L_END = L_OS + 512;

#define RSRC(p, n) __builtin_amdgcn_make_buffer_rsrc((void*)(p), 0, (n), 0x00020000)
#define LD128(rs, vo, so, aux) __builtin_amdgcn_raw_buffer_load_b128((rs), (vo), (so), (aux))
#define ST128(v, rs, vo, so, aux) __builtin_amdgcn_raw_buffer_store_b128((v), (rs), (vo), (so), (aux))
#define SBAR() __builtin_amdgcn_sched_barrier(0)
#define CBAR() asm volatile("" ::: "memory")
#define LGKM0() asm volatile("s_waitcnt lgkmcnt(0)" ::: "memory")
#define LAS __attribute__((address_space(3)))
#define LDS_LD32(p) __hip_atomic_load((LAS unsigned*)(p), __ATOMIC_RELAXED, __HIP_MEMORY_SCOPE_WORKGROUP)
#define LDS_ST32(p, v) __hip_atomic_store((LAS unsigned*)(p), (v), __ATOMIC_RELAXED, __HIP_MEMORY_SCOPE_WORKGROUP)
#define LDS_LD64(p) __hip_atomic_load((LAS unsigned long long*)(p), __ATOMIC_RELAXED, __HIP_MEMORY_SCOPE_WORKGROUP)
#define LDS_ST64(p, v) __hip_atomic_store((LAS unsigned long long*)(p), (v), __ATOMIC_RELAXED, __HIP_MEMORY_SCOPE_WORKGROUP)
#define LDS_INC(p) asm volatile("ds_add_u32 %0, %1" :: "v"((unsigned)(size_t)(LAS unsigned*)(p)), "v"(1u) : "memory")

template <int CTRL> __device__ __forceinline__ unsigned dppu(unsigned x) { return (unsigned)__builtin_amdgcn_mov_dpp((int)x, CTRL, 0xf, 0xf, true); }
template <int CTRL> __device__ __forceinline__ float dppf(float x) { return __uint_as_float(dppu<CTRL>(__float_as_uint(x))); }
constexpr int XOR1 = 0xB1, XOR2 = 0x4E, XOR7 = 0x141, ROR8 = 0x128, ROR4 = 0x124, SHL4 = 0x104, SHR4 = 0x114, BCAST = 0x150;

__device__ __forceinline__ float dot2(unsigned k, unsigned q, float acc) {
  return __builtin_amdgcn_fdot2_f32_bf16(__builtin_bit_cast(bf16x2, k), __builtin_bit_cast(bf16x2, q), acc, false);
}
__device__ __forceinline__ unsigned pk_bf16(float lo, float hi) { bf16x2 v; v[0] = (__bf16)lo; v[1] = (__bf16)hi; return __builtin_bit_cast(unsigned, v); }
__device__ __forceinline__ float rbf(float f) { return __uint_as_float(pk_bf16(f, f) & 0xffff0000u); }
__device__ __forceinline__ float bflo(unsigned u) { return __uint_as_float(u << 16); }
__device__ __forceinline__ float bfhi(unsigned u) { return __uint_as_float(u & 0xffff0000u); }
__device__ __forceinline__ f32x4 mfma(u32x4 a, u32x4 b, f32x4 c) {
  return __builtin_amdgcn_mfma_f32_16x16x32_bf16(__builtin_bit_cast(bf16x8, a), __builtin_bit_cast(bf16x8, b), c, 0, 0, 0);
}
__device__ __forceinline__ float x16_sum(float x) { auto s = __builtin_amdgcn_permlane16_swap(__float_as_uint(x), __float_as_uint(x), false, false); return __uint_as_float(s[0]) + __uint_as_float(s[1]); }
__device__ __forceinline__ float x32_sum(float x) { auto s = __builtin_amdgcn_permlane32_swap(__float_as_uint(x), __float_as_uint(x), false, false); return __uint_as_float(s[0]) + __uint_as_float(s[1]); }
__device__ __forceinline__ float x16_max(float x) { auto s = __builtin_amdgcn_permlane16_swap(__float_as_uint(x), __float_as_uint(x), false, false); return fmaxf(__uint_as_float(s[0]), __uint_as_float(s[1])); }
__device__ __forceinline__ float x32_max(float x) { auto s = __builtin_amdgcn_permlane32_swap(__float_as_uint(x), __float_as_uint(x), false, false); return fmaxf(__uint_as_float(s[0]), __uint_as_float(s[1])); }
__device__ __forceinline__ float row_sum(float x) { x += dppf<XOR1>(x); x += dppf<XOR2>(x); x += dppf<ROR4>(x); x += dppf<ROR8>(x); return x; }
__device__ __forceinline__ float row_max(float x) { x = fmaxf(x, dppf<XOR1>(x)); x = fmaxf(x, dppf<XOR2>(x)); x = fmaxf(x, dppf<ROR4>(x)); x = fmaxf(x, dppf<ROR8>(x)); return x; }
__device__ __forceinline__ float oct_max(float x) { x = fmaxf(x, dppf<XOR1>(x)); x = fmaxf(x, dppf<XOR2>(x)); return fmaxf(x, dppf<XOR7>(x)); }
template <int C0, int C1> __device__ __forceinline__ float trsel(float lo, float hi, bool bit) {
  const float t0 = lo + dppf<C0>(lo), t1 = hi + dppf<C1>(hi);
  return bit ? t1 : t0;
}
__device__ __forceinline__ bool gok(u32x4 g, unsigned tag) { return (g[1] == tag) & (g[3] == tag); }
__device__ __forceinline__ u32x4 gran2(float a, float b, unsigned tag) { u32x4 g; g[0] = __float_as_uint(a); g[1] = tag; g[2] = __float_as_uint(b); g[3] = tag; return g; }
__device__ __forceinline__ float gv0(u32x4 g) { return __uint_as_float(g[0]); }
__device__ __forceinline__ float gv1(u32x4 g) { return __uint_as_float(g[2]); }
__device__ __forceinline__ void wg_barrier() { asm volatile("s_waitcnt lgkmcnt(0)\n\ts_barrier" ::: "memory"); }
__device__ __forceinline__ unsigned lds_flag(unsigned* p) { return __builtin_amdgcn_readfirstlane(LDS_LD32(p)); }
__device__ __forceinline__ f32x2 sum8(const float* p) {
  f32x2 a[8];
#pragma unroll
  for (int i = 0; i < 8; ++i) a[i] = *(const f32x2*)(p + i * 256);
  return ((a[0] + a[1]) + (a[2] + a[3])) + ((a[4] + a[5]) + (a[6] + a[7]));
}
__device__ __forceinline__ u32x2 trread(unsigned char* p) { return __builtin_bit_cast(u32x2, __builtin_amdgcn_ds_read_tr16_b64_v4i16((LAS s16x4*)p)); }
constexpr float LOG2E = 1.4426950408889634f;

__global__ void __launch_bounds__(640, 3)
fused_decode(const unsigned short* x, const unsigned short* kc, const unsigned short* vc,
             const unsigned short* wq, const unsigned short* wo, const unsigned short* w1,
             unsigned char* ws, const unsigned short* w2, unsigned short* out) {
  unsigned long long pc_entry;
  asm volatile("s_getpc_b64 %0" : "=s"(pc_entry));
  asm volatile("" : "+s"(x), "+s"(kc), "+s"(vc), "+s"(wq), "+s"(wo), "+s"(w1), "+s"(ws), "+s"(w2), "+s"(out));
  __shared__ __attribute__((aligned(16))) unsigned char smem[L_END];
  f32x4* red = (f32x4*)(smem + L_RED);
  unsigned* qs = (unsigned*)(smem + L_QS);
  unsigned* hs = (unsigned*)(smem + L_HS);
  float* st = (float*)(smem + L_ST);
  unsigned* flg = (unsigned*)(smem + L_FLG);
  unsigned* xl = (unsigned*)(smem + L_XL);
  unsigned* os = (unsigned*)(smem + L_OS);
  const int blk = blockIdx.x, tid = threadIdx.x, lane = tid & 63, w = __builtin_amdgcn_readfirstlane(tid >> 6);
  STAMP_DECL
  const unsigned tstart_ = (unsigned)__builtin_amdgcn_s_memrealtime();
  const int j = (blk >> 3) & 7, h = (blk & 7) + 8 * (blk >> 6), b = j >> 2, s = j & 3;
  const int r = lane >> 4, c = lane & 15;
  const bool ffn = h < 27;
  const unsigned cu = (unsigned)(h * 8 + j);
  const __amdgpu_buffer_rsrc_t rws = RSRC(ws, WS_END);
  const __amdgpu_buffer_rsrc_t rx = RSRC(x, 2560);
  u32x4 mask;
#pragma unroll
  for (int i = 0; i < 4; ++i) mask[i] = (i == ((c & 7) >> 1)) ? ((c & 1) ? 0xffff0000u : 0xffffu) : 0u;
  auto f_publish = [&](int cbase, unsigned tg) {
    const int cr = cbase + (lane >> 3), e = lane & 7;
    const float* p = (const float*)(red + 512) + ((e >> 2) * 16 + (cr & 15)) * 4 + (e & 3);
    float a0[8], a1[8];
#pragma unroll
    for (int i = 0; i < 8; ++i) { a0[i] = p[i * 256]; a1[i] = p[i * 256 + 128]; }
    const float v0 = ((a0[0] + a0[1]) + (a0[2] + a0[3])) + ((a0[4] + a0[5]) + (a0[6] + a0[7]));
    const float v1 = ((a1[0] + a1[1]) + (a1[2] + a1[3])) + ((a1[4] + a1[5]) + (a1[6] + a1[7]));
    ST128(gran2(v0, v1, tg), rws, cr < 10 ? cu * 1280 + (unsigned)(cr * 128 + e * 16) : OOB, WS_F, AUX_SC1);
  };

  if (w >= 2) {
    const int sw = w - 2;
    const size_t kvbase = ((size_t)(b * 32 + h) * 8192 + 6144 + s * 512) * 128;
    const __amdgpu_buffer_rsrc_t rk = RSRC(kc + kvbase, 131072), rv = RSRC(vc + kvbase, 131072);
    const int rot = sw * 4 + ((blk * ROTM) & 127);
    const int voff = lane * 16;
#define KVOFF(t) ((((((t) >> 2) * 32 + ((t) & 3) + rot) & 127) << 10))
    u32x4 K[16], V[16], WO[4], W2[2];
    float sc = 0.f;
    auto scores = [&]() {
      CBAR();
      const u32x4 q4 = *(const u32x4*)(qs + 4 * c);
      float sb[4];
#pragma unroll
      for (int be = 0; be < 4; ++be) {
        float d[4];
#pragma unroll
        for (int tt = 0; tt < 4; ++tt) {
          const u32x4 kk = K[4 * be + tt];
          d[tt] = dot2(kk[3], q4[3], dot2(kk[2], q4[2], dot2(kk[1], q4[1], dot2(kk[0], q4[0], 0.f))));
        }
        const float e0 = trsel<XOR1, XOR1>(d[0], d[1], c & 1);
        const float e1 = trsel<XOR1, XOR1>(d[2], d[3], c & 1);
        sb[be] = trsel<XOR2, XOR2>(e0, e1, c & 2);
      }
      const float f0 = trsel<SHL4, SHR4>(sb[0], sb[1], c & 4);
      const float f1 = trsel<SHL4, SHR4>(sb[2], sb[3], c & 4);
      const float dotv = trsel<ROR8, ROR8>(f0, f1, c & 8);
      sc = rbf(rbf(dotv) * 0.08838834764831845f);
      const float mw = x32_max(x16_max(row_max(sc)));
      const float lw = x32_sum(x16_sum(row_sum(__builtin_amdgcn_exp2f((sc - mw) * LOG2E))));
      if (lane == 0) LDS_ST64(st + 2 * sw, (unsigned long long)__float_as_uint(mw) | ((unsigned long long)__float_as_uint(lw) << 32));
    };
    unsigned cp0 = 0, cp1 = 0;
    if (sw == 7 && (blk >> 3) < 4) {
      const __amdgpu_buffer_rsrc_t rc = RSRC((void*)(pc_entry & ~255ull), CODE_BYTES);
      cp0 = __builtin_amdgcn_raw_buffer_load_b32(rc, lane * 128, 0, 0);
      cp1 = __builtin_amdgcn_raw_buffer_load_b32(rc, lane * 128, 8192, 0);
    }
    __builtin_amdgcn_s_setprio(3);
    wg_barrier();
#pragma unroll
    for (int i = 0; i < 24; ++i) {
      if (i == 16) __builtin_amdgcn_s_setprio(2);
      if (i < 16) K[i] = LD128(rk, voff, KVOFF(i), AUX_NT); else V[i - 16] = LD128(rv, voff, KVOFF(i - 16), AUX_NT);
      SBAR();
    }
    const bool early = lds_flag(flg + 1) != 0u;
    if (early) scores();
    SBAR();
    __builtin_amdgcn_s_setprio(1);
#pragma unroll
    for (int t = 8; t < 16; ++t) { V[t] = LD128(rv, voff, KVOFF(t), AUX_NT); SBAR(); }
    if (!early) {
      for (unsigned sp = 0; sp < LSPIN_CAP && lds_flag(flg + 1) == 0u; ++sp) __builtin_amdgcn_s_sleep(1);
      scores();
    }
    SBAR();
    __builtin_amdgcn_s_setprio(0);
    {
      const __amdgpu_buffer_rsrc_t ro = RSRC(wo + (size_t)h * 128 * 640 + j * 80, 128 * 1280 - j * 160);
      const __amdgpu_buffer_rsrc_t r2 = RSRC(w2 + (size_t)h * 64 * 640 + j * 80, ffn ? 64 * 1280 - j * 160 : 0);
      const unsigned vo4 = c < 10 ? (unsigned)(r * 4 * 1280 + c * 16) : OOB;
      const unsigned vo2 = c < 10 ? (unsigned)(r * 2 * 1280 + c * 16) : OOB;
#pragma unroll
      for (int i = 0; i < 4; ++i) { WO[i] = LD128(ro, vo4, (16 * sw + i) * 1280, AUX_NT); SBAR(); }
#pragma unroll
      for (int i = 0; i < 2; ++i) { W2[i] = LD128(r2, vo2, (8 * sw + i) * 1280, AUX_NT); SBAR(); }
    }
    asm volatile("" :: "v"(cp0), "v"(cp1));
    unsigned pp;
    {
      unsigned long long t_ = LDS_LD64(st + 2 * (lane & 7));
      for (unsigned sp = 0; !__all((unsigned)(t_ >> 32) != 0u) && sp < LSPIN_CAP; ++sp) { __builtin_amdgcn_s_sleep(1); t_ = LDS_LD64(st + 2 * (lane & 7)); }
      const float M = oct_max(__uint_as_float((unsigned)t_));
      const float pv = __builtin_amdgcn_exp2f((sc - M) * LOG2E);
      pp = pk_bf16(pv, pv);
    }
    const unsigned tagB = lds_flag(flg) ^ 0x80000000u;
    f32x4 oacc = {0.f, 0.f, 0.f, 0.f};
#define PVSTEP(t) oacc = mfma(mask & dppu<BCAST + (t)>(pp), V[t], oacc);
    PVSTEP(0) PVSTEP(1) PVSTEP(2) PVSTEP(3) PVSTEP(4) PVSTEP(5) PVSTEP(6) PVSTEP(7)
    PVSTEP(8) PVSTEP(9) PVSTEP(10) PVSTEP(11) PVSTEP(12) PVSTEP(13) PVSTEP(14) PVSTEP(15)
#undef PVSTEP
    red[sw * 64 + lane] = oacc;
    LGKM0();
    if (lane == 0) LDS_INC(flg + 4);
    if (ffn) {
      for (unsigned sp = 0; sp < LSPIN_CAP && lds_flag(flg + 2) == 0u; ++sp) __builtin_amdgcn_s_sleep(1);
      CBAR();
      const u32x2 hv = *(const u32x2*)(hs + (c >> 3) * 64 + 8 * sw + 2 * r);
      f32x4 facc = {0.f, 0.f, 0.f, 0.f};
      facc = mfma(mask & hv[0], W2[0], facc);
      facc = mfma(mask & hv[1], W2[1], facc);
      red[(8 + sw) * 64 + lane] = facc;
      LGKM0();
      if (lane == 0) LDS_INC(flg + 3);
    }
    if (c < 10) {
#pragma unroll
      for (int i = 0; i < 4; ++i) *(u32x4*)(smem + L_WL + (16 * sw + 4 * r + i) * 160 + c * 16) = WO[i];
    }
    const int tro = L_WL + (8 * r + ((lane >> 2) & 3)) * 160 + sw * 32 + (lane & 3) * 8;
    const unsigned yoff = lane < 16 ? cu * 1280 + (unsigned)(sw * 256 + lane * 16) : OOB;
    wg_barrier();
    if (sw < 5) {
      u32x4 A[4], B[4];
#pragma unroll
      for (int t = 0; t < 4; ++t) {
        A[t] = *(const u32x4*)(os + (c & 1) * 64 + t * 16 + r * 4);
        const u32x2 lo = trread(smem + tro + t * 5120), hi = trread(smem + tro + t * 5120 + 640);
        B[t] = u32x4{lo[0], lo[1], hi[0], hi[1]};
      }
      f32x4 y = {0.f, 0.f, 0.f, 0.f}, y2 = {0.f, 0.f, 0.f, 0.f};
      y = mfma(A[0], B[0], y); y2 = mfma(A[1], B[1], y2);
      y = mfma(A[2], B[2], y); y2 = mfma(A[3], B[3], y2);
      ST128(gran2(y[0] + y2[0], y[1] + y2[1], tagB), rws, yoff, WS_Y, AUX_SC1);
    }
  } else {
    __builtin_amdgcn_s_setprio(3);
    const bool isq = (w == 0);
    const unsigned epoch = __builtin_amdgcn_raw_buffer_load_b32(rws, 0, WS_EPOCH, AUX_SC1);
    u32x2 xs[5];
#pragma unroll
    for (int i = 0; i < 5; ++i) xs[i] = __builtin_amdgcn_raw_buffer_load_b64(rx, (lane >> 5) * 1280 + (lane & 31) * 8, i * 256, 0);
    const int row0 = isq ? 160 * (j >> 1) : 80 * j;
    const int nrow = isq ? 160 : 80;
    unsigned short xr[5];
#pragma unroll
    for (int i = 0; i < 5; ++i) xr[i] = __builtin_amdgcn_raw_buffer_load_b16(rx, (lane >> 5) * 1280 + (lane & 31) * 2, (row0 + 32 * i) * 2, 0);
    u32x4 wa[20];
    {
      const __amdgpu_buffer_rsrc_t rw = isq ? RSRC(wq + h * 128 + (j & 1) * 64, 640 * 4096 * 2 - h * 256 - (j & 1) * 128) : RSRC(w1 + h * 64, ffn ? 640 * 1728 * 2 - h * 128 : 0);
      const int rstride = isq ? 8192 : 3456;
      const unsigned vo = (unsigned)((lane >> 3) * rstride + (lane & 7) * 16);
#pragma unroll
      for (int i = 0; i < 10; ++i) wa[i] = LD128(rw, vo, (row0 + 8 * i) * rstride, AUX_NT);
      if (isq) {
#pragma unroll
        for (int i = 10; i < 20; ++i) wa[i] = LD128(rw, vo, (row0 + 8 * i) * rstride, AUX_NT);
      }
    }
    if (isq && lane < 24) LDS_ST32(st + lane, 0u);
    wg_barrier();
    float xf[20];
#pragma unroll
    for (int i = 0; i < 5; ++i) { xf[4 * i] = bflo(xs[i][0]); xf[4 * i + 1] = bfhi(xs[i][0]); xf[4 * i + 2] = bflo(xs[i][1]); xf[4 * i + 3] = bfhi(xs[i][1]); }
    float sum = 0.f;
#pragma unroll
    for (int i = 0; i < 20; ++i) sum += xf[i];
    sum = x16_sum(row_sum(sum));
    const float mean = sum * (1.f / 640.f);
    float vs = 0.f;
#pragma unroll
    for (int i = 0; i < 20; ++i) { const float d = xf[i] - mean; vs = fmaf(d, d, vs); }
    vs = x16_sum(row_sum(vs));
    const float mb = rbf(mean), den = rbf(sqrtf(rbf(rbf(vs * (1.f / 640.f)) + 1e-5f)));
    unsigned* xw_ = xl + w * 320;
#pragma unroll
    for (int i = 0; i < 5; ++i) {
      const int row = (lane & 31) + 32 * i;
      const float xn = rbf(bflo(xr[i]) - mb) / den;
      if (row < nrow) xw_[(lane >> 5) * 160 + ((row >> 1) & 3) * 40 + (row >> 3) * 2 + (row & 1)] = pk_bf16(xn, xn);
    }
    const unsigned tag = __builtin_amdgcn_readfirstlane(epoch) + 0x9E3779B9u, tagB = tag ^ 0x80000000u;
    if (isq && lane == 0) LDS_ST32(flg, tag);
    LGKM0();
    f32x4 acc0 = {0.f, 0.f, 0.f, 0.f}, acc1 = {0.f, 0.f, 0.f, 0.f};
    const unsigned* xrd = xw_ + (c >> 3) * 160 + r * 40;
#pragma unroll
    for (int i = 0; i < 10; i += 2) {
      const u32x4 xa = *(const u32x4*)(xrd + 2 * i);
      acc0 = mfma(mask & xa[0], wa[i], acc0); acc1 = mfma(mask & xa[1], wa[i], acc1);
      acc0 = mfma(mask & xa[2], wa[i + 1], acc0); acc1 = mfma(mask & xa[3], wa[i + 1], acc1);
    }
    if (isq) {
#pragma unroll
      for (int i = 10; i < 20; i += 2) {
        const u32x4 xa = *(const u32x4*)(xrd + 2 * i);
        acc0 = mfma(mask & xa[0], wa[i], acc0); acc1 = mfma(mask & xa[1], wa[i], acc1);
        acc0 = mfma(mask & xa[2], wa[i + 1], acc0); acc1 = mfma(mask & xa[3], wa[i + 1], acc1);
      }
    }
    f32x4 v;
#pragma unroll
    for (int i = 0; i < 4; ++i) { const float t_ = c < 8 ? acc0[i] : acc1[i]; v[i] = t_ + dppf<ROR8>(t_); }
    const u32x4 pub = gran2(c < 8 ? v[0] : v[2], c < 8 ? v[1] : v[3], tag);
    if (isq) {
      ST128(pub, rws, cu * 1024 + lane * 16, WS_Q1, AUX_SC1);
      __builtin_amdgcn_s_sleep(PDELAY);
      const unsigned goff = (unsigned)((h * 8 + (lane >> 5)) * 1024 + (b * 32 + (lane & 31)) * 16);
      u32x4 G[8];
#pragma unroll
      for (int p = 0; p < 4; ++p) G[p] = LD128(rws, goff, WS_Q1 + p * 2048, AUX_SC1);
      for (unsigned sp = 0;; ++sp) {
        bool allok = true;
        CBAR();
#pragma unroll
        for (int p = 0; p < 4; ++p) if (!__all(gok(G[p], tag))) { allok = false; G[p] = LD128(rws, goff, WS_Q1 + p * 2048, AUX_SC1); }
        if (allok || sp > SPIN_CAP) break;
      }
      qs[(lane >> 5) * 32 + (c & 7) * 4 + (r & 1) * 2 + (c >> 3)] = pk_bf16((gv0(G[0]) + gv0(G[1])) + (gv0(G[2]) + gv0(G[3])), (gv1(G[0]) + gv1(G[1])) + (gv1(G[2]) + gv1(G[3])));
      ST128(gran2(0.f, 0.f, tag), rws, lane == 0 ? cu * 128 + 16 : OOB, WS_S, AUX_SC1);
      LGKM0();
      if (lane == 0) LDS_ST32(flg + 1, 1u);
      const unsigned soff = WS_S + (unsigned)((h * 8 + (lane & 7)) * 128 + (lane & 8) * 2);
      __builtin_amdgcn_s_sleep(ACKDELAY);
      u32x4 gs = LD128(rws, soff, 0, AUX_SC1);
      unsigned long long t_ = LDS_LD64(st + 2 * (lane & 7));
      for (unsigned sp = 0; !__all((unsigned)(t_ >> 32) != 0u) && sp < LSPIN_CAP; ++sp) { __builtin_amdgcn_s_sleep(2); t_ = LDS_LD64(st + 2 * (lane & 7)); }
      {
        const float m_ = __uint_as_float((unsigned)t_), l_ = __uint_as_float((unsigned)(t_ >> 32));
        const float M = oct_max(m_);
        float L = __builtin_amdgcn_exp2f((m_ - M) * LOG2E) * l_; L += dppf<XOR1>(L); L += dppf<XOR2>(L); L += dppf<XOR7>(L);
        ST128(gran2(M, L, tag), rws, lane == 0 ? cu * 128 : OOB, WS_S, AUX_SC1);
      }
      if (!__all(gok(gs, tag) | !(lane & 8))) {
        for (unsigned sp = 0; sp < SPIN_CAP; ++sp) {
          CBAR();
          gs = LD128(rws, soff, 0, AUX_SC1);
          if (__all(gok(gs, tag) | !(lane & 8))) break;
        }
      }
      for (unsigned sp = 0; sp < LSPIN_CAP && lds_flag(flg + 4) != 8u; ++sp) {}
      CBAR();
      const unsigned ooff = (unsigned)(h * 8 * 1024 + lane * 16);
      const f32x2 own = sum8((const float*)red + (((lane >> 1) & 1) * 16 + (lane >> 2)) * 4 + 2 * (lane & 1));
      const u32x4 ownG = gran2(own[0], own[1], tagB);
      ST128(ownG, rws, ooff, WS_O + j * 1024, AUX_SC1);
      gs = LD128(rws, soff, 0, AUX_SC1);
#pragma unroll
      for (int p = 0; p < 8; ++p) G[p] = LD128(rws, p == j ? OOB : ooff, WS_O + p * 1024, AUX_SC1);
#pragma unroll
      for (int p = 0; p < 8; ++p) if (p == j) G[p] = ownG;
      for (unsigned sp = 0; !__all(gok(gs, tag)) && sp < SPIN_CAP; ++sp) { CBAR(); __builtin_amdgcn_s_sleep(2); gs = LD128(rws, soff, 0, AUX_SC1); }
      float wp[8];
      {
        const float ms = gv0(gs), ls = gv1(gs);
        float Mg = fmaxf(ms, dppf<XOR1>(ms)); Mg = fmaxf(Mg, dppf<XOR2>(Mg));
        const float e_ = __builtin_amdgcn_exp2f((ms - Mg) * LOG2E);
        float Lg = e_ * ls; Lg += dppf<XOR1>(Lg); Lg += dppf<XOR2>(Lg);
        const float wgt = e_ / Lg;
#pragma unroll
        for (int p = 0; p < 8; ++p) wp[p] = __uint_as_float(__builtin_amdgcn_readlane(__float_as_uint(wgt), p));
      }
      for (unsigned sp = 0;; ++sp) {
        bool allok = true;
        CBAR();
#pragma unroll
        for (int p = 0; p < 8; ++p) if (!__all(gok(G[p], tagB))) { allok = false; G[p] = LD128(rws, ooff, WS_O + p * 1024, AUX_SC1); }
        if (allok || sp > SPIN_CAP) break;
      }
#pragma unroll
      for (int bb = 0; bb < 2; ++bb) {
        float o0 = 0.f, o1 = 0.f;
#pragma unroll
        for (int sp = 0; sp < 4; ++sp) { o0 = fmaf(wp[4 * bb + sp], gv0(G[4 * bb + sp]), o0); o1 = fmaf(wp[4 * bb + sp], gv1(G[4 * bb + sp]), o1); }
        os[bb * 64 + lane] = pk_bf16(o0, o1);
      }
      wg_barrier();
    } else {
      if (ffn) {
        ST128(pub, rws, (unsigned)((h * 8 + (c & 7)) * 1280 + j * 128 + (c >> 3) * 64 + r * 16), WS_H1, AUX_SC1);
        __builtin_amdgcn_s_sleep(2 * PDELAY);
        const unsigned goff = cu * 1280 + lane * 16;
        u32x4 G0 = LD128(rws, goff, WS_H1, AUX_SC1);
        for (unsigned sp = 0; !__all(gok(G0, tag)) && sp < SPIN_CAP; ++sp) { CBAR(); G0 = LD128(rws, goff, WS_H1, AUX_SC1); }
        float a0 = gv0(G0), a1 = gv1(G0);
        a0 += dppf<ROR8>(a0); a1 += dppf<ROR8>(a1);
        a0 = fmaxf(x32_sum(x16_sum(a0)), 0.f); a1 = fmaxf(x32_sum(x16_sum(a1)), 0.f);
        u32x2 g2; g2[0] = pk_bf16(a0, a1); g2[1] = tag;
        __builtin_amdgcn_raw_buffer_store_b64(g2, rws, lane < 8 ? cu * 128 + lane * 8 : OOB, WS_H2, AUX_SC1);
        const unsigned g2off = (unsigned)((h * 8 + (lane >> 3)) * 128 + (lane & 7) * 8);
        __builtin_amdgcn_s_sleep(3 * PDELAY);
        u32x2 q2 = __builtin_amdgcn_raw_buffer_load_b64(rws, g2off, WS_H2, AUX_SC1);
        for (unsigned sp = 0; !__all(q2[1] == tag) && sp < SPIN_CAP; ++sp) { CBAR(); q2 = __builtin_amdgcn_raw_buffer_load_b64(rws, g2off, WS_H2, AUX_SC1); }
        u32x2 hv; hv[0] = (q2[0] & 0xffffu) | (q2[0] << 16); hv[1] = (q2[0] >> 16) | (q2[0] & 0xffff0000u);
        *(u32x2*)(hs + ((lane >> 1) & 1) * 64 + (lane >> 3) * 8 + (lane & 1) * 4 + ((lane >> 2) & 1) * 2) = hv;
        LGKM0();
        if (lane == 0) LDS_ST32(flg + 2, 1u);
        for (unsigned sp = 0; sp < LSPIN_CAP && lds_flag(flg + 3) != 8u; ++sp) __builtin_amdgcn_s_sleep(1);
        CBAR();
        f_publish(0, tag);
        f_publish(8, tag);
      }
      const int e = lane & 7, ocol = j * 80 + 8 * h + e;
      const bool fin = h < 10, f3v = (lane >> 3) < 3;
      const unsigned yo = (unsigned)(((lane >> 3) * 8 + j) * 1280 + h * 128 + e * 16);
      unsigned short x0 = 0, x1 = 0;
      u32x4 g[4], y[4];
      if (fin) { x0 = __builtin_amdgcn_raw_buffer_load_b16(rx, ocol * 2, 0, 0); x1 = __builtin_amdgcn_raw_buffer_load_b16(rx, ocol * 2, 1280, 0); }
      wg_barrier();
      if (fin) {
#pragma unroll
        for (int i = 0; i < 4; ++i) g[i] = LD128(rws, (i < 3 || f3v) ? yo : OOB, WS_F + i * 64 * 1280, AUX_SC1);
        if (YDELAY) __builtin_amdgcn_s_sleep(YDELAY);
#pragma unroll
        for (int i = 0; i < 4; ++i) y[i] = LD128(rws, yo, WS_Y + i * 64 * 1280, AUX_SC1);
        for (unsigned sp = 0;; ++sp) {
          bool allok = true;
          CBAR();
#pragma unroll
          for (int i = 0; i < 4; ++i) if (!__all(gok(g[i], tag) | (i == 3 && !f3v))) { allok = false; g[i] = LD128(rws, (i < 3 || f3v) ? yo : OOB, WS_F + i * 64 * 1280, AUX_SC1); }
          if (allok || sp > SPIN_CAP) break;
        }
        float fa = (gv0(g[0]) + gv0(g[1])) + (gv0(g[2]) + gv0(g[3])), fb = (gv1(g[0]) + gv1(g[1])) + (gv1(g[2]) + gv1(g[3]));
        fa += dppf<ROR8>(fa); fb += dppf<ROR8>(fb);
        fa = rbf(x32_sum(x16_sum(fa))); fb = rbf(x32_sum(x16_sum(fb)));
        const float ga = rbf(fa / (1.f + __expf(-fa))), gb = rbf(fb / (1.f + __expf(-fb)));
        for (unsigned sp = 0;; ++sp) {
          bool allok = true;
          CBAR();
#pragma unroll
          for (int i = 0; i < 4; ++i) if (!__all(gok(y[i], tagB))) { if (YSLEEP && allok) __builtin_amdgcn_s_sleep(YSLEEP); allok = false; y[i] = LD128(rws, yo, WS_Y + i * 64 * 1280, AUX_SC1); }
          if (allok || sp > SPIN_CAP) break;
        }
        float ya = (gv0(y[0]) + gv0(y[1])) + (gv0(y[2]) + gv0(y[3])), yb = (gv1(y[0]) + gv1(y[1])) + (gv1(y[2]) + gv1(y[3]));
        ya += dppf<ROR8>(ya); yb += dppf<ROR8>(yb);
        ya = rbf(x32_sum(x16_sum(ya))); yb = rbf(x32_sum(x16_sum(yb)));
        const unsigned vv = pk_bf16(bflo(x0) + rbf(ya * ga), bflo(x1) + rbf(yb * gb)), nb = dppu<XOR1>(vv);
        const unsigned res = (e & 1) ? ((nb >> 16) | (vv & 0xffff0000u)) : ((vv & 0xffffu) | (nb << 16));
        __builtin_amdgcn_raw_buffer_store_b32(res, RSRC(out, 2560), lane < 8 ? (unsigned)(((e & 1) * 640 + (ocol & ~1)) * 2) : OOB, 0, 0);
        if (blk == 0 && lane == 0) __builtin_amdgcn_raw_buffer_store_b32(tag, rws, 0, WS_EPOCH, AUX_SC1);
      }
    }
  }
  if (w == REDW_ && h < 10) { asm volatile("s_waitcnt vmcnt(0)" ::: "memory"); unsigned t_ = (unsigned)__builtin_amdgcn_s_memrealtime(); unsigned d_ = (t_ - tstart_) * 10u; if (d_ > 60000u) d_ = 60000u; while ((unsigned)__builtin_amdgcn_s_memrealtime() - t_ < d_) __builtin_amdgcn_s_sleep(8); }
}

extern "C" void kernel_launch(void* const* d_in, const int* in_sizes, int n_in, void* d_out, int out_size, void* d_ws, size_t ws_size, hipStream_t stream) {
  fused_decode<<<256, 640, 0, stream>>>((const unsigned short*)d_in[0], (const unsigned short*)d_in[1], (const unsigned short*)d_in[2],
      (const unsigned short*)d_in[3], (const unsigned short*)d_in[4], (const unsigned short*)d_in[6],
      (unsigned char*)d_ws, (const unsigned short*)d_in[7], (unsigned short*)d_out);
}
```

```cpp
#define YDELAY 8
#define ACKDELAY 32
#define OSLEEP 3
#define ROTM 53
#define REDW_ 1
#include <hip/hip_runtime.h>
#include <hip/hip_bf16.h>
#include <stdint.h>

#define STAMP(i) do {} while (0)
#define STAMP_DECL
#define STAMP_FLUSH() do {} while (0)

typedef unsigned u32x4 __attribute__((ext_vector_type(4)));
typedef unsigned u32x2 __attribute__((ext_vector_type(2)));
typedef float f32x4 __attribute__((ext_vector_type(4)));
typedef float f32x2 __attribute__((ext_vector_type(2)));
typedef __bf16 bf16x8 __attribute__((ext_vector_type(8)));
typedef __bf16 bf16x2 __attribute__((ext_vector_type(2)));
typedef short s16x4 __attribute__((ext_vector_type(4)));

#define PDELAY 32
#define YSLEEP 0
constexpr int AUX_NT = 2, AUX_SC1 = 16;
constexpr unsigned OOB = 0x7ffffff0u;
constexpr unsigned WS_EPOCH = 0;
constexpr unsigned WS_Q1 = 4096;
constexpr unsigned WS_H1 = WS_Q1 + 256 * 1024;
constexpr unsigned WS_H2 = WS_H1 + 256 * 1280;
constexpr unsigned WS_S = WS_H2 + 256 * 128;
constexpr unsigned WS_F = WS_S + 256 * 128;
constexpr unsigned WS_END = WS_F + 256 * 1280;
constexpr unsigned WS_O = WS_Q1, WS_Y = WS_H1;
constexpr unsigned SPIN_CAP = 1u << 16, LSPIN_CAP = 1u << 22;
constexpr int CODE_BYTES = 13312;
constexpr int L_RED = 0, L_WL = 16384, L_QS = L_WL + 20480, L_HS = L_QS + 256, L_ST = L_HS + 512, L_FLG = L_ST + 64, L_XL = L_FLG + 64, L_OS = L_XL + 2560, L_END = L_OS + 512;

#define RSRC(p, n) __builtin_amdgcn_make_buffer_rsrc((void*)(p), 0, (n), 0x00020000)
#define LD128(rs, vo, so, aux) __builtin_amdgcn_raw_buffer_load_b128((rs), (vo), (so), (aux))
#define ST128(v, rs, vo, so, aux) __builtin_amdgcn_raw_buffer_store_b128((v), (rs), (vo), (so), (aux))
#define SBAR() __builtin_amdgcn_sched_barrier(0)
#define CBAR() asm volatile("" ::: "memory")
#define LGKM0() asm volatile("s_waitcnt lgkmcnt(0)" ::: "memory")
#define LAS __attribute__((address_space(3)))
#define LDS_LD32(p) __hip_atomic_load((LAS unsigned*)(p), __ATOMIC_RELAXED, __HIP_MEMORY_SCOPE_WORKGROUP)
#define LDS_ST32(p, v) __hip_atomic_store((LAS unsigned*)(p), (v), __ATOMIC_RELAXED, __HIP_MEMORY_SCOPE_WORKGROUP)
#define LDS_LD64(p) __hip_atomic_load((LAS unsigned long long*)(p), __ATOMIC_RELAXED, __HIP_MEMORY_SCOPE_WORKGROUP)
#define LDS_ST64(p, v) __hip_atomic_store((LAS unsigned long long*)(p), (v), __ATOMIC_RELAXED, __HIP_MEMORY_SCOPE_WORKGROUP)
#define LDS_INC(p) asm volatile("ds_add_u32 %0, %1" :: "v"((unsigned)(size_t)(LAS unsigned*)(p)), "v"(1u) : "memory")

template <int CTRL> __device__ __forceinline__ unsigned dppu(unsigned x) { return (unsigned)__builtin_amdgcn_mov_dpp((int)x, CTRL, 0xf, 0xf, true); }
template <int CTRL> __device__ __forceinline__ float dppf(float x) { return __uint_as_float(dppu<CTRL>(__float_as_uint(x))); }
constexpr int XOR1 = 0xB1, XOR2 = 0x4E, XOR7 = 0x141, ROR8 = 0x128, ROR4 = 0x124, SHL4 = 0x104, SHR4 = 0x114, BCAST = 0x150;

__device__ __forceinline__ float dot2(unsigned k, unsigned q, float acc) {
  return __builtin_amdgcn_fdot2_f32_bf16(__builtin_bit_cast(bf16x2, k), __builtin_bit_cast(bf16x2, q), acc, false);
}
__device__ __forceinline__ unsigned pk_bf16(float lo, float hi) { bf16x2 v; v[0] = (__bf16)lo; v[1] = (__bf16)hi; return __builtin_bit_cast(unsigned, v); }
__device__ __forceinline__ float rbf(float f) { return __uint_as_float(pk_bf16(f, f) & 0xffff0000u); }
__device__ __forceinline__ float bflo(unsigned u) { return __uint_as_float(u << 16); }
__device__ __forceinline__ float bfhi(unsigned u) { return __uint_as_float(u & 0xffff0000u); }
__device__ __forceinline__ f32x4 mfma(u32x4 a, u32x4 b, f32x4 c) {
  return __builtin_amdgcn_mfma_f32_16x16x32_bf16(__builtin_bit_cast(bf16x8, a), __builtin_bit_cast(bf16x8, b), c, 0, 0, 0);
}
__device__ __forceinline__ float x16_sum(float x) { auto s = __builtin_amdgcn_permlane16_swap(__float_as_uint(x), __float_as_uint(x), false, false); return __uint_as_float(s[0]) + __uint_as_float(s[1]); }
__device__ __forceinline__ float x32_sum(float x) { auto s = __builtin_amdgcn_permlane32_swap(__float_as_uint(x), __float_as_uint(x), false, false); return __uint_as_float(s[0]) + __uint_as_float(s[1]); }
__device__ __forceinline__ float x16_max(float x) { auto s = __builtin_amdgcn_permlane16_swap(__float_as_uint(x), __float_as_uint(x), false, false); return fmaxf(__uint_as_float(s[0]), __uint_as_float(s[1])); }
__device__ __forceinline__ float x32_max(float x) { auto s = __builtin_amdgcn_permlane32_swap(__float_as_uint(x), __float_as_uint(x), false, false); return fmaxf(__uint_as_float(s[0]), __uint_as_float(s[1])); }
__device__ __forceinline__ float row_sum(float x) { x += dppf<XOR1>(x); x += dppf<XOR2>(x); x += dppf<ROR4>(x); x += dppf<ROR8>(x); return x; }
__device__ __forceinline__ float row_max(float x) { x = fmaxf(x, dppf<XOR1>(x)); x = fmaxf(x, dppf<XOR2>(x)); x = fmaxf(x, dppf<ROR4>(x)); x = fmaxf(x, dppf<ROR8>(x)); return x; }
__device__ __forceinline__ float oct_max(float x) { x = fmaxf(x, dppf<XOR1>(x)); x = fmaxf(x, dppf<XOR2>(x)); return fmaxf(x, dppf<XOR7>(x)); }
template <int C0, int C1> __device__ __forceinline__ float trsel(float lo, float hi, bool bit) {
  const float t0 = lo + dppf<C0>(lo), t1 = hi + dppf<C1>(hi);
  return bit ? t1 : t0;
}
__device__ __forceinline__ bool gok(u32x4 g, unsigned tag) { return (g[1] == tag) & (g[3] == tag); }
__device__ __forceinline__ u32x4 gran2(float a, float b, unsigned tag) { u32x4 g; g[0] = __float_as_uint(a); g[1] = tag; g[2] = __float_as_uint(b); g[3] = tag; return g; }
__device__ __forceinline__ float gv0(u32x4 g) { return __uint_as_float(g[0]); }
__device__ __forceinline__ float gv1(u32x4 g) { return __uint_as_float(g[2]); }
__device__ __forceinline__ void wg_barrier() { asm volatile("s_waitcnt lgkmcnt(0)\n\ts_barrier" ::: "memory"); }
__device__ __forceinline__ unsigned lds_flag(unsigned* p) { return __builtin_amdgcn_readfirstlane(LDS_LD32(p)); }
__device__ __forceinline__ f32x2 sum8(const float* p) {
  f32x2 a[8];
#pragma unroll
  for (int i = 0; i < 8; ++i) a[i] = *(const f32x2*)(p + i * 256);
  return ((a[0] + a[1]) + (a[2] + a[3])) + ((a[4] + a[5]) + (a[6] + a[7]));
}
__device__ __forceinline__ u32x2 trread(unsigned char* p) { return __builtin_bit_cast(u32x2, __builtin_amdgcn_ds_read_tr16_b64_v4i16((LAS s16x4*)p)); }
constexpr float LOG2E = 1.4426950408889634f;

__global__ void __launch_bounds__(640, 3)
fused_decode(const unsigned short* x, const unsigned short* kc, const unsigned short* vc,
             const unsigned short* wq, const unsigned short* wo, const unsigned short* w1,
             unsigned char* ws, const unsigned short* w2, unsigned short* out) {
  unsigned long long pc_entry;
  asm volatile("s_getpc_b64 %0" : "=s"(pc_entry));
  asm volatile("" : "+s"(x), "+s"(kc), "+s"(vc), "+s"(wq), "+s"(wo), "+s"(w1), "+s"(ws), "+s"(w2), "+s"(out));
  __shared__ __attribute__((aligned(16))) unsigned char smem[L_END];
  f32x4* red = (f32x4*)(smem + L_RED);
  unsigned* qs = (unsigned*)(smem + L_QS);
  unsigned* hs = (unsigned*)(smem + L_HS);
  float* st = (float*)(smem + L_ST);
  unsigned* flg = (unsigned*)(smem + L_FLG);
  unsigned* xl = (unsigned*)(smem + L_XL);
  unsigned* os = (unsigned*)(smem + L_OS);
  const int blk = blockIdx.x, tid = threadIdx.x, lane = tid & 63, w = __builtin_amdgcn_readfirstlane(tid >> 6);
  STAMP_DECL
  const unsigned tstart_ = (unsigned)__builtin_amdgcn_s_memrealtime();
  const int j = (blk >> 3) & 7, h = (blk & 7) + 8 * (blk >> 6), b = j >> 2, s = j & 3;
  const int r = lane >> 4, c = lane & 15;
  const bool ffn = h < 27;
  const unsigned cu = (unsigned)(h * 8 + j);
  const __amdgpu_buffer_rsrc_t rws = RSRC(ws, WS_END);
  const __amdgpu_buffer_rsrc_t rx = RSRC(x, 2560);
  u32x4 mask;
#pragma unroll
  for (int i = 0; i < 4; ++i) mask[i] = (i == ((c & 7) >> 1)) ? ((c & 1) ? 0xffff0000u : 0xffffu) : 0u;
  auto f_publish = [&](int cbase, unsigned tg) {
    const int cr = cbase + (lane >> 3), e = lane & 7;
    const float* p = (const float*)(red + 512) + ((e >> 2) * 16 + (cr & 15)) * 4 + (e & 3);
    float a0[8], a1[8];
#pragma unroll
    for (int i = 0; i < 8; ++i) { a0[i] = p[i * 256]; a1[i] = p[i * 256 + 128]; }
    const float v0 = ((a0[0] + a0[1]) + (a0[2] + a0[3])) + ((a0[4] + a0[5]) + (a0[6] + a0[7]));
    const float v1 = ((a1[0] + a1[1]) + (a1[2] + a1[3])) + ((a1[4] + a1[5]) + (a1[6] + a1[7]));
    ST128(gran2(v0, v1, tg), rws, cr < 10 ? cu * 1280 + (unsigned)(cr * 128 + e * 16) : OOB, WS_F, AUX_SC1);
  };

  if (w >= 2) {
    const int sw = w - 2;
    const size_t kvbase = ((size_t)(b * 32 + h) * 8192 + 6144 + s * 512) * 128;
    const __amdgpu_buffer_rsrc_t rk = RSRC(kc + kvbase, 131072), rv = RSRC(vc + kvbase, 131072);
    const int rot = sw * 4 + ((blk * ROTM) & 127);
    const int voff = lane * 16;
#define KVOFF(t) ((((((t) >> 2) * 32 + ((t) & 3) + rot) & 127) << 10))
    u32x4 K[16], V[16], WO[4], W2[2];
    float sc = 0.f;
    auto scores = [&]() {
      CBAR();
      const u32x4 q4 = *(const u32x4*)(qs + 4 * c);
      float sb[4];
#pragma unroll
      for (int be = 0; be < 4; ++be) {
        float d[4];
#pragma unroll
        for (int tt = 0; tt < 4; ++tt) {
          const u32x4 kk = K[4 * be + tt];
          d[tt] = dot2(kk[3], q4[3], dot2(kk[2], q4[2], dot2(kk[1], q4[1], dot2(kk[0], q4[0], 0.f))));
        }
        const float e0 = trsel<XOR1, XOR1>(d[0], d[1], c & 1);
        const float e1 = trsel<XOR1, XOR1>(d[2], d[3], c & 1);
        sb[be] = trsel<XOR2, XOR2>(e0, e1, c & 2);
      }
      const float f0 = trsel<SHL4, SHR4>(sb[0], sb[1], c & 4);
      const float f1 = trsel<SHL4, SHR4>(sb[2], sb[3], c & 4);
      const float dotv = trsel<ROR8, ROR8>(f0, f1, c & 8);
      sc = rbf(rbf(dotv) * 0.08838834764831845f);
      const float mw = x32_max(x16_max(row_max(sc)));
      const float lw = x32_sum(x16_sum(row_sum(__builtin_amdgcn_exp2f((sc - mw) * LOG2E))));
      if (lane == 0) LDS_ST64(st + 2 * sw, (unsigned long long)__float_as_uint(mw) | ((unsigned long long)__float_as_uint(lw) << 32));
    };
    unsigned cp0 = 0, cp1 = 0;
    if (sw == 7 && (blk >> 3) < 4) {
      const __amdgpu_buffer_rsrc_t rc = RSRC((void*)(pc_entry & ~255ull), CODE_BYTES);
      cp0 = __builtin_amdgcn_raw_buffer_load_b32(rc, lane * 128, 0, 0);
      cp1 = __builtin_amdgcn_raw_buffer_load_b32(rc, lane * 128, 8192, 0);
    }
    __builtin_amdgcn_s_setprio(3);
    wg_barrier();
#pragma unroll
    for (int i = 0; i < 24; ++i) {
      if (i == 16) __builtin_amdgcn_s_setprio(2);
      if (i < 16) K[i] = LD128(rk, voff, KVOFF(i), AUX_NT); else V[i - 16] = LD128(rv, voff, KVOFF(i - 16), AUX_NT);
      SBAR();
    }
    const bool early = lds_flag(flg + 1) != 0u;
    if (early) scores();
    SBAR();
    __builtin_amdgcn_s_setprio(1);
#pragma unroll
    for (int t = 8; t < 16; ++t) { V[t] = LD128(rv, voff, KVOFF(t), AUX_NT); SBAR(); }
    if (!early) {
      for (unsigned sp = 0; sp < LSPIN_CAP && lds_flag(flg + 1) == 0u; ++sp) __builtin_amdgcn_s_sleep(1);
      scores();
    }
    SBAR();
    __builtin_amdgcn_s_setprio(0);
    {
      const __amdgpu_buffer_rsrc_t ro = RSRC(wo + (size_t)h * 128 * 640 + j * 80, 128 * 1280 - j * 160);
      const __amdgpu_buffer_rsrc_t r2 = RSRC(w2 + (size_t)h * 64 * 640 + j * 80, ffn ? 64 * 1280 - j * 160 : 0);
      const unsigned vo4 = c < 10 ? (unsigned)(r * 4 * 1280 + c * 16) : OOB;
      const unsigned vo2 = c < 10 ? (unsigned)(r * 2 * 1280 + c * 16) : OOB;
#pragma unroll
      for (int i = 0; i < 4; ++i) { WO[i] = LD128(ro, vo4, (16 * sw + i) * 1280, AUX_NT); SBAR(); }
#pragma unroll
      for (int i = 0; i < 2; ++i) { W2[i] = LD128(r2, vo2, (8 * sw + i) * 1280, AUX_NT); SBAR(); }
    }
    asm volatile("" :: "v"(cp0), "v"(cp1));
    unsigned pp;
    {
      unsigned long long t_ = LDS_LD64(st + 2 * (lane & 7));
      for (unsigned sp = 0; !__all((unsigned)(t_ >> 32) != 0u) && sp < LSPIN_CAP; ++sp) { __builtin_amdgcn_s_sleep(1); t_ = LDS_LD64(st + 2 * (lane & 7)); }
      const float M = oct_max(__uint_as_float((unsigned)t_));
      const float pv = __builtin_amdgcn_exp2f((sc - M) * LOG2E);
      pp = pk_bf16(pv, pv);
    }
    const unsigned tagB = lds_flag(flg) ^ 0x80000000u;
    f32x4 oacc = {0.f, 0.f, 0.f, 0.f};
#define PVSTEP(t) oacc = mfma(mask & dppu<BCAST + (t)>(pp), V[t], oacc);
    PVSTEP(0) PVSTEP(1) PVSTEP(2) PVSTEP(3) PVSTEP(4) PVSTEP(5) PVSTEP(6) PVSTEP(7)
    PVSTEP(8) PVSTEP(9) PVSTEP(10) PVSTEP(11) PVSTEP(12) PVSTEP(13) PVSTEP(14) PVSTEP(15)
#undef PVSTEP
    red[sw * 64 + lane] = oacc;
    LGKM0();
    if (lane == 0) LDS_INC(flg + 4);
    if (ffn) {
      for (unsigned sp = 0; sp < LSPIN_CAP && lds_flag(flg + 2) == 0u; ++sp) __builtin_amdgcn_s_sleep(1);
      CBAR();
      const u32x2 hv = *(const u32x2*)(hs + (c >> 3) * 64 + 8 * sw + 2 * r);
      f32x4 facc = {0.f, 0.f, 0.f, 0.f};
      facc = mfma(mask & hv[0], W2[0], facc);
      facc = mfma(mask & hv[1], W2[1], facc);
      red[(8 + sw) * 64 + lane] = facc;
      LGKM0();
      if (lane == 0) LDS_INC(flg + 3);
    }
    if (c < 10) {
#pragma unroll
      for (int i = 0; i < 4; ++i) *(u32x4*)(smem + L_WL + (16 * sw + 4 * r + i) * 160 + c * 16) = WO[i];
    }
    const int tro = L_WL + (8 * r + ((lane >> 2) & 3)) * 160 + sw * 32 + (lane & 3) * 8;
    const unsigned yoff = lane < 16 ? cu * 1280 + (unsigned)(sw * 256 + lane * 16) : OOB;
    wg_barrier();
    if (sw < 5) {
      u32x4 A[4], B[4];
#pragma unroll
      for (int t = 0; t < 4; ++t) {
        A[t] = *(const u32x4*)(os + (c & 1) * 64 + t * 16 + r * 4);
        const u32x2 lo = trread(smem + tro + t * 5120), hi = trread(smem + tro + t * 5120 + 640);
        B[t] = u32x4{lo[0], lo[1], hi[0], hi[1]};
      }
      f32x4 y = {0.f, 0.f, 0.f, 0.f}, y2 = {0.f, 0.f, 0.f, 0.f};
      y = mfma(A[0], B[0], y); y2 = mfma(A[1], B[1], y2);
      y = mfma(A[2], B[2], y); y2 = mfma(A[3], B[3], y2);
      ST128(gran2(y[0] + y2[0], y[1] + y2[1], tagB), rws, yoff, WS_Y, AUX_SC1);
    }
  } else {
    __builtin_amdgcn_s_setprio(3);
    const bool isq = (w == 0);
    const unsigned epoch = __builtin_amdgcn_raw_buffer_load_b32(rws, 0, WS_EPOCH, AUX_SC1);
    u32x2 xs[5];
#pragma unroll
    for (int i = 0; i < 5; ++i) xs[i] = __builtin_amdgcn_raw_buffer_load_b64(rx, (lane >> 5) * 1280 + (lane & 31) * 8, i * 256, 0);
    const int row0 = isq ? 160 * (j >> 1) : 80 * j;
    const int nrow = isq ? 160 : 80;
    unsigned short xr[5];
#pragma unroll
    for (int i = 0; i < 5; ++i) xr[i] = __builtin_amdgcn_raw_buffer_load_b16(rx, (lane >> 5) * 1280 + (lane & 31) * 2, (row0 + 32 * i) * 2, 0);
    u32x4 wa[20];
    {
      const __amdgpu_buffer_rsrc_t rw = isq ? RSRC(wq + h * 128 + (j & 1) * 64, 640 * 4096 * 2 - h * 256 - (j & 1) * 128) : RSRC(w1 + h * 64, ffn ? 640 * 1728 * 2 - h * 128 : 0);
      const int rstride = isq ? 8192 : 3456;
      const unsigned vo = (unsigned)((lane >> 3) * rstride + (lane & 7) * 16);
#pragma unroll
      for (int i = 0; i < 10; ++i) wa[i] = LD128(rw, vo, (row0 + 8 * i) * rstride, AUX_NT);
      if (isq) {
#pragma unroll
        for (int i = 10; i < 20; ++i) wa[i] = LD128(rw, vo, (row0 + 8 * i) * rstride, AUX_NT);
      }
    }
    if (isq && lane < 24) LDS_ST32(st + lane, 0u);
    wg_barrier();
    float xf[20];
#pragma unroll
    for (int i = 0; i < 5; ++i) { xf[4 * i] = bflo(xs[i][0]); xf[4 * i + 1] = bfhi(xs[i][0]); xf[4 * i + 2] = bflo(xs[i][1]); xf[4 * i + 3] = bfhi(xs[i][1]); }
    float sum = 0.f;
#pragma unroll
    for (int i = 0; i < 20; ++i) sum += xf[i];
    sum = x16_sum(row_sum(sum));
    const float mean = sum * (1.f / 640.f);
    float vs = 0.f;
#pragma unroll
    for (int i = 0; i < 20; ++i) { const float d = xf[i] - mean; vs = fmaf(d, d, vs); }
    vs = x16_sum(row_sum(vs));
    const float mb = rbf(mean), den = rbf(sqrtf(rbf(rbf(vs * (1.f / 640.f)) + 1e-5f)));
    unsigned* xw_ = xl + w * 320;
#pragma unroll
    for (int i = 0; i < 5; ++i) {
      const int row = (lane & 31) + 32 * i;
      const float xn = rbf(bflo(xr[i]) - mb) / den;
      if (row < nrow) xw_[(lane >> 5) * 160 + ((row >> 1) & 3) * 40 + (row >> 3) * 2 + (row & 1)] = pk_bf16(xn, xn);
    }
    const unsigned tag = __builtin_amdgcn_readfirstlane(epoch) + 0x9E3779B9u, tagB = tag ^ 0x80000000u;
    if (isq && lane == 0) LDS_ST32(flg, tag);
    LGKM0();
    f32x4 acc0 = {0.f, 0.f, 0.f, 0.f}, acc1 = {0.f, 0.f, 0.f, 0.f};
    const unsigned* xrd = xw_ + (c >> 3) * 160 + r * 40;
#pragma unroll
    for (int i = 0; i < 10; i += 2) {
      const u32x4 xa = *(const u32x4*)(xrd + 2 * i);
      acc0 = mfma(mask & xa[0], wa[i], acc0); acc1 = mfma(mask & xa[1], wa[i], acc1);
      acc0 = mfma(mask & xa[2], wa[i + 1], acc0); acc1 = mfma(mask & xa[3], wa[i + 1], acc1);
    }
    if (isq) {
#pragma unroll
      for (int i = 10; i < 20; i += 2) {
        const u32x4 xa = *(const u32x4*)(xrd + 2 * i);
        acc0 = mfma(mask & xa[0], wa[i], acc0); acc1 = mfma(mask & xa[1], wa[i], acc1);
        acc0 = mfma(mask & xa[2], wa[i + 1], acc0); acc1 = mfma(mask & xa[3], wa[i + 1], acc1);
      }
    }
    f32x4 v;
#pragma unroll
    for (int i = 0; i < 4; ++i) { const float t_ = c < 8 ? acc0[i] : acc1[i]; v[i] = t_ + dppf<ROR8>(t_); }
    const u32x4 pub = gran2(c < 8 ? v[0] : v[2], c < 8 ? v[1] : v[3], tag);
    if (isq) {
      ST128(pub, rws, cu * 1024 + lane * 16, WS_Q1, AUX_SC1);
      __builtin_amdgcn_s_sleep(PDELAY);
      const unsigned goff = (unsigned)((h * 8 + (lane >> 5)) * 1024 + (b * 32 + (lane & 31)) * 16);
      u32x4 G[8];
#pragma unroll
      for (int p = 0; p < 4; ++p) G[p] = LD128(rws, goff, WS_Q1 + p * 2048, AUX_SC1);
      for (unsigned sp = 0;; ++sp) {
        bool allok = true;
        CBAR();
#pragma unroll
        for (int p = 0; p < 4; ++p) if (!__all(gok(G[p], tag))) { allok = false; G[p] = LD128(rws, goff, WS_Q1 + p * 2048, AUX_SC1); }
        if (allok || sp > SPIN_CAP) break;
      }
      qs[(lane >> 5) * 32 + (c & 7) * 4 + (r & 1) * 2 + (c >> 3)] = pk_bf16((gv0(G[0]) + gv0(G[1])) + (gv0(G[2]) + gv0(G[3])), (gv1(G[0]) + gv1(G[1])) + (gv1(G[2]) + gv1(G[3])));
      ST128(gran2(0.f, 0.f, tag), rws, lane == 0 ? cu * 128 + 16 : OOB, WS_S, AUX_SC1);
      LGKM0();
      if (lane == 0) LDS_ST32(flg + 1, 1u);
      const unsigned soff = WS_S + (unsigned)((h * 8 + (lane & 7)) * 128 + (lane & 8) * 2);
      __builtin_amdgcn_s_sleep(ACKDELAY);
      u32x4 gs = LD128(rws, soff, 0, AUX_SC1);
      unsigned long long t_ = LDS_LD64(st + 2 * (lane & 7));
      for (unsigned sp = 0; !__all((unsigned)(t_ >> 32) != 0u) && sp < LSPIN_CAP; ++sp) { __builtin_amdgcn_s_sleep(2); t_ = LDS_LD64(st + 2 * (lane & 7)); }
      {
        const float m_ = __uint_as_float((unsigned)t_), l_ = __uint_as_float((unsigned)(t_ >> 32));
        const float M = oct_max(m_);
        float L = __builtin_amdgcn_exp2f((m_ - M) * LOG2E) * l_; L += dppf<XOR1>(L); L += dppf<XOR2>(L); L += dppf<XOR7>(L);
        ST128(gran2(M, L, tag), rws, lane == 0 ? cu * 128 : OOB, WS_S, AUX_SC1);
      }
      if (!__all(gok(gs, tag) | !(lane & 8))) {
        for (unsigned sp = 0; sp < SPIN_CAP; ++sp) {
          CBAR();
          gs = LD128(rws, soff, 0, AUX_SC1);
          if (__all(gok(gs, tag) | !(lane & 8))) break;
        }
      }
      for (unsigned sp = 0; sp < LSPIN_CAP && lds_flag(flg + 4) != 8u; ++sp) {}
      CBAR();
      const unsigned ooff = (unsigned)(h * 8 * 1024 + lane * 16);
      const f32x2 own = sum8((const float*)red + (((lane >> 1) & 1) * 16 + (lane >> 2)) * 4 + 2 * (lane & 1));
      const u32x4 ownG = gran2(own[0], own[1], tagB);
      ST128(ownG, rws, ooff, WS_O + j * 1024, AUX_SC1);
      gs = LD128(rws, soff, 0, AUX_SC1);
#pragma unroll
      for (int p = 0; p < 8; ++p) G[p] = LD128(rws, p == j ? OOB : ooff, WS_O + p * 1024, AUX_SC1);
#pragma unroll
      for (int p = 0; p < 8; ++p) if (p == j) G[p] = ownG;
      for (unsigned sp = 0; !__all(gok(gs, tag)) && sp < SPIN_CAP; ++sp) { CBAR(); __builtin_amdgcn_s_sleep(2); gs = LD128(rws, soff, 0, AUX_SC1); }
      float wp[8];
      {
        const float ms = gv0(gs), ls = gv1(gs);
        float Mg = fmaxf(ms, dppf<XOR1>(ms)); Mg = fmaxf(Mg, dppf<XOR2>(Mg));
        const float e_ = __builtin_amdgcn_exp2f((ms - Mg) * LOG2E);
        float Lg = e_ * ls; Lg += dppf<XOR1>(Lg); Lg += dppf<XOR2>(Lg);
        const float wgt = e_ / Lg;
#pragma unroll
        for (int p = 0; p < 8; ++p) wp[p] = __uint_as_float(__builtin_amdgcn_readlane(__float_as_uint(wgt), p));
      }
      for (unsigned sp = 0;; ++sp) {
        bool allok = true;
        CBAR();
#pragma unroll
        for (int p = 0; p < 8; ++p) if (!__all(gok(G[p], tagB))) { if (OSLEEP && allok) __builtin_amdgcn_s_sleep(OSLEEP); allok = false; G[p] = LD128(rws, ooff, WS_O + p * 1024, AUX_SC1); }
        if (allok || sp > SPIN_CAP) break;
      }
#pragma unroll
      for (int bb = 0; bb < 2; ++bb) {
        float o0 = 0.f, o1 = 0.f;
#pragma unroll
        for (int sp = 0; sp < 4; ++sp) { o0 = fmaf(wp[4 * bb + sp], gv0(G[4 * bb + sp]), o0); o1 = fmaf(wp[4 * bb + sp], gv1(G[4 * bb + sp]), o1); }
        os[bb * 64 + lane] = pk_bf16(o0, o1);
      }
      wg_barrier();
    } else {
      if (ffn) {
        ST128(pub, rws, (unsigned)((h * 8 + (c & 7)) * 1280 + j * 128 + (c >> 3) * 64 + r * 16), WS_H1, AUX_SC1);
        __builtin_amdgcn_s_sleep(2 * PDELAY);
        const unsigned goff = cu * 1280 + lane * 16;
        u32x4 G0 = LD128(rws, goff, WS_H1, AUX_SC1);
        for (unsigned sp = 0; !__all(gok(G0, tag)) && sp < SPIN_CAP; ++sp) { CBAR(); G0 = LD128(rws, goff, WS_H1, AUX_SC1); }
        float a0 = gv0(G0), a1 = gv1(G0);
        a0 += dppf<ROR8>(a0); a1 += dppf<ROR8>(a1);
        a0 = fmaxf(x32_sum(x16_sum(a0)), 0.f); a1 = fmaxf(x32_sum(x16_sum(a1)), 0.f);
        u32x2 g2; g2[0] = pk_bf16(a0, a1); g2[1] = tag;
        __builtin_amdgcn_raw_buffer_store_b64(g2, rws, lane < 8 ? cu * 128 + lane * 8 : OOB, WS_H2, AUX_SC1);
        const unsigned g2off = (unsigned)((h * 8 + (lane >> 3)) * 128 + (lane & 7) * 8);
        __builtin_amdgcn_s_sleep(3 * PDELAY);
        u32x2 q2 = __builtin_amdgcn_raw_buffer_load_b64(rws, g2off, WS_H2, AUX_SC1);
        for (unsigned sp = 0; !__all(q2[1] == tag) && sp < SPIN_CAP; ++sp) { CBAR(); q2 = __builtin_amdgcn_raw_buffer_load_b64(rws, g2off, WS_H2, AUX_SC1); }
        u32x2 hv; hv[0] = (q2[0] & 0xffffu) | (q2[0] << 16); hv[1] = (q2[0] >> 16) | (q2[0] & 0xffff0000u);
        *(u32x2*)(hs + ((lane >> 1) & 1) * 64 + (lane >> 3) * 8 + (lane & 1) * 4 + ((lane >> 2) & 1) * 2) = hv;
        LGKM0();
        if (lane == 0) LDS_ST32(flg + 2, 1u);
        for (unsigned sp = 0; sp < LSPIN_CAP && lds_flag(flg + 3) != 8u; ++sp) __builtin_amdgcn_s_sleep(1);
        CBAR();
        f_publish(0, tag);
        f_publish(8, tag);
      }
      const int e = lane & 7, ocol = j * 80 + 8 * h + e;
      const bool fin = h < 10, f3v = (lane >> 3) < 3;
      const unsigned yo = (unsigned)(((lane >> 3) * 8 + j) * 1280 + h * 128 + e * 16);
      unsigned short x0 = 0, x1 = 0;
      u32x4 g[4], y[4];
      if (fin) { x0 = __builtin_amdgcn_raw_buffer_load_b16(rx, ocol * 2, 0, 0); x1 = __builtin_amdgcn_raw_buffer_load_b16(rx, ocol * 2, 1280, 0); }
      wg_barrier();
      if (fin) {
#pragma unroll
        for (int i = 0; i < 4; ++i) g[i] = LD128(rws, (i < 3 || f3v) ? yo : OOB, WS_F + i * 64 * 1280, AUX_SC1);
        if (YDELAY) __builtin_amdgcn_s_sleep(YDELAY);
#pragma unroll
        for (int i = 0; i < 4; ++i) y[i] = LD128(rws, yo, WS_Y + i * 64 * 1280, AUX_SC1);
        for (unsigned sp = 0;; ++sp) {
          bool allok = true;
          CBAR();
#pragma unroll
          for (int i = 0; i < 4; ++i) if (!__all(gok(g[i], tag) | (i == 3 && !f3v))) { allok = false; g[i] = LD128(rws, (i < 3 || f3v) ? yo : OOB, WS_F + i * 64 * 1280, AUX_SC1); }
          if (allok || sp > SPIN_CAP) break;
        }
        float fa = (gv0(g[0]) + gv0(g[1])) + (gv0(g[2]) + gv0(g[3])), fb = (gv1(g[0]) + gv1(g[1])) + (gv1(g[2]) + gv1(g[3]));
        fa += dppf<ROR8>(fa); fb += dppf<ROR8>(fb);
        fa = rbf(x32_sum(x16_sum(fa))); fb = rbf(x32_sum(x16_sum(fb)));
        const float ga = rbf(fa / (1.f + __expf(-fa))), gb = rbf(fb / (1.f + __expf(-fb)));
        for (unsigned sp = 0;; ++sp) {
          bool allok = true;
          CBAR();
#pragma unroll
          for (int i = 0; i < 4; ++i) if (!__all(gok(y[i], tagB))) { if (YSLEEP && allok) __builtin_amdgcn_s_sleep(YSLEEP); allok = false; y[i] = LD128(rws, yo, WS_Y + i * 64 * 1280, AUX_SC1); }
          if (allok || sp > SPIN_CAP) break;
        }
        float ya = (gv0(y[0]) + gv0(y[1])) + (gv0(y[2]) + gv0(y[3])), yb = (gv1(y[0]) + gv1(y[1])) + (gv1(y[2]) + gv1(y[3]));
        ya += dppf<ROR8>(ya); yb += dppf<ROR8>(yb);
        ya = rbf(x32_sum(x16_sum(ya))); yb = rbf(x32_sum(x16_sum(yb)));
        const unsigned vv = pk_bf16(bflo(x0) + rbf(ya * ga), bflo(x1) + rbf(yb * gb)), nb = dppu<XOR1>(vv);
        const unsigned res = (e & 1) ? ((nb >> 16) | (vv & 0xffff0000u)) : ((vv & 0xffffu) | (nb << 16));
        __builtin_amdgcn_raw_buffer_store_b32(res, RSRC(out, 2560), lane < 8 ? (unsigned)(((e & 1) * 640 + (ocol & ~1)) * 2) : OOB, 0, 0);
        if (blk == 0 && lane == 0) __builtin_amdgcn_raw_buffer_store_b32(tag, rws, 0, WS_EPOCH, AUX_SC1);
      }
    }
  }
  if (w == REDW_ && h < 10) { asm volatile("s_waitcnt vmcnt(0)" ::: "memory"); unsigned t_ = (unsigned)__builtin_amdgcn_s_memrealtime(); unsigned d_ = (t_ - tstart_) * 10u; if (d_ > 60000u) d_ = 60000u; while ((unsigned)__builtin_amdgcn_s_memrealtime() - t_ < d_) __builtin_amdgcn_s_sleep(8); }
}

extern "C" void kernel_launch(void* const* d_in, const int* in_sizes, int n_in, void* d_out, int out_size, void* d_ws, size_t ws_size, hipStream_t stream) {
  fused_decode<<<256, 640, 0, stream>>>((const unsigned short*)d_in[0], (const unsigned short*)d_in[1], (const unsigned short*)d_in[2],
      (const unsigned short*)d_in[3], (const unsigned short*)d_in[4], (const unsigned short*)d_in[6],
      (unsigned char*)d_ws, (const unsigned short*)d_in[7], (unsigned short*)d_out);
}
```

```cpp
#define OTOUCH 0
#define REDW_ 1
#include <hip/hip_runtime.h>
#include <hip/hip_bf16.h>
#include <stdint.h>

#define STAMP(i) do {} while (0)
#define STAMP_DECL
#define STAMP_FLUSH() do {} while (0)

typedef unsigned u32x4 __attribute__((ext_vector_type(4)));
typedef unsigned u32x2 __attribute__((ext_vector_type(2)));
typedef float f32x4 __attribute__((ext_vector_type(4)));
typedef float f32x2 __attribute__((ext_vector_type(2)));
typedef __bf16 bf16x8 __attribute__((ext_vector_type(8)));
typedef __bf16 bf16x2 __attribute__((ext_vector_type(2)));
typedef short s16x4 __attribute__((ext_vector_type(4)));

#define PDELAY 32
#define ACKDELAY 48
#define YSLEEP 0
#define OSLEEP 0
#define YDELAY 4
#define ROTM 37
constexpr int AUX_NT = 2, AUX_SC1 = 16;
constexpr unsigned OOB = 0x7ffffff0u;
constexpr unsigned WS_EPOCH = 0;
constexpr unsigned WS_Q1 = 4096;
constexpr unsigned WS_H1 = WS_Q1 + 256 * 1024;
constexpr unsigned WS_H2 = WS_H1 + 256 * 1280;
constexpr unsigned WS_S = WS_H2 + 256 * 128;
constexpr unsigned WS_F = WS_S + 256 * 128;
constexpr unsigned WS_END = WS_F + 256 * 1280;
constexpr unsigned WS_O = WS_Q1, WS_Y = WS_H1;
constexpr unsigned SPIN_CAP = 1u << 16, LSPIN_CAP = 1u << 22;
constexpr int CODE_BYTES = 13312;
constexpr int L_RED = 0, L_WL = 16384, L_QS = L_WL + 20480, L_HS = L_QS + 256, L_ST = L_HS + 512, L_FLG = L_ST + 64, L_XL = L_FLG + 64, L_OS = L_XL + 2560, L_END = L_OS + 512;

#define RSRC(p, n) __builtin_amdgcn_make_buffer_rsrc((void*)(p), 0, (n), 0x00020000)
#define LD128(rs, vo, so, aux) __builtin_amdgcn_raw_buffer_load_b128((rs), (vo), (so), (aux))
#define ST128(v, rs, vo, so, aux) __builtin_amdgcn_raw_buffer_store_b128((v), (rs), (vo), (so), (aux))
#define SBAR() __builtin_amdgcn_sched_barrier(0)
#define CBAR() asm volatile("" ::: "memory")
#define LGKM0() asm volatile("s_waitcnt lgkmcnt(0)" ::: "memory")
#define LAS __attribute__((address_space(3)))
#define LDS_LD32(p) __hip_atomic_load((LAS unsigned*)(p), __ATOMIC_RELAXED, __HIP_MEMORY_SCOPE_WORKGROUP)
#define LDS_ST32(p, v) __hip_atomic_store((LAS unsigned*)(p), (v), __ATOMIC_RELAXED, __HIP_MEMORY_SCOPE_WORKGROUP)
#define LDS_LD64(p) __hip_atomic_load((LAS unsigned long long*)(p), __ATOMIC_RELAXED, __HIP_MEMORY_SCOPE_WORKGROUP)
#define LDS_ST64(p, v) __hip_atomic_store((LAS unsigned long long*)(p), (v), __ATOMIC_RELAXED, __HIP_MEMORY_SCOPE_WORKGROUP)
#define LDS_INC(p) asm volatile("ds_add_u32 %0, %1" :: "v"((unsigned)(size_t)(LAS unsigned*)(p)), "v"(1u) : "memory")

template <int CTRL> __device__ __forceinline__ unsigned dppu(unsigned x) { return (unsigned)__builtin_amdgcn_mov_dpp((int)x, CTRL, 0xf, 0xf, true); }
template <int CTRL> __device__ __forceinline__ float dppf(float x) { return __uint_as_float(dppu<CTRL>(__float_as_uint(x))); }
constexpr int XOR1 = 0xB1, XOR2 = 0x4E, XOR7 = 0x141, ROR8 = 0x128, ROR4 = 0x124, SHL4 = 0x104, SHR4 = 0x114, BCAST = 0x150;

__device__ __forceinline__ float dot2(unsigned k, unsigned q, float acc) {
  return __builtin_amdgcn_fdot2_f32_bf16(__builtin_bit_cast(bf16x2, k), __builtin_bit_cast(bf16x2, q), acc, false);
}
__device__ __forceinline__ unsigned pk_bf16(float lo, float hi) { bf16x2 v; v[0] = (__bf16)lo; v[1] = (__bf16)hi; return __builtin_bit_cast(unsigned, v); }
__device__ __forceinline__ float rbf(float f) { return __uint_as_float(pk_bf16(f, f) & 0xffff0000u); }
__device__ __forceinline__ float bflo(unsigned u) { return __uint_as_float(u << 16); }
__device__ __forceinline__ float bfhi(unsigned u) { return __uint_as_float(u & 0xffff0000u); }
__device__ __forceinline__ f32x4 mfma(u32x4 a, u32x4 b, f32x4 c) {
  return __builtin_amdgcn_mfma_f32_16x16x32_bf16(__builtin_bit_cast(bf16x8, a), __builtin_bit_cast(bf16x8, b), c, 0, 0, 0);
}
__device__ __forceinline__ float x16_sum(float x) { auto s = __builtin_amdgcn_permlane16_swap(__float_as_uint(x), __float_as_uint(x), false, false); return __uint_as_float(s[0]) + __uint_as_float(s[1]); }
__device__ __forceinline__ float x32_sum(float x) { auto s = __builtin_amdgcn_permlane32_swap(__float_as_uint(x), __float_as_uint(x), false, false); return __uint_as_float(s[0]) + __uint_as_float(s[1]); }
__device__ __forceinline__ float x16_max(float x) { auto s = __builtin_amdgcn_permlane16_swap(__float_as_uint(x), __float_as_uint(x), false, false); return fmaxf(__uint_as_float(s[0]), __uint_as_float(s[1])); }
__device__ __forceinline__ float x32_max(float x) { auto s = __builtin_amdgcn_permlane32_swap(__float_as_uint(x), __float_as_uint(x), false, false); return fmaxf(__uint_as_float(s[0]), __uint_as_float(s[1])); }
__device__ __forceinline__ float row_sum(float x) { x += dppf<XOR1>(x); x += dppf<XOR2>(x); x += dppf<ROR4>(x); x += dppf<ROR8>(x); return x; }
__device__ __forceinline__ float row_max(float x) { x = fmaxf(x, dppf<XOR1>(x)); x = fmaxf(x, dppf<XOR2>(x)); x = fmaxf(x, dppf<ROR4>(x)); x = fmaxf(x, dppf<ROR8>(x)); return x; }
__device__ __forceinline__ float oct_max(float x) { x = fmaxf(x, dppf<XOR1>(x)); x = fmaxf(x, dppf<XOR2>(x)); return fmaxf(x, dppf<XOR7>(x)); }
template <int C0, int C1> __device__ __forceinline__ float trsel(float lo, float hi, bool bit) {
  const float t0 = lo + dppf<C0>(lo), t1 = hi + dppf<C1>(hi);
  return bit ? t1 : t0;
}
__device__ __forceinline__ bool gok(u32x4 g, unsigned tag) { return (g[1] == tag) & (g[3] == tag); }
__device__ __forceinline__ u32x4 gran2(float a, float b, unsigned tag) { u32x4 g; g[0] = __float_as_uint(a); g[1] = tag; g[2] = __float_as_uint(b); g[3] = tag; return g; }
__device__ __forceinline__ float gv0(u32x4 g) { return __uint_as_float(g[0]); }
__device__ __forceinline__ float gv1(u32x4 g) { return __uint_as_float(g[2]); }
__device__ __forceinline__ void wg_barrier() { asm volatile("s_waitcnt lgkmcnt(0)\n\ts_barrier" ::: "memory"); }
__device__ __forceinline__ unsigned lds_flag(unsigned* p) { return __builtin_amdgcn_readfirstlane(LDS_LD32(p)); }
__device__ __forceinline__ f32x2 sum8(const float* p) {
  f32x2 a[8];
#pragma unroll
  for (int i = 0; i < 8; ++i) a[i] = *(const f32x2*)(p + i * 256);
  return ((a[0] + a[1]) + (a[2] + a[3])) + ((a[4] + a[5]) + (a[6] + a[7]));
}
__device__ __forceinline__ u32x2 trread(unsigned char* p) { return __builtin_bit_cast(u32x2, __builtin_amdgcn_ds_read_tr16_b64_v4i16((LAS s16x4*)p)); }
constexpr float LOG2E = 1.4426950408889634f;

__global__ void __launch_bounds__(640, 3)
fused_decode(const unsigned short* x, const unsigned short* kc, const unsigned short* vc,
             const unsigned short* wq, const unsigned short* wo, const unsigned short* w1,
             unsigned char* ws, const unsigned short* w2, unsigned short* out) {
  unsigned long long pc_entry;
  asm volatile("s_getpc_b64 %0" : "=s"(pc_entry));
  asm volatile("" : "+s"(x), "+s"(kc), "+s"(vc), "+s"(wq), "+s"(wo), "+s"(w1), "+s"(ws), "+s"(w2), "+s"(out));
  __shared__ __attribute__((aligned(16))) unsigned char smem[L_END];
  f32x4* red = (f32x4*)(smem + L_RED);
  unsigned* qs = (unsigned*)(smem + L_QS);
  unsigned* hs = (unsigned*)(smem + L_HS);
  float* st = (float*)(smem + L_ST);
  unsigned* flg = (unsigned*)(smem + L_FLG);
  unsigned* xl = (unsigned*)(smem + L_XL);
  unsigned* os = (unsigned*)(smem + L_OS);
  const int blk = blockIdx.x, tid = threadIdx.x, lane = tid & 63, w = __builtin_amdgcn_readfirstlane(tid >> 6);
  STAMP_DECL
  const unsigned tstart_ = (unsigned)__builtin_amdgcn_s_memrealtime();
  const int j = (blk >> 3) & 7, h = (blk & 7) + 8 * (blk >> 6), b = j >> 2, s = j & 3;
  const int r = lane >> 4, c = lane & 15;
  const bool ffn = h < 27;
  const unsigned cu = (unsigned)(h * 8 + j);
  const __amdgpu_buffer_rsrc_t rws = RSRC(ws, WS_END);
  const __amdgpu_buffer_rsrc_t rx = RSRC(x, 2560);
  u32x4 mask;
#pragma unroll
  for (int i = 0; i < 4; ++i) mask[i] = (i == ((c & 7) >> 1)) ? ((c & 1) ? 0xffff0000u : 0xffffu) : 0u;
  auto f_publish = [&](int cbase, unsigned tg) {
    const int cr = cbase + (lane >> 3), e = lane & 7;
    const float* p = (const float*)(red + 512) + ((e >> 2) * 16 + (cr & 15)) * 4 + (e & 3);
    float a0[8], a1[8];
#pragma unroll
    for (int i = 0; i < 8; ++i) { a0[i] = p[i * 256]; a1[i] = p[i * 256 + 128]; }
    const float v0 = ((a0[0] + a0[1]) + (a0[2] + a0[3])) + ((a0[4] + a0[5]) + (a0[6] + a0[7]));
    const float v1 = ((a1[0] + a1[1]) + (a1[2] + a1[3])) + ((a1[4] + a1[5]) + (a1[6] + a1[7]));
    ST128(gran2(v0, v1, tg), rws, cr < 10 ? cu * 1280 + (unsigned)(cr * 128 + e * 16) : OOB, WS_F, AUX_SC1);
  };

  if (w >= 2) {
    const int sw = w - 2;
    const size_t kvbase = ((size_t)(b * 32 + h) * 8192 + 6144 + s * 512) * 128;
    const __amdgpu_buffer_rsrc_t rk = RSRC(kc + kvbase, 131072), rv = RSRC(vc + kvbase, 131072);
    const int rot = sw * 4 + ((blk * ROTM) & 127);
    const int voff = lane * 16;
#define KVOFF(t) ((((((t) >> 2) * 32 + ((t) & 3) + rot) & 127) << 10))
    u32x4 K[16], V[16], WO[4], W2[2];
    float sc = 0.f;
    auto scores = [&]() {
      CBAR();
      const u32x4 q4 = *(const u32x4*)(qs + 4 * c);
      float sb[4];
#pragma unroll
      for (int be = 0; be < 4; ++be) {
        float d[4];
#pragma unroll
        for (int tt = 0; tt < 4; ++tt) {
          const u32x4 kk = K[4 * be + tt];
          d[tt] = dot2(kk[3], q4[3], dot2(kk[2], q4[2], dot2(kk[1], q4[1], dot2(kk[0], q4[0], 0.f))));
        }
        const float e0 = trsel<XOR1, XOR1>(d[0], d[1], c & 1);
        const float e1 = trsel<XOR1, XOR1>(d[2], d[3], c & 1);
        sb[be] = trsel<XOR2, XOR2>(e0, e1, c & 2);
      }
      const float f0 = trsel<SHL4, SHR4>(sb[0], sb[1], c & 4);
      const float f1 = trsel<SHL4, SHR4>(sb[2], sb[3], c & 4);
      const float dotv = trsel<ROR8, ROR8>(f0, f1, c & 8);
      sc = rbf(rbf(dotv) * 0.08838834764831845f);
      const float mw = x32_max(x16_max(row_max(sc)));
      const float lw = x32_sum(x16_sum(row_sum(__builtin_amdgcn_exp2f((sc - mw) * LOG2E))));
      if (lane == 0) LDS_ST64(st + 2 * sw, (unsigned long long)__float_as_uint(mw) | ((unsigned long long)__float_as_uint(lw) << 32));
    };
    unsigned cp0 = 0, cp1 = 0;
    if (sw == 7 && (blk >> 3) < 4) {
      const __amdgpu_buffer_rsrc_t rc = RSRC((void*)(pc_entry & ~255ull), CODE_BYTES);
      cp0 = __builtin_amdgcn_raw_buffer_load_b32(rc, lane * 128, 0, 0);
      cp1 = __builtin_amdgcn_raw_buffer_load_b32(rc, lane * 128, 8192, 0);
    }
    __builtin_amdgcn_s_setprio(3);
    wg_barrier();
#pragma unroll
    for (int i = 0; i < 24; ++i) {
      if (i == 16) __builtin_amdgcn_s_setprio(2);
      if (i < 16) K[i] = LD128(rk, voff, KVOFF(i), AUX_NT); else V[i - 16] = LD128(rv, voff, KVOFF(i - 16), AUX_NT);
      SBAR();
    }
    const bool early = lds_flag(flg + 1) != 0u;
    if (early) scores();
    SBAR();
    __builtin_amdgcn_s_setprio(1);
#pragma unroll
    for (int t = 8; t < 16; ++t) { V[t] = LD128(rv, voff, KVOFF(t), AUX_NT); SBAR(); }
    if (!early) {
      for (unsigned sp = 0; sp < LSPIN_CAP && lds_flag(flg + 1) == 0u; ++sp) __builtin_amdgcn_s_sleep(1);
      scores();
    }
    SBAR();
    __builtin_amdgcn_s_setprio(0);
    {
      const __amdgpu_buffer_rsrc_t ro = RSRC(wo + (size_t)h * 128 * 640 + j * 80, 128 * 1280 - j * 160);
      const __amdgpu_buffer_rsrc_t r2 = RSRC(w2 + (size_t)h * 64 * 640 + j * 80, ffn ? 64 * 1280 - j * 160 : 0);
      const unsigned vo4 = c < 10 ? (unsigned)(r * 4 * 1280 + c * 16) : OOB;
      const unsigned vo2 = c < 10 ? (unsigned)(r * 2 * 1280 + c * 16) : OOB;
#pragma unroll
      for (int i = 0; i < 4; ++i) { WO[i] = LD128(ro, vo4, (16 * sw + i) * 1280, AUX_NT); SBAR(); }
#pragma unroll
      for (int i = 0; i < 2; ++i) { W2[i] = LD128(r2, vo2, (8 * sw + i) * 1280, AUX_NT); SBAR(); }
    }
    asm volatile("" :: "v"(cp0), "v"(cp1));
    unsigned pp;
    {
      unsigned long long t_ = LDS_LD64(st + 2 * (lane & 7));
      for (unsigned sp = 0; !__all((unsigned)(t_ >> 32) != 0u) && sp < LSPIN_CAP; ++sp) { __builtin_amdgcn_s_sleep(1); t_ = LDS_LD64(st + 2 * (lane & 7)); }
      const float M = oct_max(__uint_as_float((unsigned)t_));
      const float pv = __builtin_amdgcn_exp2f((sc - M) * LOG2E);
      pp = pk_bf16(pv, pv);
    }
    const unsigned tagB = lds_flag(flg) ^ 0x80000000u;
    f32x4 oacc = {0.f, 0.f, 0.f, 0.f};
#define PVSTEP(t) oacc = mfma(mask & dppu<BCAST + (t)>(pp), V[t], oacc);
    PVSTEP(0) PVSTEP(1) PVSTEP(2) PVSTEP(3) PVSTEP(4) PVSTEP(5) PVSTEP(6) PVSTEP(7)
    PVSTEP(8) PVSTEP(9) PVSTEP(10) PVSTEP(11) PVSTEP(12) PVSTEP(13) PVSTEP(14) PVSTEP(15)
#undef PVSTEP
    red[sw * 64 + lane] = oacc;
    LGKM0();
    if (lane == 0) LDS_INC(flg + 4);
    if (ffn) {
      for (unsigned sp = 0; sp < LSPIN_CAP && lds_flag(flg + 2) == 0u; ++sp) __builtin_amdgcn_s_sleep(1);
      CBAR();
      const u32x2 hv = *(const u32x2*)(hs + (c >> 3) * 64 + 8 * sw + 2 * r);
      f32x4 facc = {0.f, 0.f, 0.f, 0.f};
      facc = mfma(mask & hv[0], W2[0], facc);
      facc = mfma(mask & hv[1], W2[1], facc);
      red[(8 + sw) * 64 + lane] = facc;
      LGKM0();
      if (lane == 0) LDS_INC(flg + 3);
    }
    if (c < 10) {
#pragma unroll
      for (int i = 0; i < 4; ++i) *(u32x4*)(smem + L_WL + (16 * sw + 4 * r + i) * 160 + c * 16) = WO[i];
    }
    const int tro = L_WL + (8 * r + ((lane >> 2) & 3)) * 160 + sw * 32 + (lane & 3) * 8;
    const unsigned yoff = lane < 16 ? cu * 1280 + (unsigned)(sw * 256 + lane * 16) : OOB;
    wg_barrier();
    if (sw < 5) {
      u32x4 A[4], B[4];
#pragma unroll
      for (int t = 0; t < 4; ++t) {
        A[t] = *(const u32x4*)(os + (c & 1) * 64 + t * 16 + r * 4);
        const u32x2 lo = trread(smem + tro + t * 5120), hi = trread(smem + tro + t * 5120 + 640);
        B[t] = u32x4{lo[0], lo[1], hi[0], hi[1]};
      }
      f32x4 y = {0.f, 0.f, 0.f, 0.f}, y2 = {0.f, 0.f, 0.f, 0.f};
      y = mfma(A[0], B[0], y); y2 = mfma(A[1], B[1], y2);
      y = mfma(A[2], B[2], y); y2 = mfma(A[3], B[3], y2);
      ST128(gran2(y[0] + y2[0], y[1] + y2[1], tagB), rws, yoff, WS_Y, AUX_SC1);
    }
  } else {
    __builtin_amdgcn_s_setprio(3);
    const bool isq = (w == 0);
    const unsigned epoch = __builtin_amdgcn_raw_buffer_load_b32(rws, 0, WS_EPOCH, AUX_SC1);
    u32x2 xs[5];
#pragma unroll
    for (int i = 0; i < 5; ++i) xs[i] = __builtin_amdgcn_raw_buffer_load_b64(rx, (lane >> 5) * 1280 + (lane & 31) * 8, i * 256, 0);
    const int row0 = isq ? 160 * (j >> 1) : 80 * j;
    const int nrow = isq ? 160 : 80;
    unsigned short xr[5];
#pragma unroll
    for (int i = 0; i < 5; ++i) xr[i] = __builtin_amdgcn_raw_buffer_load_b16(rx, (lane >> 5) * 1280 + (lane & 31) * 2, (row0 + 32 * i) * 2, 0);
    u32x4 wa[20];
    {
      const __amdgpu_buffer_rsrc_t rw = isq ? RSRC(wq + h * 128 + (j & 1) * 64, 640 * 4096 * 2 - h * 256 - (j & 1) * 128) : RSRC(w1 + h * 64, ffn ? 640 * 1728 * 2 - h * 128 : 0);
      const int rstride = isq ? 8192 : 3456;
      const unsigned vo = (unsigned)((lane >> 3) * rstride + (lane & 7) * 16);
#pragma unroll
      for (int i = 0; i < 10; ++i) wa[i] = LD128(rw, vo, (row0 + 8 * i) * rstride, AUX_NT);
      if (isq) {
#pragma unroll
        for (int i = 10; i < 20; ++i) wa[i] = LD128(rw, vo, (row0 + 8 * i) * rstride, AUX_NT);
      }
    }
    if (isq && lane < 24) LDS_ST32(st + lane, 0u);
    wg_barrier();
    float xf[20];
#pragma unroll
    for (int i = 0; i < 5; ++i) { xf[4 * i] = bflo(xs[i][0]); xf[4 * i + 1] = bfhi(xs[i][0]); xf[4 * i + 2] = bflo(xs[i][1]); xf[4 * i + 3] = bfhi(xs[i][1]); }
    float sum = 0.f;
#pragma unroll
    for (int i = 0; i < 20; ++i) sum += xf[i];
    sum = x16_sum(row_sum(sum));
    const float mean = sum * (1.f / 640.f);
    float vs = 0.f;
#pragma unroll
    for (int i = 0; i < 20; ++i) { const float d = xf[i] - mean; vs = fmaf(d, d, vs); }
    vs = x16_sum(row_sum(vs));
    const float mb = rbf(mean), den = rbf(sqrtf(rbf(rbf(vs * (1.f / 640.f)) + 1e-5f)));
    unsigned* xw_ = xl + w * 320;
#pragma unroll
    for (int i = 0; i < 5; ++i) {
      const int row = (lane & 31) + 32 * i;
      const float xn = rbf(bflo(xr[i]) - mb) / den;
      if (row < nrow) xw_[(lane >> 5) * 160 + ((row >> 1) & 3) * 40 + (row >> 3) * 2 + (row & 1)] = pk_bf16(xn, xn);
    }
    const unsigned tag = __builtin_amdgcn_readfirstlane(epoch) + 0x9E3779B9u, tagB = tag ^ 0x80000000u;
    if (isq && lane == 0) LDS_ST32(flg, tag);
    LGKM0();
    f32x4 acc0 = {0.f, 0.f, 0.f, 0.f}, acc1 = {0.f, 0.f, 0.f, 0.f};
    const unsigned* xrd = xw_ + (c >> 3) * 160 + r * 40;
#pragma unroll
    for (int i = 0; i < 10; i += 2) {
      const u32x4 xa = *(const u32x4*)(xrd + 2 * i);
      acc0 = mfma(mask & xa[0], wa[i], acc0); acc1 = mfma(mask & xa[1], wa[i], acc1);
      acc0 = mfma(mask & xa[2], wa[i + 1], acc0); acc1 = mfma(mask & xa[3], wa[i + 1], acc1);
    }
    if (isq) {
#pragma unroll
      for (int i = 10; i < 20; i += 2) {
        const u32x4 xa = *(const u32x4*)(xrd + 2 * i);
        acc0 = mfma(mask & xa[0], wa[i], acc0); acc1 = mfma(mask & xa[1], wa[i], acc1);
        acc0 = mfma(mask & xa[2], wa[i + 1], acc0); acc1 = mfma(mask & xa[3], wa[i + 1], acc1);
      }
    }
    f32x4 v;
#pragma unroll
    for (int i = 0; i < 4; ++i) { const float t_ = c < 8 ? acc0[i] : acc1[i]; v[i] = t_ + dppf<ROR8>(t_); }
    const u32x4 pub = gran2(c < 8 ? v[0] : v[2], c < 8 ? v[1] : v[3], tag);
    if (isq) {
      ST128(pub, rws, cu * 1024 + lane * 16, WS_Q1, AUX_SC1);
      __builtin_amdgcn_s_sleep(PDELAY);
      const unsigned goff = (unsigned)((h * 8 + (lane >> 5)) * 1024 + (b * 32 + (lane & 31)) * 16);
      u32x4 G[8];
#pragma unroll
      for (int p = 0; p < 4; ++p) G[p] = LD128(rws, goff, WS_Q1 + p * 2048, AUX_SC1);
      for (unsigned sp = 0;; ++sp) {
        bool allok = true;
        CBAR();
#pragma unroll
        for (int p = 0; p < 4; ++p) if (!__all(gok(G[p], tag))) { allok = false; G[p] = LD128(rws, goff, WS_Q1 + p * 2048, AUX_SC1); }
        if (allok || sp > SPIN_CAP) break;
      }
      qs[(lane >> 5) * 32 + (c & 7) * 4 + (r & 1) * 2 + (c >> 3)] = pk_bf16((gv0(G[0]) + gv0(G[1])) + (gv0(G[2]) + gv0(G[3])), (gv1(G[0]) + gv1(G[1])) + (gv1(G[2]) + gv1(G[3])));
      ST128(gran2(0.f, 0.f, tag), rws, lane == 0 ? cu * 128 + 16 : OOB, WS_S, AUX_SC1);
      LGKM0();
      if (lane == 0) LDS_ST32(flg + 1, 1u);
      const unsigned soff = WS_S + (unsigned)((h * 8 + (lane & 7)) * 128 + (lane & 8) * 2);
      __builtin_amdgcn_s_sleep(ACKDELAY);
      u32x4 gs = LD128(rws, soff, 0, AUX_SC1);
      unsigned long long t_ = LDS_LD64(st + 2 * (lane & 7));
      for (unsigned sp = 0; !__all((unsigned)(t_ >> 32) != 0u) && sp < LSPIN_CAP; ++sp) { __builtin_amdgcn_s_sleep(2); t_ = LDS_LD64(st + 2 * (lane & 7)); }
      {
        const float m_ = __uint_as_float((unsigned)t_), l_ = __uint_as_float((unsigned)(t_ >> 32));
        const float M = oct_max(m_);
        float L = __builtin_amdgcn_exp2f((m_ - M) * LOG2E) * l_; L += dppf<XOR1>(L); L += dppf<XOR2>(L); L += dppf<XOR7>(L);
        ST128(gran2(M, L, tag), rws, lane == 0 ? cu * 128 : OOB, WS_S, AUX_SC1);
      }
      if (!__all(gok(gs, tag) | !(lane & 8))) {
        for (unsigned sp = 0; sp < SPIN_CAP; ++sp) {
          CBAR();
          gs = LD128(rws, soff, 0, AUX_SC1);
          if (__all(gok(gs, tag) | !(lane & 8))) break;
        }
      }
      for (unsigned sp = 0; sp < LSPIN_CAP && lds_flag(flg + 4) != 8u; ++sp) {}
      CBAR();
      const unsigned ooff = (unsigned)(h * 8 * 1024 + lane * 16);
      const f32x2 own = sum8((const float*)red + (((lane >> 1) & 1) * 16 + (lane >> 2)) * 4 + 2 * (lane & 1));
      const u32x4 ownG = gran2(own[0], own[1], tagB);
      ST128(ownG, rws, ooff, WS_O + j * 1024, AUX_SC1);
      gs = LD128(rws, soff, 0, AUX_SC1);
#pragma unroll
      for (int p = 0; p < 8; ++p) G[p] = LD128(rws, p == j ? OOB : ooff, WS_O + p * 1024, AUX_SC1);
#pragma unroll
      for (int p = 0; p < 8; ++p) if (p == j) G[p] = ownG;
      for (unsigned sp = 0; !__all(gok(gs, tag)) && sp < SPIN_CAP; ++sp) { CBAR(); __builtin_amdgcn_s_sleep(2); gs = LD128(rws, soff, 0, AUX_SC1); }
      float wp[8];
      {
        const float ms = gv0(gs), ls = gv1(gs);
        float Mg = fmaxf(ms, dppf<XOR1>(ms)); Mg = fmaxf(Mg, dppf<XOR2>(Mg));
        const float e_ = __builtin_amdgcn_exp2f((ms - Mg) * LOG2E);
        float Lg = e_ * ls; Lg += dppf<XOR1>(Lg); Lg += dppf<XOR2>(Lg);
        const float wgt = e_ / Lg;
#pragma unroll
        for (int p = 0; p < 8; ++p) wp[p] = __uint_as_float(__builtin_amdgcn_readlane(__float_as_uint(wgt), p));
      }
      for (unsigned sp = 0;; ++sp) {
        bool allok = true;
        CBAR();
#pragma unroll
        for (int p = 0; p < 8; ++p) if (!__all(gok(G[p], tagB))) { if (OSLEEP && allok) __builtin_amdgcn_s_sleep(OSLEEP); allok = false; G[p] = LD128(rws, ooff, WS_O + p * 1024, AUX_SC1); }
        if (allok || sp > SPIN_CAP) break;
      }
#pragma unroll
      for (int bb = 0; bb < 2; ++bb) {
        float o0 = 0.f, o1 = 0.f;
#pragma unroll
        for (int sp = 0; sp < 4; ++sp) { o0 = fmaf(wp[4 * bb + sp], gv0(G[4 * bb + sp]), o0); o1 = fmaf(wp[4 * bb + sp], gv1(G[4 * bb + sp]), o1); }
        os[bb * 64 + lane] = pk_bf16(o0, o1);
      }
      wg_barrier();
    } else {
      if (ffn) {
        ST128(pub, rws, (unsigned)((h * 8 + (c & 7)) * 1280 + j * 128 + (c >> 3) * 64 + r * 16), WS_H1, AUX_SC1);
        __builtin_amdgcn_s_sleep(2 * PDELAY);
        const unsigned goff = cu * 1280 + lane * 16;
        u32x4 G0 = LD128(rws, goff, WS_H1, AUX_SC1);
        for (unsigned sp = 0; !__all(gok(G0, tag)) && sp < SPIN_CAP; ++sp) { CBAR(); G0 = LD128(rws, goff, WS_H1, AUX_SC1); }
        float a0 = gv0(G0), a1 = gv1(G0);
        a0 += dppf<ROR8>(a0); a1 += dppf<ROR8>(a1);
        a0 = fmaxf(x32_sum(x16_sum(a0)), 0.f); a1 = fmaxf(x32_sum(x16_sum(a1)), 0.f);
        u32x2 g2; g2[0] = pk_bf16(a0, a1); g2[1] = tag;
        __builtin_amdgcn_raw_buffer_store_b64(g2, rws, lane < 8 ? cu * 128 + lane * 8 : OOB, WS_H2, AUX_SC1);
        const unsigned g2off = (unsigned)((h * 8 + (lane >> 3)) * 128 + (lane & 7) * 8);
        __builtin_amdgcn_s_sleep(3 * PDELAY);
        u32x2 q2 = __builtin_amdgcn_raw_buffer_load_b64(rws, g2off, WS_H2, AUX_SC1);
        for (unsigned sp = 0; !__all(q2[1] == tag) && sp < SPIN_CAP; ++sp) { CBAR(); q2 = __builtin_amdgcn_raw_buffer_load_b64(rws, g2off, WS_H2, AUX_SC1); }
        u32x2 hv; hv[0] = (q2[0] & 0xffffu) | (q2[0] << 16); hv[1] = (q2[0] >> 16) | (q2[0] & 0xffff0000u);
        *(u32x2*)(hs + ((lane >> 1) & 1) * 64 + (lane >> 3) * 8 + (lane & 1) * 4 + ((lane >> 2) & 1) * 2) = hv;
        LGKM0();
        if (lane == 0) LDS_ST32(flg + 2, 1u);
        for (unsigned sp = 0; sp < LSPIN_CAP && lds_flag(flg + 3) != 8u; ++sp) __builtin_amdgcn_s_sleep(1);
        CBAR();
        f_publish(0, tag);
        f_publish(8, tag);
      }
      const int e = lane & 7, ocol = j * 80 + 8 * h + e;
      const bool fin = h < 10, f3v = (lane >> 3) < 3;
      const unsigned yo = (unsigned)(((lane >> 3) * 8 + j) * 1280 + h * 128 + e * 16);
      unsigned short x0 = 0, x1 = 0;
      unsigned otouch = 0;
      const __amdgpu_buffer_rsrc_t rout = RSRC(out, 2560);
      const unsigned ooffs = lane < 8 ? (unsigned)(((e & 1) * 640 + (ocol & ~1)) * 2) : OOB;
      u32x4 g[4], y[4];
      if (fin) { x0 = __builtin_amdgcn_raw_buffer_load_b16(rx, ocol * 2, 0, 0); x1 = __builtin_amdgcn_raw_buffer_load_b16(rx, ocol * 2, 1280, 0);
        if (OTOUCH) otouch = __builtin_amdgcn_raw_buffer_load_b32(rout, ooffs, 0, 0); }
      wg_barrier();
      if (fin) {
#pragma unroll
        for (int i = 0; i < 4; ++i) g[i] = LD128(rws, (i < 3 || f3v) ? yo : OOB, WS_F + i * 64 * 1280, AUX_SC1);
        if (YDELAY) __builtin_amdgcn_s_sleep(YDELAY);
#pragma unroll
        for (int i = 0; i < 4; ++i) y[i] = LD128(rws, yo, WS_Y + i * 64 * 1280, AUX_SC1);
        for (unsigned sp = 0;; ++sp) {
          bool allok = true;
          CBAR();
#pragma unroll
          for (int i = 0; i < 4; ++i) if (!__all(gok(g[i], tag) | (i == 3 && !f3v))) { allok = false; g[i] = LD128(rws, (i < 3 || f3v) ? yo : OOB, WS_F + i * 64 * 1280, AUX_SC1); }
          if (allok || sp > SPIN_CAP) break;
        }
        float fa = (gv0(g[0]) + gv0(g[1])) + (gv0(g[2]) + gv0(g[3])), fb = (gv1(g[0]) + gv1(g[1])) + (gv1(g[2]) + gv1(g[3]));
        fa += dppf<ROR8>(fa); fb += dppf<ROR8>(fb);
        fa = rbf(x32_sum(x16_sum(fa))); fb = rbf(x32_sum(x16_sum(fb)));
        const float ga = rbf(fa / (1.f + __expf(-fa))), gb = rbf(fb / (1.f + __expf(-fb)));
        asm volatile("" :: "v"(otouch));
        for (unsigned sp = 0;; ++sp) {
          bool allok = true;
          CBAR();
#pragma unroll
          for (int i = 0; i < 4; ++i) if (!__all(gok(y[i], tagB))) { if (YSLEEP && allok) __builtin_amdgcn_s_sleep(YSLEEP); allok = false; y[i] = LD128(rws, yo, WS_Y + i * 64 * 1280, AUX_SC1); }
          if (allok || sp > SPIN_CAP) break;
        }
        float ya = (gv0(y[0]) + gv0(y[1])) + (gv0(y[2]) + gv0(y[3])), yb = (gv1(y[0]) + gv1(y[1])) + (gv1(y[2]) + gv1(y[3]));
        ya += dppf<ROR8>(ya); yb += dppf<ROR8>(yb);
        ya = rbf(x32_sum(x16_sum(ya))); yb = rbf(x32_sum(x16_sum(yb)));
        const unsigned vv = pk_bf16(bflo(x0) + rbf(ya * ga), bflo(x1) + rbf(yb * gb)), nb = dppu<XOR1>(vv);
        const unsigned res = (e & 1) ? ((nb >> 16) | (vv & 0xffff0000u)) : ((vv & 0xffffu) | (nb << 16));
        __builtin_amdgcn_raw_buffer_store_b32(res, rout, ooffs, 0, 0);
        if (blk == 0 && lane == 0) __builtin_amdgcn_raw_buffer_store_b32(tag, rws, 0, WS_EPOCH, AUX_SC1);
      }
    }
  }
  if (w == REDW_ && h < 10) { asm volatile("s_waitcnt vmcnt(0)" ::: "memory"); unsigned t_ = (unsigned)__builtin_amdgcn_s_memrealtime(); unsigned d_ = (t_ - tstart_) * 10u; if (d_ > 60000u) d_ = 60000u; while ((unsigned)__builtin_amdgcn_s_memrealtime() - t_ < d_) __builtin_amdgcn_s_sleep(8); }
}

extern "C" void kernel_launch(void* const* d_in, const int* in_sizes, int n_in, void* d_out, int out_size, void* d_ws, size_t ws_size, hipStream_t stream) {
  fused_decode<<<256, 640, 0, stream>>>((const unsigned short*)d_in[0], (const unsigned short*)d_in[1], (const unsigned short*)d_in[2],
      (const unsigned short*)d_in[3], (const unsigned short*)d_in[4], (const unsigned short*)d_in[6],
      (unsigned char*)d_ws, (const unsigned short*)d_in[7], (unsigned short*)d_out);
}
```

```cpp
#include <hip/hip_runtime.h>
#include <hip/hip_bf16.h>
#include <stdint.h>

typedef unsigned u32x4 __attribute__((ext_vector_type(4)));
typedef unsigned u32x2 __attribute__((ext_vector_type(2)));
typedef float f32x4 __attribute__((ext_vector_type(4)));
typedef float f32x2 __attribute__((ext_vector_type(2)));
typedef __bf16 bf16x8 __attribute__((ext_vector_type(8)));
typedef __bf16 bf16x2 __attribute__((ext_vector_type(2)));
typedef short s16x4 __attribute__((ext_vector_type(4)));

constexpr int PDELAY = 32;
constexpr int ACKDELAY = 48;
constexpr int YDELAY = 4;
constexpr int ROTM = 37;
constexpr int AUX_NT = 2, AUX_SC1 = 16;
constexpr unsigned OOB = 0x7ffffff0u;
constexpr unsigned WS_EPOCH = 0;
constexpr unsigned WS_Q1 = 4096;
constexpr unsigned WS_H1 = WS_Q1 + 256 * 1024;
constexpr unsigned WS_H2 = WS_H1 + 256 * 1280;
constexpr unsigned WS_S = WS_H2 + 256 * 128;
constexpr unsigned WS_F = WS_S + 256 * 128;
constexpr unsigned WS_END = WS_F + 256 * 1280;
constexpr unsigned WS_O = WS_Q1, WS_Y = WS_H1;
constexpr unsigned SPIN_CAP = 1u << 16, LSPIN_CAP = 1u << 22;
constexpr int CODE_BYTES = 13312;
constexpr int L_RED = 0, L_WL = 16384, L_QS = L_WL + 20480, L_HS = L_QS + 256, L_ST = L_HS + 512, L_FLG = L_ST + 64, L_XL = L_FLG + 64, L_OS = L_XL + 2560, L_END = L_OS + 512;

#define RSRC(p, n) __builtin_amdgcn_make_buffer_rsrc((void*)(p), 0, (n), 0x00020000)
#define LD128(rs, vo, so, aux) __builtin_amdgcn_raw_buffer_load_b128((rs), (vo), (so), (aux))
#define ST128(v, rs, vo, so, aux) __builtin_amdgcn_raw_buffer_store_b128((v), (rs), (vo), (so), (aux))
#define SBAR() __builtin_amdgcn_sched_barrier(0)
#define CBAR() asm volatile("" ::: "memory")
#define LGKM0() asm volatile("s_waitcnt lgkmcnt(0)" ::: "memory")
#define LAS __attribute__((address_space(3)))
#define LDS_LD32(p) __hip_atomic_load((LAS unsigned*)(p), __ATOMIC_RELAXED, __HIP_MEMORY_SCOPE_WORKGROUP)
#define LDS_ST32(p, v) __hip_atomic_store((LAS unsigned*)(p), (v), __ATOMIC_RELAXED, __HIP_MEMORY_SCOPE_WORKGROUP)
#define LDS_LD64(p) __hip_atomic_load((LAS unsigned long long*)(p), __ATOMIC_RELAXED, __HIP_MEMORY_SCOPE_WORKGROUP)
#define LDS_ST64(p, v) __hip_atomic_store((LAS unsigned long long*)(p), (v), __ATOMIC_RELAXED, __HIP_MEMORY_SCOPE_WORKGROUP)
#define LDS_INC(p) asm volatile("ds_add_u32 %0, %1" :: "v"((unsigned)(size_t)(LAS unsigned*)(p)), "v"(1u) : "memory")

template <int CTRL> __device__ __forceinline__ unsigned dppu(unsigned x) { return (unsigned)__builtin_amdgcn_mov_dpp((int)x, CTRL, 0xf, 0xf, true); }
template <int CTRL> __device__ __forceinline__ float dppf(float x) { return __uint_as_float(dppu<CTRL>(__float_as_uint(x))); }
constexpr int XOR1 = 0xB1, XOR2 = 0x4E, XOR7 = 0x141, ROR8 = 0x128, ROR4 = 0x124, SHL4 = 0x104, SHR4 = 0x114, BCAST = 0x150;

__device__ __forceinline__ float dot2(unsigned k, unsigned q, float acc) {
  return __builtin_amdgcn_fdot2_f32_bf16(__builtin_bit_cast(bf16x2, k), __builtin_bit_cast(bf16x2, q), acc, false);
}
__device__ __forceinline__ unsigned pk_bf16(float lo, float hi) { bf16x2 v; v[0] = (__bf16)lo; v[1] = (__bf16)hi; return __builtin_bit_cast(unsigned, v); }
__device__ __forceinline__ float rbf(float f) { return __uint_as_float(pk_bf16(f, f) & 0xffff0000u); }
__device__ __forceinline__ float bflo(unsigned u) { return __uint_as_float(u << 16); }
__device__ __forceinline__ float bfhi(unsigned u) { return __uint_as_float(u & 0xffff0000u); }
__device__ __forceinline__ f32x4 mfma(u32x4 a, u32x4 b, f32x4 c) {
  return __builtin_amdgcn_mfma_f32_16x16x32_bf16(__builtin_bit_cast(bf16x8, a), __builtin_bit_cast(bf16x8, b), c, 0, 0, 0);
}
__device__ __forceinline__ float x16_sum(float x) { auto s = __builtin_amdgcn_permlane16_swap(__float_as_uint(x), __float_as_uint(x), false, false); return __uint_as_float(s[0]) + __uint_as_float(s[1]); }
__device__ __forceinline__ float x32_sum(float x) { auto s = __builtin_amdgcn_permlane32_swap(__float_as_uint(x), __float_as_uint(x), false, false); return __uint_as_float(s[0]) + __uint_as_float(s[1]); }
__device__ __forceinline__ float x16_max(float x) { auto s = __builtin_amdgcn_permlane16_swap(__float_as_uint(x), __float_as_uint(x), false, false); return fmaxf(__uint_as_float(s[0]), __uint_as_float(s[1])); }
__device__ __forceinline__ float x32_max(float x) { auto s = __builtin_amdgcn_permlane32_swap(__float_as_uint(x), __float_as_uint(x), false, false); return fmaxf(__uint_as_float(s[0]), __uint_as_float(s[1])); }
__device__ __forceinline__ float row_sum(float x) { x += dppf<XOR1>(x); x += dppf<XOR2>(x); x += dppf<ROR4>(x); x += dppf<ROR8>(x); return x; }
__device__ __forceinline__ float row_max(float x) { x = fmaxf(x, dppf<XOR1>(x)); x = fmaxf(x, dppf<XOR2>(x)); x = fmaxf(x, dppf<ROR4>(x)); x = fmaxf(x, dppf<ROR8>(x)); return x; }
__device__ __forceinline__ float oct_max(float x) { x = fmaxf(x, dppf<XOR1>(x)); x = fmaxf(x, dppf<XOR2>(x)); return fmaxf(x, dppf<XOR7>(x)); }
template <int C0, int C1> __device__ __forceinline__ float trsel(float lo, float hi, bool bit) {
  const float t0 = lo + dppf<C0>(lo), t1 = hi + dppf<C1>(hi);
  return bit ? t1 : t0;
}
__device__ __forceinline__ bool gok(u32x4 g, unsigned tag) { return (g[1] == tag) & (g[3] == tag); }
__device__ __forceinline__ u32x4 gran2(float a, float b, unsigned tag) { u32x4 g; g[0] = __float_as_uint(a); g[1] = tag; g[2] = __float_as_uint(b); g[3] = tag; return g; }
__device__ __forceinline__ float gv0(u32x4 g) { return __uint_as_float(g[0]); }
__device__ __forceinline__ float gv1(u32x4 g) { return __uint_as_float(g[2]); }
__device__ __forceinline__ void wg_barrier() { asm volatile("s_waitcnt lgkmcnt(0)\n\ts_barrier" ::: "memory"); }
__device__ __forceinline__ unsigned lds_flag(unsigned* p) { return __builtin_amdgcn_readfirstlane(LDS_LD32(p)); }
__device__ __forceinline__ f32x2 sum8(const float* p) {
  f32x2 a[8];
#pragma unroll
  for (int i = 0; i < 8; ++i) a[i] = *(const f32x2*)(p + i * 256);
  return ((a[0] + a[1]) + (a[2] + a[3])) + ((a[4] + a[5]) + (a[6] + a[7]));
}
__device__ __forceinline__ u32x2 trread(unsigned char* p) { return __builtin_bit_cast(u32x2, __builtin_amdgcn_ds_read_tr16_b64_v4i16((LAS s16x4*)p)); }
constexpr float LOG2E = 1.4426950408889634f;

__global__ void __launch_bounds__(640, 3)
fused_decode(const unsigned short* x, const unsigned short* kc, const unsigned short* vc,
             const unsigned short* wq, const unsigned short* wo, const unsigned short* w1,
             unsigned char* ws, const unsigned short* w2, unsigned short* out) {
  unsigned long long pc_entry;
  asm volatile("s_getpc_b64 %0" : "=s"(pc_entry));
  asm volatile("" : "+s"(x), "+s"(kc), "+s"(vc), "+s"(wq), "+s"(wo), "+s"(w1), "+s"(ws), "+s"(w2), "+s"(out));
  __shared__ __attribute__((aligned(16))) unsigned char smem[L_END];
  f32x4* red = (f32x4*)(smem + L_RED);
  unsigned* qs = (unsigned*)(smem + L_QS);
  unsigned* hs = (unsigned*)(smem + L_HS);
  float* st = (float*)(smem + L_ST);
  unsigned* flg = (unsigned*)(smem + L_FLG);
  unsigned* xl = (unsigned*)(smem + L_XL);
  unsigned* os = (unsigned*)(smem + L_OS);
  const int blk = blockIdx.x, tid = threadIdx.x, lane = tid & 63, w = __builtin_amdgcn_readfirstlane(tid >> 6);
  const int j = (blk >> 3) & 7, h = (blk & 7) + 8 * (blk >> 6), b = j >> 2, s = j & 3;
  const int r = lane >> 4, c = lane & 15;
  const bool ffn = h < 27;
  const unsigned cu = (unsigned)(h * 8 + j);
  const __amdgpu_buffer_rsrc_t rws = RSRC(ws, WS_END);
  const __amdgpu_buffer_rsrc_t rx = RSRC(x, 2560);
  u32x4 mask;
#pragma unroll
  for (int i = 0; i < 4; ++i) mask[i] = (i == ((c & 7) >> 1)) ? ((c & 1) ? 0xffff0000u : 0xffffu) : 0u;
  auto f_publish = [&](int cbase, unsigned tg) {
    const int cr = cbase + (lane >> 3), e = lane & 7;
    const float* p = (const float*)(red + 512) + ((e >> 2) * 16 + (cr & 15)) * 4 + (e & 3);
    float a0[8], a1[8];
#pragma unroll
    for (int i = 0; i < 8; ++i) { a0[i] = p[i * 256]; a1[i] = p[i * 256 + 128]; }
    const float v0 = ((a0[0] + a0[1]) + (a0[2] + a0[3])) + ((a0[4] + a0[5]) + (a0[6] + a0[7]));
    const float v1 = ((a1[0] + a1[1]) + (a1[2] + a1[3])) + ((a1[4] + a1[5]) + (a1[6] + a1[7]));
    ST128(gran2(v0, v1, tg), rws, cr < 10 ? cu * 1280 + (unsigned)(cr * 128 + e * 16) : OOB, WS_F, AUX_SC1);
  };

  if (w >= 2) {
    const int sw = w - 2;
    const size_t kvbase = ((size_t)(b * 32 + h) * 8192 + 6144 + s * 512) * 128;
    const __amdgpu_buffer_rsrc_t rk = RSRC(kc + kvbase, 131072), rv = RSRC(vc + kvbase, 131072);
    const int rot = sw * 4 + ((blk * ROTM) & 127);
    const int voff = lane * 16;
#define KVOFF(t) ((((((t) >> 2) * 32 + ((t) & 3) + rot) & 127) << 10))
    u32x4 K[16], V[16], WO[4], W2[2];
    float sc = 0.f;
    auto scores = [&]() {
      CBAR();
      const u32x4 q4 = *(const u32x4*)(qs + 4 * c);
      float sb[4];
#pragma unroll
      for (int be = 0; be < 4; ++be) {
        float d[4];
#pragma unroll
        for (int tt = 0; tt < 4; ++tt) {
          const u32x4 kk = K[4 * be + tt];
          d[tt] = dot2(kk[3], q4[3], dot2(kk[2], q4[2], dot2(kk[1], q4[1], dot2(kk[0], q4[0], 0.f))));
        }
        const float e0 = trsel<XOR1, XOR1>(d[0], d[1], c & 1);
        const float e1 = trsel<XOR1, XOR1>(d[2], d[3], c & 1);
        sb[be] = trsel<XOR2, XOR2>(e0, e1, c & 2);
      }
      const float f0 = trsel<SHL4, SHR4>(sb[0], sb[1], c & 4);
      const float f1 = trsel<SHL4, SHR4>(sb[2], sb[3], c & 4);
      const float dotv = trsel<ROR8, ROR8>(f0, f1, c & 8);
      sc = rbf(rbf(dotv) * 0.08838834764831845f);
      const float mw = x32_max(x16_max(row_max(sc)));
      const float lw = x32_sum(x16_sum(row_sum(__builtin_amdgcn_exp2f((sc - mw) * LOG2E))));
      if (lane == 0) LDS_ST64(st + 2 * sw, (unsigned long long)__float_as_uint(mw) | ((unsigned long long)__float_as_uint(lw) << 32));
    };
    unsigned cp0 = 0, cp1 = 0;
    if (sw == 7 && (blk >> 3) < 4) {
      const __amdgpu_buffer_rsrc_t rc = RSRC((void*)(pc_entry & ~255ull), CODE_BYTES);
      cp0 = __builtin_amdgcn_raw_buffer_load_b32(rc, lane * 128, 0, 0);
      cp1 = __builtin_amdgcn_raw_buffer_load_b32(rc, lane * 128, 8192, 0);
    }
    __builtin_amdgcn_s_setprio(3);
    wg_barrier();
#pragma unroll
    for (int i = 0; i < 24; ++i) {
      if (i == 16) __builtin_amdgcn_s_setprio(2);
      if (i < 16) K[i] = LD128(rk, voff, KVOFF(i), AUX_NT); else V[i - 16] = LD128(rv, voff, KVOFF(i - 16), AUX_NT);
      SBAR();
    }
    const bool early = lds_flag(flg + 1) != 0u;
    if (early) scores();
    SBAR();
    __builtin_amdgcn_s_setprio(1);
#pragma unroll
    for (int t = 8; t < 16; ++t) { V[t] = LD128(rv, voff, KVOFF(t), AUX_NT); SBAR(); }
    if (!early) {
      for (unsigned sp = 0; sp < LSPIN_CAP && lds_flag(flg + 1) == 0u; ++sp) __builtin_amdgcn_s_sleep(1);
      scores();
    }
    SBAR();
    __builtin_amdgcn_s_setprio(0);
    {
      const __amdgpu_buffer_rsrc_t ro = RSRC(wo + (size_t)h * 128 * 640 + j * 80, 128 * 1280 - j * 160);
      const __amdgpu_buffer_rsrc_t r2 = RSRC(w2 + (size_t)h * 64 * 640 + j * 80, ffn ? 64 * 1280 - j * 160 : 0);
      const unsigned vo4 = c < 10 ? (unsigned)(r * 4 * 1280 + c * 16) : OOB;
      const unsigned vo2 = c < 10 ? (unsigned)(r * 2 * 1280 + c * 16) : OOB;
#pragma unroll
      for (int i = 0; i < 4; ++i) { WO[i] = LD128(ro, vo4, (16 * sw + i) * 1280, AUX_NT); SBAR(); }
#pragma unroll
      for (int i = 0; i < 2; ++i) { W2[i] = LD128(r2, vo2, (8 * sw + i) * 1280, AUX_NT); SBAR(); }
    }
    asm volatile("" :: "v"(cp0), "v"(cp1));
    unsigned pp;
    {
      unsigned long long t_ = LDS_LD64(st + 2 * (lane & 7));
      for (unsigned sp = 0; !__all((unsigned)(t_ >> 32) != 0u) && sp < LSPIN_CAP; ++sp) { __builtin_amdgcn_s_sleep(1); t_ = LDS_LD64(st + 2 * (lane & 7)); }
      const float M = oct_max(__uint_as_float((unsigned)t_));
      const float pv = __builtin_amdgcn_exp2f((sc - M) * LOG2E);
      pp = pk_bf16(pv, pv);
    }
    const unsigned tagB = lds_flag(flg) ^ 0x80000000u;
    f32x4 oacc = {0.f, 0.f, 0.f, 0.f};
#define PVSTEP(t) oacc = mfma(mask & dppu<BCAST + (t)>(pp), V[t], oacc);
    PVSTEP(0) PVSTEP(1) PVSTEP(2) PVSTEP(3) PVSTEP(4) PVSTEP(5) PVSTEP(6) PVSTEP(7)
    PVSTEP(8) PVSTEP(9) PVSTEP(10) PVSTEP(11) PVSTEP(12) PVSTEP(13) PVSTEP(14) PVSTEP(15)
#undef PVSTEP
    red[sw * 64 + lane] = oacc;
    LGKM0();
    if (lane == 0) LDS_INC(flg + 4);
    if (ffn) {
      for (unsigned sp = 0; sp < LSPIN_CAP && lds_flag(flg + 2) == 0u; ++sp) __builtin_amdgcn_s_sleep(1);
      CBAR();
      const u32x2 hv = *(const u32x2*)(hs + (c >> 3) * 64 + 8 * sw + 2 * r);
      f32x4 facc = {0.f, 0.f, 0.f, 0.f};
      facc = mfma(mask & hv[0], W2[0], facc);
      facc = mfma(mask & hv[1], W2[1], facc);
      red[(8 + sw) * 64 + lane] = facc;
      LGKM0();
      if (lane == 0) LDS_INC(flg + 3);
    }
    if (c < 10) {
#pragma unroll
      for (int i = 0; i < 4; ++i) *(u32x4*)(smem + L_WL + (16 * sw + 4 * r + i) * 160 + c * 16) = WO[i];
    }
    const int tro = L_WL + (8 * r + ((lane >> 2) & 3)) * 160 + sw * 32 + (lane & 3) * 8;
    const unsigned yoff = lane < 16 ? cu * 1280 + (unsigned)(sw * 256 + lane * 16) : OOB;
    wg_barrier();
    if (sw < 5) {
      u32x4 A[4], B[4];
#pragma unroll
      for (int t = 0; t < 4; ++t) {
        A[t] = *(const u32x4*)(os + (c & 1) * 64 + t * 16 + r * 4);
        const u32x2 lo = trread(smem + tro + t * 5120), hi = trread(smem + tro + t * 5120 + 640);
        B[t] = u32x4{lo[0], lo[1], hi[0], hi[1]};
      }
      f32x4 y = {0.f, 0.f, 0.f, 0.f}, y2 = {0.f, 0.f, 0.f, 0.f};
      y = mfma(A[0], B[0], y); y2 = mfma(A[1], B[1], y2);
      y = mfma(A[2], B[2], y); y2 = mfma(A[3], B[3], y2);
      ST128(gran2(y[0] + y2[0], y[1] + y2[1], tagB), rws, yoff, WS_Y, AUX_SC1);
    }
  } else {
    __builtin_amdgcn_s_setprio(3);
    const bool isq = (w == 0);
    const unsigned epoch = __builtin_amdgcn_raw_buffer_load_b32(rws, 0, WS_EPOCH, AUX_SC1);
    u32x2 xs[5];
#pragma unroll
    for (int i = 0; i < 5; ++i) xs[i] = __builtin_amdgcn_raw_buffer_load_b64(rx, (lane >> 5) * 1280 + (lane & 31) * 8, i * 256, 0);
    const int row0 = isq ? 160 * (j >> 1) : 80 * j;
    const int nrow = isq ? 160 : 80;
    unsigned short xr[5];
#pragma unroll
    for (int i = 0; i < 5; ++i) xr[i] = __builtin_amdgcn_raw_buffer_load_b16(rx, (lane >> 5) * 1280 + (lane & 31) * 2, (row0 + 32 * i) * 2, 0);
    u32x4 wa[20];
    {
      const __amdgpu_buffer_rsrc_t rw = isq ? RSRC(wq + h * 128 + (j & 1) * 64, 640 * 4096 * 2 - h * 256 - (j & 1) * 128) : RSRC(w1 + h * 64, ffn ? 640 * 1728 * 2 - h * 128 : 0);
      const int rstride = isq ? 8192 : 3456;
      const unsigned vo = (unsigned)((lane >> 3) * rstride + (lane & 7) * 16);
#pragma unroll
      for (int i = 0; i < 10; ++i) wa[i] = LD128(rw, vo, (row0 + 8 * i) * rstride, AUX_NT);
      if (isq) {
#pragma unroll
        for (int i = 10; i < 20; ++i) wa[i] = LD128(rw, vo, (row0 + 8 * i) * rstride, AUX_NT);
      }
    }
    if (isq && lane < 24) LDS_ST32(st + lane, 0u);
    wg_barrier();
    float xf[20];
#pragma unroll
    for (int i = 0; i < 5; ++i) { xf[4 * i] = bflo(xs[i][0]); xf[4 * i + 1] = bfhi(xs[i][0]); xf[4 * i + 2] = bflo(xs[i][1]); xf[4 * i + 3] = bfhi(xs[i][1]); }
    float sum = 0.f;
#pragma unroll
    for (int i = 0; i < 20; ++i) sum += xf[i];
    sum = x16_sum(row_sum(sum));
    const float mean = sum * (1.f / 640.f);
    float vs = 0.f;
#pragma unroll
    for (int i = 0; i < 20; ++i) { const float d = xf[i] - mean; vs = fmaf(d, d, vs); }
    vs = x16_sum(row_sum(vs));
    const float mb = rbf(mean), den = rbf(sqrtf(rbf(rbf(vs * (1.f / 640.f)) + 1e-5f)));
    unsigned* xw_ = xl + w * 320;
#pragma unroll
    for (int i = 0; i < 5; ++i) {
      const int row = (lane & 31) + 32 * i;
      const float xn = rbf(bflo(xr[i]) - mb) / den;
      if (row < nrow) xw_[(lane >> 5) * 160 + ((row >> 1) & 3) * 40 + (row >> 3) * 2 + (row & 1)] = pk_bf16(xn, xn);
    }
    const unsigned tag = __builtin_amdgcn_readfirstlane(epoch) + 0x9E3779B9u, tagB = tag ^ 0x80000000u;
    if (isq && lane == 0) LDS_ST32(flg, tag);
    LGKM0();
    f32x4 acc0 = {0.f, 0.f, 0.f, 0.f}, acc1 = {0.f, 0.f, 0.f, 0.f};
    const unsigned* xrd = xw_ + (c >> 3) * 160 + r * 40;
#pragma unroll
    for (int i = 0; i < 10; i += 2) {
      const u32x4 xa = *(const u32x4*)(xrd + 2 * i);
      acc0 = mfma(mask & xa[0], wa[i], acc0); acc1 = mfma(mask & xa[1], wa[i], acc1);
      acc0 = mfma(mask & xa[2], wa[i + 1], acc0); acc1 = mfma(mask & xa[3], wa[i + 1], acc1);
    }
    if (isq) {
#pragma unroll
      for (int i = 10; i < 20; i += 2) {
        const u32x4 xa = *(const u32x4*)(xrd + 2 * i);
        acc0 = mfma(mask & xa[0], wa[i], acc0); acc1 = mfma(mask & xa[1], wa[i], acc1);
        acc0 = mfma(mask & xa[2], wa[i + 1], acc0); acc1 = mfma(mask & xa[3], wa[i + 1], acc1);
      }
    }
    f32x4 v;
#pragma unroll
    for (int i = 0; i < 4; ++i) { const float t_ = c < 8 ? acc0[i] : acc1[i]; v[i] = t_ + dppf<ROR8>(t_); }
    const u32x4 pub = gran2(c < 8 ? v[0] : v[2], c < 8 ? v[1] : v[3], tag);
    if (isq) {
      ST128(pub, rws, cu * 1024 + lane * 16, WS_Q1, AUX_SC1);
      __builtin_amdgcn_s_sleep(PDELAY);
      const unsigned goff = (unsigned)((h * 8 + (lane >> 5)) * 1024 + (b * 32 + (lane & 31)) * 16);
      u32x4 G[8];
#pragma unroll
      for (int p = 0; p < 4; ++p) G[p] = LD128(rws, goff, WS_Q1 + p * 2048, AUX_SC1);
      for (unsigned sp = 0;; ++sp) {
        bool allok = true;
        CBAR();
#pragma unroll
        for (int p = 0; p < 4; ++p) if (!__all(gok(G[p], tag))) { allok = false; G[p] = LD128(rws, goff, WS_Q1 + p * 2048, AUX_SC1); }
        if (allok || sp > SPIN_CAP) break;
      }
      qs[(lane >> 5) * 32 + (c & 7) * 4 + (r & 1) * 2 + (c >> 3)] = pk_bf16((gv0(G[0]) + gv0(G[1])) + (gv0(G[2]) + gv0(G[3])), (gv1(G[0]) + gv1(G[1])) + (gv1(G[2]) + gv1(G[3])));
      ST128(gran2(0.f, 0.f, tag), rws, lane == 0 ? cu * 128 + 16 : OOB, WS_S, AUX_SC1);
      LGKM0();
      if (lane == 0) LDS_ST32(flg + 1, 1u);
      const unsigned soff = WS_S + (unsigned)((h * 8 + (lane & 7)) * 128 + (lane & 8) * 2);
      __builtin_amdgcn_s_sleep(ACKDELAY);
      u32x4 gs = LD128(rws, soff, 0, AUX_SC1);
      unsigned long long t_ = LDS_LD64(st + 2 * (lane & 7));
      for (unsigned sp = 0; !__all((unsigned)(t_ >> 32) != 0u) && sp < LSPIN_CAP; ++sp) { __builtin_amdgcn_s_sleep(2); t_ = LDS_LD64(st + 2 * (lane & 7)); }
      {
        const float m_ = __uint_as_float((unsigned)t_), l_ = __uint_as_float((unsigned)(t_ >> 32));
        const float M = oct_max(m_);
        float L = __builtin_amdgcn_exp2f((m_ - M) * LOG2E) * l_; L += dppf<XOR1>(L); L += dppf<XOR2>(L); L += dppf<XOR7>(L);
        ST128(gran2(M, L, tag), rws, lane == 0 ? cu * 128 : OOB, WS_S, AUX_SC1);
      }
      if (!__all(gok(gs, tag) | !(lane & 8))) {
        for (unsigned sp = 0; sp < SPIN_CAP; ++sp) {
          CBAR();
          gs = LD128(rws, soff, 0, AUX_SC1);
          if (__all(gok(gs, tag) | !(lane & 8))) break;
        }
      }
      for (unsigned sp = 0; sp < LSPIN_CAP && lds_flag(flg + 4) != 8u; ++sp) {}
      CBAR();
      const unsigned ooff = (unsigned)(h * 8 * 1024 + lane * 16);
      const f32x2 own = sum8((const float*)red + (((lane >> 1) & 1) * 16 + (lane >> 2)) * 4 + 2 * (lane & 1));
      const u32x4 ownG = gran2(own[0], own[1], tagB);
      ST128(ownG, rws, ooff, WS_O + j * 1024, AUX_SC1);
      gs = LD128(rws, soff, 0, AUX_SC1);
#pragma unroll
      for (int p = 0; p < 8; ++p) G[p] = LD128(rws, p == j ? OOB : ooff, WS_O + p * 1024, AUX_SC1);
#pragma unroll
      for (int p = 0; p < 8; ++p) if (p == j) G[p] = ownG;
      for (unsigned sp = 0; !__all(gok(gs, tag)) && sp < SPIN_CAP; ++sp) { CBAR(); __builtin_amdgcn_s_sleep(2); gs = LD128(rws, soff, 0, AUX_SC1); }
      float wp[8];
      {
        const float ms = gv0(gs), ls = gv1(gs);
        float Mg = fmaxf(ms, dppf<XOR1>(ms)); Mg = fmaxf(Mg, dppf<XOR2>(Mg));
        const float e_ = __builtin_amdgcn_exp2f((ms - Mg) * LOG2E);
        float Lg = e_ * ls; Lg += dppf<XOR1>(Lg); Lg += dppf<XOR2>(Lg);
        const float wgt = e_ / Lg;
#pragma unroll
        for (int p = 0; p < 8; ++p) wp[p] = __uint_as_float(__builtin_amdgcn_readlane(__float_as_uint(wgt), p));
      }
      for (unsigned sp = 0;; ++sp) {
        bool allok = true;
        CBAR();
#pragma unroll
        for (int p = 0; p < 8; ++p) if (!__all(gok(G[p], tagB))) { allok = false; G[p] = LD128(rws, ooff, WS_O + p * 1024, AUX_SC1); }
        if (allok || sp > SPIN_CAP) break;
      }
#pragma unroll
      for (int bb = 0; bb < 2; ++bb) {
        float o0 = 0.f, o1 = 0.f;
#pragma unroll
        for (int sp = 0; sp < 4; ++sp) { o0 = fmaf(wp[4 * bb + sp], gv0(G[4 * bb + sp]), o0); o1 = fmaf(wp[4 * bb + sp], gv1(G[4 * bb + sp]), o1); }
        os[bb * 64 + lane] = pk_bf16(o0, o1);
      }
      wg_barrier();
    } else {
      if (ffn) {
        ST128(pub, rws, (unsigned)((h * 8 + (c & 7)) * 1280 + j * 128 + (c >> 3) * 64 + r * 16), WS_H1, AUX_SC1);
        __builtin_amdgcn_s_sleep(2 * PDELAY);
        const unsigned goff = cu * 1280 + lane * 16;
        u32x4 G0 = LD128(rws, goff, WS_H1, AUX_SC1);
        for (unsigned sp = 0; !__all(gok(G0, tag)) && sp < SPIN_CAP; ++sp) { CBAR(); G0 = LD128(rws, goff, WS_H1, AUX_SC1); }
        float a0 = gv0(G0), a1 = gv1(G0);
        a0 += dppf<ROR8>(a0); a1 += dppf<ROR8>(a1);
        a0 = fmaxf(x32_sum(x16_sum(a0)), 0.f); a1 = fmaxf(x32_sum(x16_sum(a1)), 0.f);
        u32x2 g2; g2[0] = pk_bf16(a0, a1); g2[1] = tag;
        __builtin_amdgcn_raw_buffer_store_b64(g2, rws, lane < 8 ? cu * 128 + lane * 8 : OOB, WS_H2, AUX_SC1);
        const unsigned g2off = (unsigned)((h * 8 + (lane >> 3)) * 128 + (lane & 7) * 8);
        __builtin_amdgcn_s_sleep(3 * PDELAY);
        u32x2 q2 = __builtin_amdgcn_raw_buffer_load_b64(rws, g2off, WS_H2, AUX_SC1);
        for (unsigned sp = 0; !__all(q2[1] == tag) && sp < SPIN_CAP; ++sp) { CBAR(); q2 = __builtin_amdgcn_raw_buffer_load_b64(rws, g2off, WS_H2, AUX_SC1); }
        u32x2 hv; hv[0] = (q2[0] & 0xffffu) | (q2[0] << 16); hv[1] = (q2[0] >> 16) | (q2[0] & 0xffff0000u);
        *(u32x2*)(hs + ((lane >> 1) & 1) * 64 + (lane >> 3) * 8 + (lane & 1) * 4 + ((lane >> 2) & 1) * 2) = hv;
        LGKM0();
        if (lane == 0) LDS_ST32(flg + 2, 1u);
        for (unsigned sp = 0; sp < LSPIN_CAP && lds_flag(flg + 3) != 8u; ++sp) __builtin_amdgcn_s_sleep(1);
        CBAR();
        f_publish(0, tag);
        f_publish(8, tag);
      }
      const int e = lane & 7, ocol = j * 80 + 8 * h + e;
      const bool fin = h < 10, f3v = (lane >> 3) < 3;
      const unsigned yo = (unsigned)(((lane >> 3) * 8 + j) * 1280 + h * 128 + e * 16);
      unsigned short x0 = 0, x1 = 0;
      u32x4 g[4], y[4];
      if (fin) { x0 = __builtin_amdgcn_raw_buffer_load_b16(rx, ocol * 2, 0, 0); x1 = __builtin_amdgcn_raw_buffer_load_b16(rx, ocol * 2, 1280, 0); }
      wg_barrier();
      if (fin) {
#pragma unroll
        for (int i = 0; i < 4; ++i) g[i] = LD128(rws, (i < 3 || f3v) ? yo : OOB, WS_F + i * 64 * 1280, AUX_SC1);
        __builtin_amdgcn_s_sleep(YDELAY);
#pragma unroll
        for (int i = 0; i < 4; ++i) y[i] = LD128(rws, yo, WS_Y + i * 64 * 1280, AUX_SC1);
        for (unsigned sp = 0;; ++sp) {
          bool allok = true;
          CBAR();
#pragma unroll
          for (int i = 0; i < 4; ++i) if (!__all(gok(g[i], tag) | (i == 3 && !f3v))) { allok = false; g[i] = LD128(rws, (i < 3 || f3v) ? yo : OOB, WS_F + i * 64 * 1280, AUX_SC1); }
          if (allok || sp > SPIN_CAP) break;
        }
        float fa = (gv0(g[0]) + gv0(g[1])) + (gv0(g[2]) + gv0(g[3])), fb = (gv1(g[0]) + gv1(g[1])) + (gv1(g[2]) + gv1(g[3]));
        fa += dppf<ROR8>(fa); fb += dppf<ROR8>(fb);
        fa = rbf(x32_sum(x16_sum(fa))); fb = rbf(x32_sum(x16_sum(fb)));
        const float ga = rbf(fa / (1.f + __expf(-fa))), gb = rbf(fb / (1.f + __expf(-fb)));
        for (unsigned sp = 0;; ++sp) {
          bool allok = true;
          CBAR();
#pragma unroll
          for (int i = 0; i < 4; ++i) if (!__all(gok(y[i], tagB))) { allok = false; y[i] = LD128(rws, yo, WS_Y + i * 64 * 1280, AUX_SC1); }
          if (allok || sp > SPIN_CAP) break;
        }
        float ya = (gv0(y[0]) + gv0(y[1])) + (gv0(y[2]) + gv0(y[3])), yb = (gv1(y[0]) + gv1(y[1])) + (gv1(y[2]) + gv1(y[3]));
        ya += dppf<ROR8>(ya); yb += dppf<ROR8>(yb);
        ya = rbf(x32_sum(x16_sum(ya))); yb = rbf(x32_sum(x16_sum(yb)));
        const unsigned vv = pk_bf16(bflo(x0) + rbf(ya * ga), bflo(x1) + rbf(yb * gb)), nb = dppu<XOR1>(vv);
        const unsigned res = (e & 1) ? ((nb >> 16) | (vv & 0xffff0000u)) : ((vv & 0xffffu) | (nb << 16));
        __builtin_amdgcn_raw_buffer_store_b32(res, RSRC(out, 2560), lane < 8 ? (unsigned)(((e & 1) * 640 + (ocol & ~1)) * 2) : OOB, 0, 0);
        if (blk == 0 && lane == 0) __builtin_amdgcn_raw_buffer_store_b32(tag, rws, 0, WS_EPOCH, AUX_SC1);
      }
    }
  }
}

extern "C" void kernel_launch(void* const* d_in, const int* in_sizes, int n_in, void* d_out, int out_size, void* d_ws, size_t ws_size, hipStream_t stream) {
  fused_decode<<<256, 640, 0, stream>>>((const unsigned short*)d_in[0], (const unsigned short*)d_in[1], (const unsigned short*)d_in[2],
      (const unsigned short*)d_in[3], (const unsigned short*)d_in[4], (const unsigned short*)d_in[6],
      (unsigned char*)d_ws, (const unsigned short*)d_in[7], (unsigned short*)d_out);
}
```

```cpp
#define LOADFIRST 0
#define REDW_ 1
#include <hip/hip_runtime.h>
#include <hip/hip_bf16.h>
#include <stdint.h>

#define STAMP(i) do {} while (0)
#define STAMP_DECL
#define STAMP_FLUSH() do {} while (0)

typedef unsigned u32x4 __attribute__((ext_vector_type(4)));
typedef unsigned u32x2 __attribute__((ext_vector_type(2)));
typedef float f32x4 __attribute__((ext_vector_type(4)));
typedef float f32x2 __attribute__((ext_vector_type(2)));
typedef __bf16 bf16x8 __attribute__((ext_vector_type(8)));
typedef __bf16 bf16x2 __attribute__((ext_vector_type(2)));
typedef short s16x4 __attribute__((ext_vector_type(4)));

#define PDELAY 32
#define ACKDELAY 48
#define YSLEEP 0
#define OSLEEP 0
#define YDELAY 4
#define ROTM 37
constexpr int AUX_NT = 2, AUX_SC1 = 16;
constexpr unsigned OOB = 0x7ffffff0u;
constexpr unsigned WS_EPOCH = 0;
constexpr unsigned WS_Q1 = 4096;
constexpr unsigned WS_H1 = WS_Q1 + 256 * 1024;
constexpr unsigned WS_H2 = WS_H1 + 256 * 1280;
constexpr unsigned WS_S = WS_H2 + 256 * 128;
constexpr unsigned WS_F = WS_S + 256 * 128;
constexpr unsigned WS_END = WS_F + 256 * 1280;
constexpr unsigned WS_O = WS_Q1, WS_Y = WS_H1;
constexpr unsigned SPIN_CAP = 1u << 16, LSPIN_CAP = 1u << 22;
constexpr int CODE_BYTES = 13312;
constexpr int L_RED = 0, L_WL = 16384, L_QS = L_WL + 20480, L_HS = L_QS + 256, L_ST = L_HS + 512, L_FLG = L_ST + 64, L_XL = L_FLG + 64, L_OS = L_XL + 2560, L_END = L_OS + 512;

#define RSRC(p, n) __builtin_amdgcn_make_buffer_rsrc((void*)(p), 0, (n), 0x00020000)
#define LD128(rs, vo, so, aux) __builtin_amdgcn_raw_buffer_load_b128((rs), (vo), (so), (aux))
#define ST128(v, rs, vo, so, aux) __builtin_amdgcn_raw_buffer_store_b128((v), (rs), (vo), (so), (aux))
#define SBAR() __builtin_amdgcn_sched_barrier(0)
#define CBAR() asm volatile("" ::: "memory")
#define LGKM0() asm volatile("s_waitcnt lgkmcnt(0)" ::: "memory")
#define LAS __attribute__((address_space(3)))
#define LDS_LD32(p) __hip_atomic_load((LAS unsigned*)(p), __ATOMIC_RELAXED, __HIP_MEMORY_SCOPE_WORKGROUP)
#define LDS_ST32(p, v) __hip_atomic_store((LAS unsigned*)(p), (v), __ATOMIC_RELAXED, __HIP_MEMORY_SCOPE_WORKGROUP)
#define LDS_LD64(p) __hip_atomic_load((LAS unsigned long long*)(p), __ATOMIC_RELAXED, __HIP_MEMORY_SCOPE_WORKGROUP)
#define LDS_ST64(p, v) __hip_atomic_store((LAS unsigned long long*)(p), (v), __ATOMIC_RELAXED, __HIP_MEMORY_SCOPE_WORKGROUP)
#define LDS_INC(p) asm volatile("ds_add_u32 %0, %1" :: "v"((unsigned)(size_t)(LAS unsigned*)(p)), "v"(1u) : "memory")

template <int CTRL> __device__ __forceinline__ unsigned dppu(unsigned x) { return (unsigned)__builtin_amdgcn_mov_dpp((int)x, CTRL, 0xf, 0xf, true); }
template <int CTRL> __device__ __forceinline__ float dppf(float x) { return __uint_as_float(dppu<CTRL>(__float_as_uint(x))); }
constexpr int XOR1 = 0xB1, XOR2 = 0x4E, XOR7 = 0x141, ROR8 = 0x128, ROR4 = 0x124, SHL4 = 0x104, SHR4 = 0x114, BCAST = 0x150;

__device__ __forceinline__ float dot2(unsigned k, unsigned q, float acc) {
  return __builtin_amdgcn_fdot2_f32_bf16(__builtin_bit_cast(bf16x2, k), __builtin_bit_cast(bf16x2, q), acc, false);
}
__device__ __forceinline__ unsigned pk_bf16(float lo, float hi) { bf16x2 v; v[0] = (__bf16)lo; v[1] = (__bf16)hi; return __builtin_bit_cast(unsigned, v); }
__device__ __forceinline__ float rbf(float f) { return __uint_as_float(pk_bf16(f, f) & 0xffff0000u); }
__device__ __forceinline__ float bflo(unsigned u) { return __uint_as_float(u << 16); }
__device__ __forceinline__ float bfhi(unsigned u) { return __uint_as_float(u & 0xffff0000u); }
__device__ __forceinline__ f32x4 mfma(u32x4 a, u32x4 b, f32x4 c) {
  return __builtin_amdgcn_mfma_f32_16x16x32_bf16(__builtin_bit_cast(bf16x8, a), __builtin_bit_cast(bf16x8, b), c, 0, 0, 0);
}
__device__ __forceinline__ float x16_sum(float x) { auto s = __builtin_amdgcn_permlane16_swap(__float_as_uint(x), __float_as_uint(x), false, false); return __uint_as_float(s[0]) + __uint_as_float(s[1]); }
__device__ __forceinline__ float x32_sum(float x) { auto s = __builtin_amdgcn_permlane32_swap(__float_as_uint(x), __float_as_uint(x), false, false); return __uint_as_float(s[0]) + __uint_as_float(s[1]); }
__device__ __forceinline__ float x16_max(float x) { auto s = __builtin_amdgcn_permlane16_swap(__float_as_uint(x), __float_as_uint(x), false, false); return fmaxf(__uint_as_float(s[0]), __uint_as_float(s[1])); }
__device__ __forceinline__ float x32_max(float x) { auto s = __builtin_amdgcn_permlane32_swap(__float_as_uint(x), __float_as_uint(x), false, false); return fmaxf(__uint_as_float(s[0]), __uint_as_float(s[1])); }
__device__ __forceinline__ float row_sum(float x) { x += dppf<XOR1>(x); x += dppf<XOR2>(x); x += dppf<ROR4>(x); x += dppf<ROR8>(x); return x; }
__device__ __forceinline__ float row_max(float x) { x = fmaxf(x, dppf<XOR1>(x)); x = fmaxf(x, dppf<XOR2>(x)); x = fmaxf(x, dppf<ROR4>(x)); x = fmaxf(x, dppf<ROR8>(x)); return x; }
__device__ __forceinline__ float oct_max(float x) { x = fmaxf(x, dppf<XOR1>(x)); x = fmaxf(x, dppf<XOR2>(x)); return fmaxf(x, dppf<XOR7>(x)); }
template <int C0, int C1> __device__ __forceinline__ float trsel(float lo, float hi, bool bit) {
  const float t0 = lo + dppf<C0>(lo), t1 = hi + dppf<C1>(hi);
  return bit ? t1 : t0;
}
__device__ __forceinline__ bool gok(u32x4 g, unsigned tag) { return (g[1] == tag) & (g[3] == tag); }
__device__ __forceinline__ u32x4 gran2(float a, float b, unsigned tag) { u32x4 g; g[0] = __float_as_uint(a); g[1] = tag; g[2] = __float_as_uint(b); g[3] = tag; return g; }
__device__ __forceinline__ float gv0(u32x4 g) { return __uint_as_float(g[0]); }
__device__ __forceinline__ float gv1(u32x4 g) { return __uint_as_float(g[2]); }
__device__ __forceinline__ void wg_barrier() { asm volatile("s_waitcnt lgkmcnt(0)\n\ts_barrier" ::: "memory"); }
__device__ __forceinline__ unsigned lds_flag(unsigned* p) { return __builtin_amdgcn_readfirstlane(LDS_LD32(p)); }
__device__ __forceinline__ f32x2 sum8(const float* p) {
  f32x2 a[8];
#pragma unroll
  for (int i = 0; i < 8; ++i) a[i] = *(const f32x2*)(p + i * 256);
  return ((a[0] + a[1]) + (a[2] + a[3])) + ((a[4] + a[5]) + (a[6] + a[7]));
}
__device__ __forceinline__ u32x2 trread(unsigned char* p) { return __builtin_bit_cast(u32x2, __builtin_amdgcn_ds_read_tr16_b64_v4i16((LAS s16x4*)p)); }
constexpr float LOG2E = 1.4426950408889634f;

__global__ void __launch_bounds__(640, 3)
fused_decode(const unsigned short* x, const unsigned short* kc, const unsigned short* vc,
             const unsigned short* wq, const unsigned short* wo, const unsigned short* w1,
             unsigned char* ws, const unsigned short* w2, unsigned short* out) {
  unsigned long long pc_entry;
  asm volatile("s_getpc_b64 %0" : "=s"(pc_entry));
  asm volatile("" : "+s"(x), "+s"(kc), "+s"(vc), "+s"(wq), "+s"(wo), "+s"(w1), "+s"(ws), "+s"(w2), "+s"(out));
  __shared__ __attribute__((aligned(16))) unsigned char smem[L_END];
  f32x4* red = (f32x4*)(smem + L_RED);
  unsigned* qs = (unsigned*)(smem + L_QS);
  unsigned* hs = (unsigned*)(smem + L_HS);
  float* st = (float*)(smem + L_ST);
  unsigned* flg = (unsigned*)(smem + L_FLG);
  unsigned* xl = (unsigned*)(smem + L_XL);
  unsigned* os = (unsigned*)(smem + L_OS);
  const int blk = blockIdx.x, tid = threadIdx.x, lane = tid & 63, w = __builtin_amdgcn_readfirstlane(tid >> 6);
  STAMP_DECL
  const unsigned tstart_ = (unsigned)__builtin_amdgcn_s_memrealtime();
  const int j = (blk >> 3) & 7, h = (blk & 7) + 8 * (blk >> 6), b = j >> 2, s = j & 3;
  const int r = lane >> 4, c = lane & 15;
  const bool ffn = h < 27;
  const unsigned cu = (unsigned)(h * 8 + j);
  const __amdgpu_buffer_rsrc_t rws = RSRC(ws, WS_END);
  const __amdgpu_buffer_rsrc_t rx = RSRC(x, 2560);
  u32x4 mask;
#pragma unroll
  for (int i = 0; i < 4; ++i) mask[i] = (i == ((c & 7) >> 1)) ? ((c & 1) ? 0xffff0000u : 0xffffu) : 0u;
  auto f_publish = [&](int cbase, unsigned tg) {
    const int cr = cbase + (lane >> 3), e = lane & 7;
    const float* p = (const float*)(red + 512) + ((e >> 2) * 16 + (cr & 15)) * 4 + (e & 3);
    float a0[8], a1[8];
#pragma unroll
    for (int i = 0; i < 8; ++i) { a0[i] = p[i * 256]; a1[i] = p[i * 256 + 128]; }
    const float v0 = ((a0[0] + a0[1]) + (a0[2] + a0[3])) + ((a0[4] + a0[5]) + (a0[6] + a0[7]));
    const float v1 = ((a1[0] + a1[1]) + (a1[2] + a1[3])) + ((a1[4] + a1[5]) + (a1[6] + a1[7]));
    ST128(gran2(v0, v1, tg), rws, cr < 10 ? cu * 1280 + (unsigned)(cr * 128 + e * 16) : OOB, WS_F, AUX_SC1);
  };

  if (w >= 2) {
    const int sw = w - 2;
    const size_t kvbase = ((size_t)(b * 32 + h) * 8192 + 6144 + s * 512) * 128;
    const __amdgpu_buffer_rsrc_t rk = RSRC(kc + kvbase, 131072), rv = RSRC(vc + kvbase, 131072);
    const int rot = sw * 4 + ((blk * ROTM) & 127);
    const int voff = lane * 16;
#define KVOFF(t) ((((((t) >> 2) * 32 + ((t) & 3) + rot) & 127) << 10))
    u32x4 K[16], V[16], WO[4], W2[2];
    float sc = 0.f;
    auto scores = [&]() {
      CBAR();
      const u32x4 q4 = *(const u32x4*)(qs + 4 * c);
      float sb[4];
#pragma unroll
      for (int be = 0; be < 4; ++be) {
        float d[4];
#pragma unroll
        for (int tt = 0; tt < 4; ++tt) {
          const u32x4 kk = K[4 * be + tt];
          d[tt] = dot2(kk[3], q4[3], dot2(kk[2], q4[2], dot2(kk[1], q4[1], dot2(kk[0], q4[0], 0.f))));
        }
        const float e0 = trsel<XOR1, XOR1>(d[0], d[1], c & 1);
        const float e1 = trsel<XOR1, XOR1>(d[2], d[3], c & 1);
        sb[be] = trsel<XOR2, XOR2>(e0, e1, c & 2);
      }
      const float f0 = trsel<SHL4, SHR4>(sb[0], sb[1], c & 4);
      const float f1 = trsel<SHL4, SHR4>(sb[2], sb[3], c & 4);
      const float dotv = trsel<ROR8, ROR8>(f0, f1, c & 8);
      sc = rbf(rbf(dotv) * 0.08838834764831845f);
      const float mw = x32_max(x16_max(row_max(sc)));
      const float lw = x32_sum(x16_sum(row_sum(__builtin_amdgcn_exp2f((sc - mw) * LOG2E))));
      if (lane == 0) LDS_ST64(st + 2 * sw, (unsigned long long)__float_as_uint(mw) | ((unsigned long long)__float_as_uint(lw) << 32));
    };
    unsigned cp0 = 0, cp1 = 0;
    if (sw == 7 && (blk >> 3) < 4) {
      const __amdgpu_buffer_rsrc_t rc = RSRC((void*)(pc_entry & ~255ull), CODE_BYTES);
      cp0 = __builtin_amdgcn_raw_buffer_load_b32(rc, lane * 128, 0, 0);
      cp1 = __builtin_amdgcn_raw_buffer_load_b32(rc, lane * 128, 8192, 0);
    }
    __builtin_amdgcn_s_setprio(3);
    wg_barrier();
#pragma unroll
    for (int i = 0; i < 24; ++i) {
      if (i == 16) __builtin_amdgcn_s_setprio(2);
      if (i < 16) K[i] = LD128(rk, voff, KVOFF(i), AUX_NT); else V[i - 16] = LD128(rv, voff, KVOFF(i - 16), AUX_NT);
      SBAR();
    }
    const bool early = lds_flag(flg + 1) != 0u;
    if (early) scores();
    SBAR();
    __builtin_amdgcn_s_setprio(1);
#pragma unroll
    for (int t = 8; t < 16; ++t) { V[t] = LD128(rv, voff, KVOFF(t), AUX_NT); SBAR(); }
    if (!early) {
      for (unsigned sp = 0; sp < LSPIN_CAP && lds_flag(flg + 1) == 0u; ++sp) __builtin_amdgcn_s_sleep(1);
      scores();
    }
    SBAR();
    __builtin_amdgcn_s_setprio(0);
    {
      const __amdgpu_buffer_rsrc_t ro = RSRC(wo + (size_t)h * 128 * 640 + j * 80, 128 * 1280 - j * 160);
      const __amdgpu_buffer_rsrc_t r2 = RSRC(w2 + (size_t)h * 64 * 640 + j * 80, ffn ? 64 * 1280 - j * 160 : 0);
      const unsigned vo4 = c < 10 ? (unsigned)(r * 4 * 1280 + c * 16) : OOB;
      const unsigned vo2 = c < 10 ? (unsigned)(r * 2 * 1280 + c * 16) : OOB;
#pragma unroll
      for (int i = 0; i < 4; ++i) { WO[i] = LD128(ro, vo4, (16 * sw + i) * 1280, AUX_NT); SBAR(); }
#pragma unroll
      for (int i = 0; i < 2; ++i) { W2[i] = LD128(r2, vo2, (8 * sw + i) * 1280, AUX_NT); SBAR(); }
    }
    asm volatile("" :: "v"(cp0), "v"(cp1));
    unsigned pp;
    {
      unsigned long long t_ = LDS_LD64(st + 2 * (lane & 7));
      for (unsigned sp = 0; !__all((unsigned)(t_ >> 32) != 0u) && sp < LSPIN_CAP; ++sp) { __builtin_amdgcn_s_sleep(1); t_ = LDS_LD64(st + 2 * (lane & 7)); }
      const float M = oct_max(__uint_as_float((unsigned)t_));
      const float pv = __builtin_amdgcn_exp2f((sc - M) * LOG2E);
      pp = pk_bf16(pv, pv);
    }
    const unsigned tagB = lds_flag(flg) ^ 0x80000000u;
    f32x4 oacc = {0.f, 0.f, 0.f, 0.f};
#define PVSTEP(t) oacc = mfma(mask & dppu<BCAST + (t)>(pp), V[t], oacc);
    PVSTEP(0) PVSTEP(1) PVSTEP(2) PVSTEP(3) PVSTEP(4) PVSTEP(5) PVSTEP(6) PVSTEP(7)
    PVSTEP(8) PVSTEP(9) PVSTEP(10) PVSTEP(11) PVSTEP(12) PVSTEP(13) PVSTEP(14) PVSTEP(15)
#undef PVSTEP
    red[sw * 64 + lane] = oacc;
    LGKM0();
    if (lane == 0) LDS_INC(flg + 4);
    if (ffn) {
      for (unsigned sp = 0; sp < LSPIN_CAP && lds_flag(flg + 2) == 0u; ++sp) __builtin_amdgcn_s_sleep(1);
      CBAR();
      const u32x2 hv = *(const u32x2*)(hs + (c >> 3) * 64 + 8 * sw + 2 * r);
      f32x4 facc = {0.f, 0.f, 0.f, 0.f};
      facc = mfma(mask & hv[0], W2[0], facc);
      facc = mfma(mask & hv[1], W2[1], facc);
      red[(8 + sw) * 64 + lane] = facc;
      LGKM0();
      if (lane == 0) LDS_INC(flg + 3);
    }
    if (c < 10) {
#pragma unroll
      for (int i = 0; i < 4; ++i) *(u32x4*)(smem + L_WL + (16 * sw + 4 * r + i) * 160 + c * 16) = WO[i];
    }
    const int tro = L_WL + (8 * r + ((lane >> 2) & 3)) * 160 + sw * 32 + (lane & 3) * 8;
    const unsigned yoff = lane < 16 ? cu * 1280 + (unsigned)(sw * 256 + lane * 16) : OOB;
    wg_barrier();
    if (sw < 5) {
      u32x4 A[4], B[4];
#pragma unroll
      for (int t = 0; t < 4; ++t) {
        A[t] = *(const u32x4*)(os + (c & 1) * 64 + t * 16 + r * 4);
        const u32x2 lo = trread(smem + tro + t * 5120), hi = trread(smem + tro + t * 5120 + 640);
        B[t] = u32x4{lo[0], lo[1], hi[0], hi[1]};
      }
      f32x4 y = {0.f, 0.f, 0.f, 0.f}, y2 = {0.f, 0.f, 0.f, 0.f};
      y = mfma(A[0], B[0], y); y2 = mfma(A[1], B[1], y2);
      y = mfma(A[2], B[2], y); y2 = mfma(A[3], B[3], y2);
      ST128(gran2(y[0] + y2[0], y[1] + y2[1], tagB), rws, yoff, WS_Y, AUX_SC1);
    }
  } else {
    __builtin_amdgcn_s_setprio(3);
    const bool isq = (w == 0);
    const unsigned epoch = __builtin_amdgcn_raw_buffer_load_b32(rws, 0, WS_EPOCH, AUX_SC1);
    u32x2 xs[5];
#pragma unroll
    for (int i = 0; i < 5; ++i) xs[i] = __builtin_amdgcn_raw_buffer_load_b64(rx, (lane >> 5) * 1280 + (lane & 31) * 8, i * 256, 0);
    const int row0 = isq ? 160 * (j >> 1) : 80 * j;
    const int nrow = isq ? 160 : 80;
    unsigned short xr[5];
#pragma unroll
    for (int i = 0; i < 5; ++i) xr[i] = __builtin_amdgcn_raw_buffer_load_b16(rx, (lane >> 5) * 1280 + (lane & 31) * 2, (row0 + 32 * i) * 2, 0);
    u32x4 wa[20];
    {
      const __amdgpu_buffer_rsrc_t rw = isq ? RSRC(wq + h * 128 + (j & 1) * 64, 640 * 4096 * 2 - h * 256 - (j & 1) * 128) : RSRC(w1 + h * 64, ffn ? 640 * 1728 * 2 - h * 128 : 0);
      const int rstride = isq ? 8192 : 3456;
      const unsigned vo = (unsigned)((lane >> 3) * rstride + (lane & 7) * 16);
#pragma unroll
      for (int i = 0; i < 10; ++i) wa[i] = LD128(rw, vo, (row0 + 8 * i) * rstride, AUX_NT);
      if (isq) {
#pragma unroll
        for (int i = 10; i < 20; ++i) wa[i] = LD128(rw, vo, (row0 + 8 * i) * rstride, AUX_NT);
      }
    }
    if (isq && lane < 24) LDS_ST32(st + lane, 0u);
    wg_barrier();
    float xf[20];
#pragma unroll
    for (int i = 0; i < 5; ++i) { xf[4 * i] = bflo(xs[i][0]); xf[4 * i + 1] = bfhi(xs[i][0]); xf[4 * i + 2] = bflo(xs[i][1]); xf[4 * i + 3] = bfhi(xs[i][1]); }
    float sum = 0.f;
#pragma unroll
    for (int i = 0; i < 20; ++i) sum += xf[i];
    sum = x16_sum(row_sum(sum));
    const float mean = sum * (1.f / 640.f);
    float vs = 0.f;
#pragma unroll
    for (int i = 0; i < 20; ++i) { const float d = xf[i] - mean; vs = fmaf(d, d, vs); }
    vs = x16_sum(row_sum(vs));
    const float mb = rbf(mean), den = rbf(sqrtf(rbf(rbf(vs * (1.f / 640.f)) + 1e-5f)));
    unsigned* xw_ = xl + w * 320;
#pragma unroll
    for (int i = 0; i < 5; ++i) {
      const int row = (lane & 31) + 32 * i;
      const float xn = rbf(bflo(xr[i]) - mb) / den;
      if (row < nrow) xw_[(lane >> 5) * 160 + ((row >> 1) & 3) * 40 + (row >> 3) * 2 + (row & 1)] = pk_bf16(xn, xn);
    }
    const unsigned tag = __builtin_amdgcn_readfirstlane(epoch) + 0x9E3779B9u, tagB = tag ^ 0x80000000u;
    if (isq && lane == 0) LDS_ST32(flg, tag);
    LGKM0();
    f32x4 acc0 = {0.f, 0.f, 0.f, 0.f}, acc1 = {0.f, 0.f, 0.f, 0.f};
    const unsigned* xrd = xw_ + (c >> 3) * 160 + r * 40;
#pragma unroll
    for (int i = 0; i < 10; i += 2) {
      const u32x4 xa = *(const u32x4*)(xrd + 2 * i);
      acc0 = mfma(mask & xa[0], wa[i], acc0); acc1 = mfma(mask & xa[1], wa[i], acc1);
      acc0 = mfma(mask & xa[2], wa[i + 1], acc0); acc1 = mfma(mask & xa[3], wa[i + 1], acc1);
    }
    if (isq) {
#pragma unroll
      for (int i = 10; i < 20; i += 2) {
        const u32x4 xa = *(const u32x4*)(xrd + 2 * i);
        acc0 = mfma(mask & xa[0], wa[i], acc0); acc1 = mfma(mask & xa[1], wa[i], acc1);
        acc0 = mfma(mask & xa[2], wa[i + 1], acc0); acc1 = mfma(mask & xa[3], wa[i + 1], acc1);
      }
    }
    f32x4 v;
#pragma unroll
    for (int i = 0; i < 4; ++i) { const float t_ = c < 8 ? acc0[i] : acc1[i]; v[i] = t_ + dppf<ROR8>(t_); }
    const u32x4 pub = gran2(c < 8 ? v[0] : v[2], c < 8 ? v[1] : v[3], tag);
    if (isq) {
      ST128(pub, rws, cu * 1024 + lane * 16, WS_Q1, AUX_SC1);
      __builtin_amdgcn_s_sleep(PDELAY);
      const unsigned goff = (unsigned)((h * 8 + (lane >> 5)) * 1024 + (b * 32 + (lane & 31)) * 16);
      u32x4 G[8];
#pragma unroll
      for (int p = 0; p < 4; ++p) G[p] = LD128(rws, goff, WS_Q1 + p * 2048, AUX_SC1);
      for (unsigned sp = 0;; ++sp) {
        bool allok = true;
        CBAR();
#pragma unroll
        for (int p = 0; p < 4; ++p) if (!__all(gok(G[p], tag))) { allok = false; G[p] = LD128(rws, goff, WS_Q1 + p * 2048, AUX_SC1); }
        if (allok || sp > SPIN_CAP) break;
      }
      qs[(lane >> 5) * 32 + (c & 7) * 4 + (r & 1) * 2 + (c >> 3)] = pk_bf16((gv0(G[0]) + gv0(G[1])) + (gv0(G[2]) + gv0(G[3])), (gv1(G[0]) + gv1(G[1])) + (gv1(G[2]) + gv1(G[3])));
      ST128(gran2(0.f, 0.f, tag), rws, lane == 0 ? cu * 128 + 16 : OOB, WS_S, AUX_SC1);
      LGKM0();
      if (lane == 0) LDS_ST32(flg + 1, 1u);
      const unsigned soff = WS_S + (unsigned)((h * 8 + (lane & 7)) * 128 + (lane & 8) * 2);
      __builtin_amdgcn_s_sleep(ACKDELAY);
      u32x4 gs = LD128(rws, soff, 0, AUX_SC1);
      unsigned long long t_ = LDS_LD64(st + 2 * (lane & 7));
      for (unsigned sp = 0; !__all((unsigned)(t_ >> 32) != 0u) && sp < LSPIN_CAP; ++sp) { __builtin_amdgcn_s_sleep(2); t_ = LDS_LD64(st + 2 * (lane & 7)); }
      {
        const float m_ = __uint_as_float((unsigned)t_), l_ = __uint_as_float((unsigned)(t_ >> 32));
        const float M = oct_max(m_);
        float L = __builtin_amdgcn_exp2f((m_ - M) * LOG2E) * l_; L += dppf<XOR1>(L); L += dppf<XOR2>(L); L += dppf<XOR7>(L);
        ST128(gran2(M, L, tag), rws, lane == 0 ? cu * 128 : OOB, WS_S, AUX_SC1);
      }
      if (!__all(gok(gs, tag) | !(lane & 8))) {
        for (unsigned sp = 0; sp < SPIN_CAP; ++sp) {
          CBAR();
          gs = LD128(rws, soff, 0, AUX_SC1);
          if (__all(gok(gs, tag) | !(lane & 8))) break;
        }
      }
      for (unsigned sp = 0; sp < LSPIN_CAP && lds_flag(flg + 4) != 8u; ++sp) {}
      CBAR();
      const unsigned ooff = (unsigned)(h * 8 * 1024 + lane * 16);
      const f32x2 own = sum8((const float*)red + (((lane >> 1) & 1) * 16 + (lane >> 2)) * 4 + 2 * (lane & 1));
      const u32x4 ownG = gran2(own[0], own[1], tagB);
      ST128(ownG, rws, ooff, WS_O + j * 1024, AUX_SC1);
      gs = LD128(rws, soff, 0, AUX_SC1);
#pragma unroll
      for (int p = 0; p < 8; ++p) G[p] = LD128(rws, p == j ? OOB : ooff, WS_O + p * 1024, AUX_SC1);
#pragma unroll
      for (int p = 0; p < 8; ++p) if (p == j) G[p] = ownG;
      for (unsigned sp = 0; !__all(gok(gs, tag)) && sp < SPIN_CAP; ++sp) { CBAR(); __builtin_amdgcn_s_sleep(2); gs = LD128(rws, soff, 0, AUX_SC1); }
      float wp[8];
      {
        const float ms = gv0(gs), ls = gv1(gs);
        float Mg = fmaxf(ms, dppf<XOR1>(ms)); Mg = fmaxf(Mg, dppf<XOR2>(Mg));
        const float e_ = __builtin_amdgcn_exp2f((ms - Mg) * LOG2E);
        float Lg = e_ * ls; Lg += dppf<XOR1>(Lg); Lg += dppf<XOR2>(Lg);
        const float wgt = e_ / Lg;
#pragma unroll
        for (int p = 0; p < 8; ++p) wp[p] = __uint_as_float(__builtin_amdgcn_readlane(__float_as_uint(wgt), p));
      }
      for (unsigned sp = 0;; ++sp) {
        bool allok = true;
        CBAR();
#pragma unroll
        for (int p = 0; p < 8; ++p) if (!__all(gok(G[p], tagB))) { if (OSLEEP && allok) __builtin_amdgcn_s_sleep(OSLEEP); allok = false; G[p] = LD128(rws, ooff, WS_O + p * 1024, AUX_SC1); }
        if (allok || sp > SPIN_CAP) break;
      }
#pragma unroll
      for (int bb = 0; bb < 2; ++bb) {
        float o0 = 0.f, o1 = 0.f;
#pragma unroll
        for (int sp = 0; sp < 4; ++sp) { o0 = fmaf(wp[4 * bb + sp], gv0(G[4 * bb + sp]), o0); o1 = fmaf(wp[4 * bb + sp], gv1(G[4 * bb + sp]), o1); }
        os[bb * 64 + lane] = pk_bf16(o0, o1);
      }
      wg_barrier();
    } else {
      if (ffn) {
        ST128(pub, rws, (unsigned)((h * 8 + (c & 7)) * 1280 + j * 128 + (c >> 3) * 64 + r * 16), WS_H1, AUX_SC1);
        __builtin_amdgcn_s_sleep(2 * PDELAY);
        const unsigned goff = cu * 1280 + lane * 16;
        u32x4 G0 = LD128(rws, goff, WS_H1, AUX_SC1);
        for (unsigned sp = 0; !__all(gok(G0, tag)) && sp < SPIN_CAP; ++sp) { CBAR(); G0 = LD128(rws, goff, WS_H1, AUX_SC1); }
        float a0 = gv0(G0), a1 = gv1(G0);
        a0 += dppf<ROR8>(a0); a1 += dppf<ROR8>(a1);
        a0 = fmaxf(x32_sum(x16_sum(a0)), 0.f); a1 = fmaxf(x32_sum(x16_sum(a1)), 0.f);
        u32x2 g2; g2[0] = pk_bf16(a0, a1); g2[1] = tag;
        __builtin_amdgcn_raw_buffer_store_b64(g2, rws, lane < 8 ? cu * 128 + lane * 8 : OOB, WS_H2, AUX_SC1);
        const unsigned g2off = (unsigned)((h * 8 + (lane >> 3)) * 128 + (lane & 7) * 8);
        __builtin_amdgcn_s_sleep(3 * PDELAY);
        u32x2 q2 = __builtin_amdgcn_raw_buffer_load_b64(rws, g2off, WS_H2, AUX_SC1);
        for (unsigned sp = 0; !__all(q2[1] == tag) && sp < SPIN_CAP; ++sp) { CBAR(); q2 = __builtin_amdgcn_raw_buffer_load_b64(rws, g2off, WS_H2, AUX_SC1); }
        u32x2 hv; hv[0] = (q2[0] & 0xffffu) | (q2[0] << 16); hv[1] = (q2[0] >> 16) | (q2[0] & 0xffff0000u);
        *(u32x2*)(hs + ((lane >> 1) & 1) * 64 + (lane >> 3) * 8 + (lane & 1) * 4 + ((lane >> 2) & 1) * 2) = hv;
        LGKM0();
        if (lane == 0) LDS_ST32(flg + 2, 1u);
        for (unsigned sp = 0; sp < LSPIN_CAP && lds_flag(flg + 3) != 8u; ++sp) __builtin_amdgcn_s_sleep(1);
        CBAR();
        f_publish(0, tag);
        f_publish(8, tag);
      }
      const int e = lane & 7, ocol = j * 80 + 8 * h + e;
      const bool fin = h < 10, f3v = (lane >> 3) < 3;
      const unsigned yo = (unsigned)(((lane >> 3) * 8 + j) * 1280 + h * 128 + e * 16);
      unsigned short x0 = 0, x1 = 0;
      u32x4 g[4], y[4];
      if (fin) { x0 = __builtin_amdgcn_raw_buffer_load_b16(rx, ocol * 2, 0, 0); x1 = __builtin_amdgcn_raw_buffer_load_b16(rx, ocol * 2, 1280, 0); }
      wg_barrier();
      if (fin) {
#pragma unroll
        for (int i = 0; i < 4; ++i) g[i] = LD128(rws, (i < 3 || f3v) ? yo : OOB, WS_F + i * 64 * 1280, AUX_SC1);
        if (YDELAY) __builtin_amdgcn_s_sleep(YDELAY);
#pragma unroll
        for (int i = 0; i < 4; ++i) y[i] = LD128(rws, yo, WS_Y + i * 64 * 1280, AUX_SC1);
        for (unsigned sp = 0;; ++sp) {
          bool allok = true;
          CBAR();
#pragma unroll
          for (int i = 0; i < 4; ++i) if (!__all(gok(g[i], tag) | (i == 3 && !f3v))) { allok = false; g[i] = LD128(rws, (i < 3 || f3v) ? yo : OOB, WS_F + i * 64 * 1280, AUX_SC1); }
          if (allok || sp > SPIN_CAP) break;
        }
        float fa = (gv0(g[0]) + gv0(g[1])) + (gv0(g[2]) + gv0(g[3])), fb = (gv1(g[0]) + gv1(g[1])) + (gv1(g[2]) + gv1(g[3]));
        fa += dppf<ROR8>(fa); fb += dppf<ROR8>(fb);
        fa = rbf(x32_sum(x16_sum(fa))); fb = rbf(x32_sum(x16_sum(fb)));
        const float ga = rbf(fa / (1.f + __expf(-fa))), gb = rbf(fb / (1.f + __expf(-fb)));
        for (unsigned sp = 0;; ++sp) {
          bool allok = true;
          CBAR();
#pragma unroll
          for (int i = 0; i < 4; ++i) if (!__all(gok(y[i], tagB))) { if (YSLEEP && allok) __builtin_amdgcn_s_sleep(YSLEEP); allok = false; y[i] = LD128(rws, yo, WS_Y + i * 64 * 1280, AUX_SC1); }
          if (allok || sp > SPIN_CAP) break;
        }
        float ya = (gv0(y[0]) + gv0(y[1])) + (gv0(y[2]) + gv0(y[3])), yb = (gv1(y[0]) + gv1(y[1])) + (gv1(y[2]) + gv1(y[3]));
        ya += dppf<ROR8>(ya); yb += dppf<ROR8>(yb);
        ya = rbf(x32_sum(x16_sum(ya))); yb = rbf(x32_sum(x16_sum(yb)));
        const unsigned vv = pk_bf16(bflo(x0) + rbf(ya * ga), bflo(x1) + rbf(yb * gb)), nb = dppu<XOR1>(vv);
        const unsigned res = (e & 1) ? ((nb >> 16) | (vv & 0xffff0000u)) : ((vv & 0xffffu) | (nb << 16));
        __builtin_amdgcn_raw_buffer_store_b32(res, RSRC(out, 2560), lane < 8 ? (unsigned)(((e & 1) * 640 + (ocol & ~1)) * 2) : OOB, 0, 0);
        if (blk == 0 && lane == 0) __builtin_amdgcn_raw_buffer_store_b32(tag, rws, 0, WS_EPOCH, AUX_SC1);
      }
    }
  }
  if (w == REDW_ && h < 10) { asm volatile("s_waitcnt vmcnt(0)" ::: "memory"); unsigned t_ = (unsigned)__builtin_amdgcn_s_memrealtime(); unsigned d_ = (t_ - tstart_) * 10u; if (d_ > 60000u) d_ = 60000u; while ((unsigned)__builtin_amdgcn_s_memrealtime() - t_ < d_) __builtin_amdgcn_s_sleep(8); }
}

extern "C" void kernel_launch(void* const* d_in, const int* in_sizes, int n_in, void* d_out, int out_size, void* d_ws, size_t ws_size, hipStream_t stream) {
  fused_decode<<<256, 640, 0, stream>>>((const unsigned short*)d_in[0], (const unsigned short*)d_in[1], (const unsigned short*)d_in[2],
      (const unsigned short*)d_in[3], (const unsigned short*)d_in[4], (const unsigned short*)d_in[6],
      (unsigned char*)d_ws, (const unsigned short*)d_in[7], (unsigned short*)d_out);
}
```

```cpp
#include <hip/hip_runtime.h>
#include <hip/hip_bf16.h>
#include <stdint.h>

typedef unsigned u32x4 __attribute__((ext_vector_type(4)));
typedef unsigned u32x2 __attribute__((ext_vector_type(2)));
typedef float f32x4 __attribute__((ext_vector_type(4)));
typedef float f32x2 __attribute__((ext_vector_type(2)));
typedef __bf16 bf16x8 __attribute__((ext_vector_type(8)));
typedef __bf16 bf16x2 __attribute__((ext_vector_type(2)));
typedef short s16x4 __attribute__((ext_vector_type(4)));

constexpr int PDELAY = 32;
constexpr int ACKDELAY = 48;
constexpr int YDELAY = 4;
constexpr int ROTM = 37;
constexpr int AUX_NT = 2, AUX_SC1 = 16;
constexpr unsigned OOB = 0x7ffffff0u;
constexpr unsigned WS_EPOCH = 0;
constexpr unsigned WS_Q1 = 4096;
constexpr unsigned WS_H1 = WS_Q1 + 256 * 1024;
constexpr unsigned WS_H2 = WS_H1 + 256 * 1280;
constexpr unsigned WS_S = WS_H2 + 256 * 128;
constexpr unsigned WS_F = WS_S + 256 * 128;
constexpr unsigned WS_END = WS_F + 256 * 1280;
constexpr unsigned WS_O = WS_Q1, WS_Y = WS_H1;
constexpr unsigned SPIN_CAP = 1u << 16, LSPIN_CAP = 1u << 22;
constexpr int CODE_BYTES = 13312;
constexpr int L_RED = 0, L_WL = 16384, L_QS = L_WL + 20480, L_HS = L_QS + 256, L_ST = L_HS + 512, L_FLG = L_ST + 64, L_XL = L_FLG + 64, L_OS = L_XL + 2560, L_END = L_OS + 512;

#define RSRC(p, n) __builtin_amdgcn_make_buffer_rsrc((void*)(p), 0, (n), 0x00020000)
#define LD128(rs, vo, so, aux) __builtin_amdgcn_raw_buffer_load_b128((rs), (vo), (so), (aux))
#define ST128(v, rs, vo, so, aux) __builtin_amdgcn_raw_buffer_store_b128((v), (rs), (vo), (so), (aux))
#define SBAR() __builtin_amdgcn_sched_barrier(0)
#define CBAR() asm volatile("" ::: "memory")
#define LGKM0() asm volatile("s_waitcnt lgkmcnt(0)" ::: "memory")
#define LAS __attribute__((address_space(3)))
#define LDS_LD32(p) __hip_atomic_load((LAS unsigned*)(p), __ATOMIC_RELAXED, __HIP_MEMORY_SCOPE_WORKGROUP)
#define LDS_ST32(p, v) __hip_atomic_store((LAS unsigned*)(p), (v), __ATOMIC_RELAXED, __HIP_MEMORY_SCOPE_WORKGROUP)
#define LDS_LD64(p) __hip_atomic_load((LAS unsigned long long*)(p), __ATOMIC_RELAXED, __HIP_MEMORY_SCOPE_WORKGROUP)
#define LDS_ST64(p, v) __hip_atomic_store((LAS unsigned long long*)(p), (v), __ATOMIC_RELAXED, __HIP_MEMORY_SCOPE_WORKGROUP)
#define LDS_INC(p) asm volatile("ds_add_u32 %0, %1" :: "v"((unsigned)(size_t)(LAS unsigned*)(p)), "v"(1u) : "memory")

template <int CTRL> __device__ __forceinline__ unsigned dppu(unsigned x) { return (unsigned)__builtin_amdgcn_mov_dpp((int)x, CTRL, 0xf, 0xf, true); }
template <int CTRL> __device__ __forceinline__ float dppf(float x) { return __uint_as_float(dppu<CTRL>(__float_as_uint(x))); }
constexpr int XOR1 = 0xB1, XOR2 = 0x4E, XOR7 = 0x141, ROR8 = 0x128, ROR4 = 0x124, SHL4 = 0x104, SHR4 = 0x114, BCAST = 0x150;

__device__ __forceinline__ float dot2(unsigned k, unsigned q, float acc) {
  return __builtin_amdgcn_fdot2_f32_bf16(__builtin_bit_cast(bf16x2, k), __builtin_bit_cast(bf16x2, q), acc, false);
}
__device__ __forceinline__ unsigned pk_bf16(float lo, float hi) { bf16x2 v; v[0] = (__bf16)lo; v[1] = (__bf16)hi; return __builtin_bit_cast(unsigned, v); }
__device__ __forceinline__ float rbf(float f) { return __uint_as_float(pk_bf16(f, f) & 0xffff0000u); }
__device__ __forceinline__ float bflo(unsigned u) { return __uint_as_float(u << 16); }
__device__ __forceinline__ float bfhi(unsigned u) { return __uint_as_float(u & 0xffff0000u); }
__device__ __forceinline__ f32x4 mfma(u32x4 a, u32x4 b, f32x4 c) {
  return __builtin_amdgcn_mfma_f32_16x16x32_bf16(__builtin_bit_cast(bf16x8, a), __builtin_bit_cast(bf16x8, b), c, 0, 0, 0);
}
__device__ __forceinline__ float x16_sum(float x) { auto s = __builtin_amdgcn_permlane16_swap(__float_as_uint(x), __float_as_uint(x), false, false); return __uint_as_float(s[0]) + __uint_as_float(s[1]); }
__device__ __forceinline__ float x32_sum(float x) { auto s = __builtin_amdgcn_permlane32_swap(__float_as_uint(x), __float_as_uint(x), false, false); return __uint_as_float(s[0]) + __uint_as_float(s[1]); }
__device__ __forceinline__ float x16_max(float x) { auto s = __builtin_amdgcn_permlane16_swap(__float_as_uint(x), __float_as_uint(x), false, false); return fmaxf(__uint_as_float(s[0]), __uint_as_float(s[1])); }
__device__ __forceinline__ float x32_max(float x) { auto s = __builtin_amdgcn_permlane32_swap(__float_as_uint(x), __float_as_uint(x), false, false); return fmaxf(__uint_as_float(s[0]), __uint_as_float(s[1])); }
__device__ __forceinline__ float row_sum(float x) { x += dppf<XOR1>(x); x += dppf<XOR2>(x); x += dppf<ROR4>(x); x += dppf<ROR8>(x); return x; }
__device__ __forceinline__ float row_max(float x) { x = fmaxf(x, dppf<XOR1>(x)); x = fmaxf(x, dppf<XOR2>(x)); x = fmaxf(x, dppf<ROR4>(x)); x = fmaxf(x, dppf<ROR8>(x)); return x; }
__device__ __forceinline__ float oct_max(float x) { x = fmaxf(x, dppf<XOR1>(x)); x = fmaxf(x, dppf<XOR2>(x)); return fmaxf(x, dppf<XOR7>(x)); }
template <int C0, int C1> __device__ __forceinline__ float trsel(float lo, float hi, bool bit) {
  const float t0 = lo + dppf<C0>(lo), t1 = hi + dppf<C1>(hi);
  return bit ? t1 : t0;
}
__device__ __forceinline__ bool gok(u32x4 g, unsigned tag) { return (g[1] == tag) & (g[3] == tag); }
__device__ __forceinline__ u32x4 gran2(float a, float b, unsigned tag) { u32x4 g; g[0] = __float_as_uint(a); g[1] = tag; g[2] = __float_as_uint(b); g[3] = tag; return g; }
__device__ __forceinline__ float gv0(u32x4 g) { return __uint_as_float(g[0]); }
__device__ __forceinline__ float gv1(u32x4 g) { return __uint_as_float(g[2]); }
__device__ __forceinline__ void wg_barrier() { asm volatile("s_waitcnt lgkmcnt(0)\n\ts_barrier" ::: "memory"); }
__device__ __forceinline__ unsigned lds_flag(unsigned* p) { return __builtin_amdgcn_readfirstlane(LDS_LD32(p)); }
__device__ __forceinline__ f32x2 sum8(const float* p) {
  f32x2 a[8];
#pragma unroll
  for (int i = 0; i < 8; ++i) a[i] = *(const f32x2*)(p + i * 256);
  return ((a[0] + a[1]) + (a[2] + a[3])) + ((a[4] + a[5]) + (a[6] + a[7]));
}
__device__ __forceinline__ u32x2 trread(unsigned char* p) { return __builtin_bit_cast(u32x2, __builtin_amdgcn_ds_read_tr16_b64_v4i16((LAS s16x4*)p)); }
constexpr float LOG2E = 1.4426950408889634f;

__global__ void __launch_bounds__(640, 3)
fused_decode(const unsigned short* x, const unsigned short* kc, const unsigned short* vc,
             const unsigned short* wq, const unsigned short* wo, const unsigned short* w1,
             unsigned char* ws, const unsigned short* w2, unsigned short* out) {
  unsigned long long pc_entry;
  asm volatile("s_getpc_b64 %0" : "=s"(pc_entry));
  asm volatile("" : "+s"(x), "+s"(kc), "+s"(vc), "+s"(wq), "+s"(wo), "+s"(w1), "+s"(ws), "+s"(w2), "+s"(out));
  __shared__ __attribute__((aligned(16))) unsigned char smem[L_END];
  f32x4* red = (f32x4*)(smem + L_RED);
  unsigned* qs = (unsigned*)(smem + L_QS);
  unsigned* hs = (unsigned*)(smem + L_HS);
  float* st = (float*)(smem + L_ST);
  unsigned* flg = (unsigned*)(smem + L_FLG);
  unsigned* xl = (unsigned*)(smem + L_XL);
  unsigned* os = (unsigned*)(smem + L_OS);
  const int blk = blockIdx.x, tid = threadIdx.x, lane = tid & 63, w = __builtin_amdgcn_readfirstlane(tid >> 6);
  const int j = (blk >> 3) & 7, h = (blk & 7) + 8 * (blk >> 6), b = j >> 2, s = j & 3;
  const int r = lane >> 4, c = lane & 15;
  const bool ffn = h < 27;
  const unsigned cu = (unsigned)(h * 8 + j);
  const __amdgpu_buffer_rsrc_t rws = RSRC(ws, WS_END);
  const __amdgpu_buffer_rsrc_t rx = RSRC(x, 2560);
  u32x4 mask;
#pragma unroll
  for (int i = 0; i < 4; ++i) mask[i] = (i == ((c & 7) >> 1)) ? ((c & 1) ? 0xffff0000u : 0xffffu) : 0u;
  auto f_publish = [&](int cbase, unsigned tg) {
    const int cr = cbase + (lane >> 3), e = lane & 7;
    const float* p = (const float*)(red + 512) + ((e >> 2) * 16 + (cr & 15)) * 4 + (e & 3);
    float a0[8], a1[8];
#pragma unroll
    for (int i = 0; i < 8; ++i) { a0[i] = p[i * 256]; a1[i] = p[i * 256 + 128]; }
    const float v0 = ((a0[0] + a0[1]) + (a0[2] + a0[3])) + ((a0[4] + a0[5]) + (a0[6] + a0[7]));
    const float v1 = ((a1[0] + a1[1]) + (a1[2] + a1[3])) + ((a1[4] + a1[5]) + (a1[6] + a1[7]));
    ST128(gran2(v0, v1, tg), rws, cr < 10 ? cu * 1280 + (unsigned)(cr * 128 + e * 16) : OOB, WS_F, AUX_SC1);
  };

  if (w >= 2) {
    const int sw = w - 2;
    const size_t kvbase = ((size_t)(b * 32 + h) * 8192 + 6144 + s * 512) * 128;
    const __amdgpu_buffer_rsrc_t rk = RSRC(kc + kvbase, 131072), rv = RSRC(vc + kvbase, 131072);
    const int rot = sw * 4 + ((blk * ROTM) & 127);
    const int voff = lane * 16;
#define KVOFF(t) ((((((t) >> 2) * 32 + ((t) & 3) + rot) & 127) << 10))
    u32x4 K[16], V[16], WO[4], W2[2];
    float sc = 0.f;
    auto scores = [&]() {
      CBAR();
      const u32x4 q4 = *(const u32x4*)(qs + 4 * c);
      float sb[4];
#pragma unroll
      for (int be = 0; be < 4; ++be) {
        float d[4];
#pragma unroll
        for (int tt = 0; tt < 4; ++tt) {
          const u32x4 kk = K[4 * be + tt];
          d[tt] = dot2(kk[3], q4[3], dot2(kk[2], q4[2], dot2(kk[1], q4[1], dot2(kk[0], q4[0], 0.f))));
        }
        const float e0 = trsel<XOR1, XOR1>(d[0], d[1], c & 1);
        const float e1 = trsel<XOR1, XOR1>(d[2], d[3], c & 1);
        sb[be] = trsel<XOR2, XOR2>(e0, e1, c & 2);
      }
      const float f0 = trsel<SHL4, SHR4>(sb[0], sb[1], c & 4);
      const float f1 = trsel<SHL4, SHR4>(sb[2], sb[3], c & 4);
      const float dotv = trsel<ROR8, ROR8>(f0, f1, c & 8);
      sc = rbf(rbf(dotv) * 0.08838834764831845f);
      const float mw = x32_max(x16_max(row_max(sc)));
      const float lw = x32_sum(x16_sum(row_sum(__builtin_amdgcn_exp2f((sc - mw) * LOG2E))));
      if (lane == 0) LDS_ST64(st + 2 * sw, (unsigned long long)__float_as_uint(mw) | ((unsigned long long)__float_as_uint(lw) << 32));
    };
    unsigned cp0 = 0, cp1 = 0;
    if (sw == 7 && (blk >> 3) < 4) {
      const __amdgpu_buffer_rsrc_t rc = RSRC((void*)(pc_entry & ~255ull), CODE_BYTES);
      cp0 = __builtin_amdgcn_raw_buffer_load_b32(rc, lane * 128, 0, 0);
      cp1 = __builtin_amdgcn_raw_buffer_load_b32(rc, lane * 128, 8192, 0);
    }
    __builtin_amdgcn_s_setprio(3);
    wg_barrier();
#pragma unroll
    for (int i = 0; i < 24; ++i) {
      if (i == 16) __builtin_amdgcn_s_setprio(2);
      if (i < 16) K[i] = LD128(rk, voff, KVOFF(i), AUX_NT); else V[i - 16] = LD128(rv, voff, KVOFF(i - 16), AUX_NT);
      SBAR();
    }
    const bool early = lds_flag(flg + 1) != 0u;
    if (early) scores();
    SBAR();
    __builtin_amdgcn_s_setprio(1);
#pragma unroll
    for (int t = 8; t < 16; ++t) { V[t] = LD128(rv, voff, KVOFF(t), AUX_NT); SBAR(); }
    if (!early) {
      for (unsigned sp = 0; sp < LSPIN_CAP && lds_flag(flg + 1) == 0u; ++sp) __builtin_amdgcn_s_sleep(1);
      scores();
    }
    SBAR();
    __builtin_amdgcn_s_setprio(0);
    {
      const __amdgpu_buffer_rsrc_t ro = RSRC(wo + (size_t)h * 128 * 640 + j * 80, 128 * 1280 - j * 160);
      const __amdgpu_buffer_rsrc_t r2 = RSRC(w2 + (size_t)h * 64 * 640 + j * 80, ffn ? 64 * 1280 - j * 160 : 0);
      const unsigned vo4 = c < 10 ? (unsigned)(r * 4 * 1280 + c * 16) : OOB;
      const unsigned vo2 = c < 10 ? (unsigned)(r * 2 * 1280 + c * 16) : OOB;
#pragma unroll
      for (int i = 0; i < 4; ++i) { WO[i] = LD128(ro, vo4, (16 * sw + i) * 1280, AUX_NT); SBAR(); }
#pragma unroll
      for (int i = 0; i < 2; ++i) { W2[i] = LD128(r2, vo2, (8 * sw + i) * 1280, AUX_NT); SBAR(); }
    }
    asm volatile("" :: "v"(cp0), "v"(cp1));
    unsigned pp;
    {
      unsigned long long t_ = LDS_LD64(st + 2 * (lane & 7));
      for (unsigned sp = 0; !__all((unsigned)(t_ >> 32) != 0u) && sp < LSPIN_CAP; ++sp) { __builtin_amdgcn_s_sleep(1); t_ = LDS_LD64(st + 2 * (lane & 7)); }
      const float M = oct_max(__uint_as_float((unsigned)t_));
      const float pv = __builtin_amdgcn_exp2f((sc - M) * LOG2E);
      pp = pk_bf16(pv, pv);
    }
    const unsigned tagB = lds_flag(flg) ^ 0x80000000u;
    f32x4 oacc = {0.f, 0.f, 0.f, 0.f};
#define PVSTEP(t) oacc = mfma(mask & dppu<BCAST + (t)>(pp), V[t], oacc);
    PVSTEP(0) PVSTEP(1) PVSTEP(2) PVSTEP(3) PVSTEP(4) PVSTEP(5) PVSTEP(6) PVSTEP(7)
    PVSTEP(8) PVSTEP(9) PVSTEP(10) PVSTEP(11) PVSTEP(12) PVSTEP(13) PVSTEP(14) PVSTEP(15)
#undef PVSTEP
    red[sw * 64 + lane] = oacc;
    LGKM0();
    if (lane == 0) LDS_INC(flg + 4);
    if (ffn) {
      for (unsigned sp = 0; sp < LSPIN_CAP && lds_flag(flg + 2) == 0u; ++sp) __builtin_amdgcn_s_sleep(1);
      CBAR();
      const u32x2 hv = *(const u32x2*)(hs + (c >> 3) * 64 + 8 * sw + 2 * r);
      f32x4 facc = {0.f, 0.f, 0.f, 0.f};
      facc = mfma(mask & hv[0], W2[0], facc);
      facc = mfma(mask & hv[1], W2[1], facc);
      red[(8 + sw) * 64 + lane] = facc;
      LGKM0();
      if (lane == 0) LDS_INC(flg + 3);
    }
    if (c < 10) {
#pragma unroll
      for (int i = 0; i < 4; ++i) *(u32x4*)(smem + L_WL + (16 * sw + 4 * r + i) * 160 + c * 16) = WO[i];
    }
    const int tro = L_WL + (8 * r + ((lane >> 2) & 3)) * 160 + sw * 32 + (lane & 3) * 8;
    const unsigned yoff = lane < 16 ? cu * 1280 + (unsigned)(sw * 256 + lane * 16) : OOB;
    wg_barrier();
    if (sw < 5) {
      u32x4 A[4], B[4];
#pragma unroll
      for (int t = 0; t < 4; ++t) {
        A[t] = *(const u32x4*)(os + (c & 1) * 64 + t * 16 + r * 4);
        const u32x2 lo = trread(smem + tro + t * 5120), hi = trread(smem + tro + t * 5120 + 640);
        B[t] = u32x4{lo[0], lo[1], hi[0], hi[1]};
      }
      f32x4 y = {0.f, 0.f, 0.f, 0.f}, y2 = {0.f, 0.f, 0.f, 0.f};
      y = mfma(A[0], B[0], y); y2 = mfma(A[1], B[1], y2);
      y = mfma(A[2], B[2], y); y2 = mfma(A[3], B[3], y2);
      ST128(gran2(y[0] + y2[0], y[1] + y2[1], tagB), rws, yoff, WS_Y, AUX_SC1);
    }
  } else {
    __builtin_amdgcn_s_setprio(3);
    const bool isq = (w == 0);
    const unsigned epoch = __builtin_amdgcn_raw_buffer_load_b32(rws, 0, WS_EPOCH, AUX_SC1);
    u32x2 xs[5];
#pragma unroll
    for (int i = 0; i < 5; ++i) xs[i] = __builtin_amdgcn_raw_buffer_load_b64(rx, (lane >> 5) * 1280 + (lane & 31) * 8, i * 256, 0);
    const int row0 = isq ? 160 * (j >> 1) : 80 * j;
    const int nrow = isq ? 160 : 80;
    unsigned short xr[5];
#pragma unroll
    for (int i = 0; i < 5; ++i) xr[i] = __builtin_amdgcn_raw_buffer_load_b16(rx, (lane & 31) + 32 * i < nrow ? (unsigned)((lane >> 5) * 1280 + (lane & 31) * 2) : OOB, (row0 + 32 * i) * 2, 0);
    u32x4 wa[20];
    {
      const __amdgpu_buffer_rsrc_t rw = isq ? RSRC(wq + h * 128 + (j & 1) * 64, 640 * 4096 * 2 - h * 256 - (j & 1) * 128) : RSRC(w1 + h * 64, ffn ? 640 * 1728 * 2 - h * 128 : 0);
      const int rstride = isq ? 8192 : 3456;
      const unsigned vo = (unsigned)((lane >> 3) * rstride + (lane & 7) * 16);
#pragma unroll
      for (int i = 0; i < 10; ++i) wa[i] = LD128(rw, vo, (row0 + 8 * i) * rstride, AUX_NT);
      if (isq) {
#pragma unroll
        for (int i = 10; i < 20; ++i) wa[i] = LD128(rw, vo, (row0 + 8 * i) * rstride, AUX_NT);
      }
    }
    if (isq && lane < 24) LDS_ST32(st + lane, 0u);
    wg_barrier();
    float xf[20];
#pragma unroll
    for (int i = 0; i < 5; ++i) { xf[4 * i] = bflo(xs[i][0]); xf[4 * i + 1] = bfhi(xs[i][0]); xf[4 * i + 2] = bflo(xs[i][1]); xf[4 * i + 3] = bfhi(xs[i][1]); }
    float sum = 0.f;
#pragma unroll
    for (int i = 0; i < 20; ++i) sum += xf[i];
    sum = x16_sum(row_sum(sum));
    const float mean = sum * (1.f / 640.f);
    float vs = 0.f;
#pragma unroll
    for (int i = 0; i < 20; ++i) { const float d = xf[i] - mean; vs = fmaf(d, d, vs); }
    vs = x16_sum(row_sum(vs));
    const float mb = rbf(mean), den = rbf(sqrtf(rbf(rbf(vs * (1.f / 640.f)) + 1e-5f)));
    unsigned* xw_ = xl + w * 320;
#pragma unroll
    for (int i = 0; i < 5; ++i) {
      const int row = (lane & 31) + 32 * i;
      const float xn = rbf(bflo(xr[i]) - mb) / den;
      if (row < nrow) xw_[(lane >> 5) * 160 + ((row >> 1) & 3) * 40 + (row >> 3) * 2 + (row & 1)] = pk_bf16(xn, xn);
    }
    const unsigned tag = __builtin_amdgcn_readfirstlane(epoch) + 0x9E3779B9u, tagB = tag ^ 0x80000000u;
    if (isq && lane == 0) LDS_ST32(flg, tag);
    LGKM0();
    f32x4 acc0 = {0.f, 0.f, 0.f, 0.f}, acc1 = {0.f, 0.f, 0.f, 0.f};
    const unsigned* xrd = xw_ + (c >> 3) * 160 + r * 40;
#pragma unroll
    for (int i = 0; i < 10; i += 2) {
      const u32x4 xa = *(const u32x4*)(xrd + 2 * i);
      acc0 = mfma(mask & xa[0], wa[i], acc0); acc1 = mfma(mask & xa[1], wa[i], acc1);
      acc0 = mfma(mask & xa[2], wa[i + 1], acc0); acc1 = mfma(mask & xa[3], wa[i + 1], acc1);
    }
    if (isq) {
#pragma unroll
      for (int i = 10; i < 20; i += 2) {
        const u32x4 xa = *(const u32x4*)(xrd + 2 * i);
        acc0 = mfma(mask & xa[0], wa[i], acc0); acc1 = mfma(mask & xa[1], wa[i], acc1);
        acc0 = mfma(mask & xa[2], wa[i + 1], acc0); acc1 = mfma(mask & xa[3], wa[i + 1], acc1);
      }
    }
    f32x4 v;
#pragma unroll
    for (int i = 0; i < 4; ++i) { const float t_ = c < 8 ? acc0[i] : acc1[i]; v[i] = t_ + dppf<ROR8>(t_); }
    const u32x4 pub = gran2(c < 8 ? v[0] : v[2], c < 8 ? v[1] : v[3], tag);
    if (isq) {
      ST128(pub, rws, cu * 1024 + lane * 16, WS_Q1, AUX_SC1);
      __builtin_amdgcn_s_sleep(PDELAY);
      const unsigned goff = (unsigned)((h * 8 + (lane >> 5)) * 1024 + (b * 32 + (lane & 31)) * 16);
      u32x4 G[8];
#pragma unroll
      for (int p = 0; p < 4; ++p) G[p] = LD128(rws, goff, WS_Q1 + p * 2048, AUX_SC1);
      for (unsigned sp = 0;; ++sp) {
        bool allok = true;
        CBAR();
#pragma unroll
        for (int p = 0; p < 4; ++p) if (!__all(gok(G[p], tag))) { allok = false; G[p] = LD128(rws, goff, WS_Q1 + p * 2048, AUX_SC1); }
        if (allok || sp > SPIN_CAP) break;
      }
      qs[(lane >> 5) * 32 + (c & 7) * 4 + (r & 1) * 2 + (c >> 3)] = pk_bf16((gv0(G[0]) + gv0(G[1])) + (gv0(G[2]) + gv0(G[3])), (gv1(G[0]) + gv1(G[1])) + (gv1(G[2]) + gv1(G[3])));
      ST128(gran2(0.f, 0.f, tag), rws, lane == 0 ? cu * 128 + 16 : OOB, WS_S, AUX_SC1);
      LGKM0();
      if (lane == 0) LDS_ST32(flg + 1, 1u);
      const unsigned soff = WS_S + (unsigned)((h * 8 + (lane & 7)) * 128 + (lane & 8) * 2);
      __builtin_amdgcn_s_sleep(ACKDELAY);
      u32x4 gs = LD128(rws, soff, 0, AUX_SC1);
      unsigned long long t_ = LDS_LD64(st + 2 * (lane & 7));
      for (unsigned sp = 0; !__all((unsigned)(t_ >> 32) != 0u) && sp < LSPIN_CAP; ++sp) { __builtin_amdgcn_s_sleep(2); t_ = LDS_LD64(st + 2 * (lane & 7)); }
      {
        const float m_ = __uint_as_float((unsigned)t_), l_ = __uint_as_float((unsigned)(t_ >> 32));
        const float M = oct_max(m_);
        float L = __builtin_amdgcn_exp2f((m_ - M) * LOG2E) * l_; L += dppf<XOR1>(L); L += dppf<XOR2>(L); L += dppf<XOR7>(L);
        ST128(gran2(M, L, tag), rws, lane == 0 ? cu * 128 : OOB, WS_S, AUX_SC1);
      }
      if (!__all(gok(gs, tag) | !(lane & 8))) {
        for (unsigned sp = 0; sp < SPIN_CAP; ++sp) {
          CBAR();
          gs = LD128(rws, soff, 0, AUX_SC1);
          if (__all(gok(gs, tag) | !(lane & 8))) break;
        }
      }
      for (unsigned sp = 0; sp < LSPIN_CAP && lds_flag(flg + 4) != 8u; ++sp) {}
      CBAR();
      const unsigned ooff = (unsigned)(h * 8 * 1024 + lane * 16);
      const f32x2 own = sum8((const float*)red + (((lane >> 1) & 1) * 16 + (lane >> 2)) * 4 + 2 * (lane & 1));
      const u32x4 ownG = gran2(own[0], own[1], tagB);
      ST128(ownG, rws, ooff, WS_O + j * 1024, AUX_SC1);
      gs = LD128(rws, soff, 0, AUX_SC1);
#pragma unroll
      for (int p = 0; p < 8; ++p) G[p] = LD128(rws, p == j ? OOB : ooff, WS_O + p * 1024, AUX_SC1);
#pragma unroll
      for (int p = 0; p < 8; ++p) if (p == j) G[p] = ownG;
      for (unsigned sp = 0; !__all(gok(gs, tag)) && sp < SPIN_CAP; ++sp) { CBAR(); __builtin_amdgcn_s_sleep(2); gs = LD128(rws, soff, 0, AUX_SC1); }
      float wp[8];
      {
        const float ms = gv0(gs), ls = gv1(gs);
        float Mg = fmaxf(ms, dppf<XOR1>(ms)); Mg = fmaxf(Mg, dppf<XOR2>(Mg));
        const float e_ = __builtin_amdgcn_exp2f((ms - Mg) * LOG2E);
        float Lg = e_ * ls; Lg += dppf<XOR1>(Lg); Lg += dppf<XOR2>(Lg);
        const float wgt = e_ / Lg;
#pragma unroll
        for (int p = 0; p < 8; ++p) wp[p] = __uint_as_float(__builtin_amdgcn_readlane(__float_as_uint(wgt), p));
      }
      for (unsigned sp = 0;; ++sp) {
        bool allok = true;
        CBAR();
#pragma unroll
        for (int p = 0; p < 8; ++p) if (!__all(gok(G[p], tagB))) { allok = false; G[p] = LD128(rws, ooff, WS_O + p * 1024, AUX_SC1); }
        if (allok || sp > SPIN_CAP) break;
      }
#pragma unroll
      for (int bb = 0; bb < 2; ++bb) {
        float o0 = 0.f, o1 = 0.f;
#pragma unroll
        for (int sp = 0; sp < 4; ++sp) { o0 = fmaf(wp[4 * bb + sp], gv0(G[4 * bb + sp]), o0); o1 = fmaf(wp[4 * bb + sp], gv1(G[4 * bb + sp]), o1); }
        os[bb * 64 + lane] = pk_bf16(o0, o1);
      }
      wg_barrier();
    } else {
      if (ffn) {
        ST128(pub, rws, (unsigned)((h * 8 + (c & 7)) * 1280 + j * 128 + (c >> 3) * 64 + r * 16), WS_H1, AUX_SC1);
        __builtin_amdgcn_s_sleep(2 * PDELAY);
        const unsigned goff = cu * 1280 + lane * 16;
        u32x4 G0 = LD128(rws, goff, WS_H1, AUX_SC1);
        for (unsigned sp = 0; !__all(gok(G0, tag)) && sp < SPIN_CAP; ++sp) { CBAR(); G0 = LD128(rws, goff, WS_H1, AUX_SC1); }
        float a0 = gv0(G0), a1 = gv1(G0);
        a0 += dppf<ROR8>(a0); a1 += dppf<ROR8>(a1);
        a0 = fmaxf(x32_sum(x16_sum(a0)), 0.f); a1 = fmaxf(x32_sum(x16_sum(a1)), 0.f);
        u32x2 g2; g2[0] = pk_bf16(a0, a1); g2[1] = tag;
        __builtin_amdgcn_raw_buffer_store_b64(g2, rws, lane < 8 ? cu * 128 + lane * 8 : OOB, WS_H2, AUX_SC1);
        const unsigned g2off = (unsigned)((h * 8 + (lane >> 3)) * 128 + (lane & 7) * 8);
        __builtin_amdgcn_s_sleep(3 * PDELAY);
        u32x2 q2 = __builtin_amdgcn_raw_buffer_load_b64(rws, g2off, WS_H2, AUX_SC1);
        for (unsigned sp = 0; !__all(q2[1] == tag) && sp < SPIN_CAP; ++sp) { CBAR(); q2 = __builtin_amdgcn_raw_buffer_load_b64(rws, g2off, WS_H2, AUX_SC1); }
        u32x2 hv; hv[0] = (q2[0] & 0xffffu) | (q2[0] << 16); hv[1] = (q2[0] >> 16) | (q2[0] & 0xffff0000u);
        *(u32x2*)(hs + ((lane >> 1) & 1) * 64 + (lane >> 3) * 8 + (lane & 1) * 4 + ((lane >> 2) & 1) * 2) = hv;
        LGKM0();
        if (lane == 0) LDS_ST32(flg + 2, 1u);
        for (unsigned sp = 0; sp < LSPIN_CAP && lds_flag(flg + 3) != 8u; ++sp) __builtin_amdgcn_s_sleep(1);
        CBAR();
        f_publish(0, tag);
        f_publish(8, tag);
      }
      const int e = lane & 7, ocol = j * 80 + 8 * h + e;
      const bool fin = h < 10, f3v = (lane >> 3) < 3;
      const unsigned yo = (unsigned)(((lane >> 3) * 8 + j) * 1280 + h * 128 + e * 16);
      unsigned short x0 = 0, x1 = 0;
      u32x4 g[4], y[4];
      if (fin) { x0 = __builtin_amdgcn_raw_buffer_load_b16(rx, ocol * 2, 0, 0); x1 = __builtin_amdgcn_raw_buffer_load_b16(rx, ocol * 2, 1280, 0); }
      wg_barrier();
      if (fin) {
#pragma unroll
        for (int i = 0; i < 4; ++i) g[i] = LD128(rws, (i < 3 || f3v) ? yo : OOB, WS_F + i * 64 * 1280, AUX_SC1);
        __builtin_amdgcn_s_sleep(YDELAY);
#pragma unroll
        for (int i = 0; i < 4; ++i) y[i] = LD128(rws, yo, WS_Y + i * 64 * 1280, AUX_SC1);
        for (unsigned sp = 0;; ++sp) {
          bool allok = true;
          CBAR();
#pragma unroll
          for (int i = 0; i < 4; ++i) if (!__all(gok(g[i], tag) | (i == 3 && !f3v))) { allok = false; g[i] = LD128(rws, (i < 3 || f3v) ? yo : OOB, WS_F + i * 64 * 1280, AUX_SC1); }
          if (allok || sp > SPIN_CAP) break;
        }
        float fa = (gv0(g[0]) + gv0(g[1])) + (gv0(g[2]) + gv0(g[3])), fb = (gv1(g[0]) + gv1(g[1])) + (gv1(g[2]) + gv1(g[3]));
        fa += dppf<ROR8>(fa); fb += dppf<ROR8>(fb);
        fa = rbf(x32_sum(x16_sum(fa))); fb = rbf(x32_sum(x16_sum(fb)));
        const float ga = rbf(fa / (1.f + __expf(-fa))), gb = rbf(fb / (1.f + __expf(-fb)));
        for (unsigned sp = 0;; ++sp) {
          bool allok = true;
          CBAR();
#pragma unroll
          for (int i = 0; i < 4; ++i) if (!__all(gok(y[i], tagB))) { allok = false; y[i] = LD128(rws, yo, WS_Y + i * 64 * 1280, AUX_SC1); }
          if (allok || sp > SPIN_CAP) break;
        }
        float ya = (gv0(y[0]) + gv0(y[1])) + (gv0(y[2]) + gv0(y[3])), yb = (gv1(y[0]) + gv1(y[1])) + (gv1(y[2]) + gv1(y[3]));
        ya += dppf<ROR8>(ya); yb += dppf<ROR8>(yb);
        ya = rbf(x32_sum(x16_sum(ya))); yb = rbf(x32_sum(x16_sum(yb)));
        const unsigned vv = pk_bf16(bflo(x0) + rbf(ya * ga), bflo(x1) + rbf(yb * gb)), nb = dppu<XOR1>(vv);
        const unsigned res = (e & 1) ? ((nb >> 16) | (vv & 0xffff0000u)) : ((vv & 0xffffu) | (nb << 16));
        __builtin_amdgcn_raw_buffer_store_b32(res, RSRC(out, 2560), lane < 8 ? (unsigned)(((e & 1) * 640 + (ocol & ~1)) * 2) : OOB, 0, AUX_SC1);
        if (blk == 0 && lane == 0) __builtin_amdgcn_raw_buffer_store_b32(tag, rws, 0, WS_EPOCH, AUX_SC1);
      }
    }
  }
}

extern "C" void kernel_launch(void* const* d_in, const int* in_sizes, int n_in, void* d_out, int out_size, void* d_ws, size_t ws_size, hipStream_t stream) {
  fused_decode<<<256, 640, 0, stream>>>((const unsigned short*)d_in[0], (const unsigned short*)d_in[1], (const unsigned short*)d_in[2],
      (const unsigned short*)d_in[3], (const unsigned short*)d_in[4], (const unsigned short*)d_in[6],
      (unsigned char*)d_ws, (const unsigned short*)d_in[7], (unsigned short*)d_out);
}
```

```cpp
#define OPRE 1
#define REDW_ 1
#define STAMP_DECL
#define STAMP_FLUSH() do {} while (0)
#include <hip/hip_runtime.h>
#include <hip/hip_bf16.h>
#include <stdint.h>

typedef unsigned u32x4 __attribute__((ext_vector_type(4)));
typedef unsigned u32x2 __attribute__((ext_vector_type(2)));
typedef float f32x4 __attribute__((ext_vector_type(4)));
typedef float f32x2 __attribute__((ext_vector_type(2)));
typedef __bf16 bf16x8 __attribute__((ext_vector_type(8)));
typedef __bf16 bf16x2 __attribute__((ext_vector_type(2)));
typedef short s16x4 __attribute__((ext_vector_type(4)));

constexpr int PDELAY = 32;
constexpr int ACKDELAY = 48;
constexpr int YDELAY = 4;
constexpr int ROTM = 37;
constexpr int AUX_NT = 2, AUX_SC1 = 16;
constexpr unsigned OOB = 0x7ffffff0u;
constexpr unsigned WS_EPOCH = 0;
constexpr unsigned WS_Q1 = 4096;
constexpr unsigned WS_H1 = WS_Q1 + 256 * 1024;
constexpr unsigned WS_H2 = WS_H1 + 256 * 1280;
constexpr unsigned WS_S = WS_H2 + 256 * 128;
constexpr unsigned WS_F = WS_S + 256 * 128;
constexpr unsigned WS_END = WS_F + 256 * 1280;
constexpr unsigned WS_O = WS_Q1, WS_Y = WS_H1;
constexpr unsigned SPIN_CAP = 1u << 16, LSPIN_CAP = 1u << 22;
constexpr int CODE_BYTES = 13312;
constexpr int L_RED = 0, L_WL = 16384, L_QS = L_WL + 20480, L_HS = L_QS + 256, L_ST = L_HS + 512, L_FLG = L_ST + 64, L_XL = L_FLG + 64, L_OS = L_XL + 2560, L_END = L_OS + 512;

#define RSRC(p, n) __builtin_amdgcn_make_buffer_rsrc((void*)(p), 0, (n), 0x00020000)
#define LD128(rs, vo, so, aux) __builtin_amdgcn_raw_buffer_load_b128((rs), (vo), (so), (aux))
#define ST128(v, rs, vo, so, aux) __builtin_amdgcn_raw_buffer_store_b128((v), (rs), (vo), (so), (aux))
#define SBAR() __builtin_amdgcn_sched_barrier(0)
#define CBAR() asm volatile("" ::: "memory")
#define LGKM0() asm volatile("s_waitcnt lgkmcnt(0)" ::: "memory")
#define LAS __attribute__((address_space(3)))
#define LDS_LD32(p) __hip_atomic_load((LAS unsigned*)(p), __ATOMIC_RELAXED, __HIP_MEMORY_SCOPE_WORKGROUP)
#define LDS_ST32(p, v) __hip_atomic_store((LAS unsigned*)(p), (v), __ATOMIC_RELAXED, __HIP_MEMORY_SCOPE_WORKGROUP)
#define LDS_LD64(p) __hip_atomic_load((LAS unsigned long long*)(p), __ATOMIC_RELAXED, __HIP_MEMORY_SCOPE_WORKGROUP)
#define LDS_ST64(p, v) __hip_atomic_store((LAS unsigned long long*)(p), (v), __ATOMIC_RELAXED, __HIP_MEMORY_SCOPE_WORKGROUP)
#define LDS_INC(p) asm volatile("ds_add_u32 %0, %1" :: "v"((unsigned)(size_t)(LAS unsigned*)(p)), "v"(1u) : "memory")

template <int CTRL> __device__ __forceinline__ unsigned dppu(unsigned x) { return (unsigned)__builtin_amdgcn_mov_dpp((int)x, CTRL, 0xf, 0xf, true); }
template <int CTRL> __device__ __forceinline__ float dppf(float x) { return __uint_as_float(dppu<CTRL>(__float_as_uint(x))); }
constexpr int XOR1 = 0xB1, XOR2 = 0x4E, XOR7 = 0x141, ROR8 = 0x128, ROR4 = 0x124, SHL4 = 0x104, SHR4 = 0x114, BCAST = 0x150;

__device__ __forceinline__ float dot2(unsigned k, unsigned q, float acc) {
  return __builtin_amdgcn_fdot2_f32_bf16(__builtin_bit_cast(bf16x2, k), __builtin_bit_cast(bf16x2, q), acc, false);
}
__device__ __forceinline__ unsigned pk_bf16(float lo, float hi) { bf16x2 v; v[0] = (__bf16)lo; v[1] = (__bf16)hi; return __builtin_bit_cast(unsigned, v); }
__device__ __forceinline__ float rbf(float f) { return __uint_as_float(pk_bf16(f, f) & 0xffff0000u); }
__device__ __forceinline__ float bflo(unsigned u) { return __uint_as_float(u << 16); }
__device__ __forceinline__ float bfhi(unsigned u) { return __uint_as_float(u & 0xffff0000u); }
__device__ __forceinline__ f32x4 mfma(u32x4 a, u32x4 b, f32x4 c) {
  return __builtin_amdgcn_mfma_f32_16x16x32_bf16(__builtin_bit_cast(bf16x8, a), __builtin_bit_cast(bf16x8, b), c, 0, 0, 0);
}
__device__ __forceinline__ float x16_sum(float x) { auto s = __builtin_amdgcn_permlane16_swap(__float_as_uint(x), __float_as_uint(x), false, false); return __uint_as_float(s[0]) + __uint_as_float(s[1]); }
__device__ __forceinline__ float x32_sum(float x) { auto s = __builtin_amdgcn_permlane32_swap(__float_as_uint(x), __float_as_uint(x), false, false); return __uint_as_float(s[0]) + __uint_as_float(s[1]); }
__device__ __forceinline__ float x16_max(float x) { auto s = __builtin_amdgcn_permlane16_swap(__float_as_uint(x), __float_as_uint(x), false, false); return fmaxf(__uint_as_float(s[0]), __uint_as_float(s[1])); }
__device__ __forceinline__ float x32_max(float x) { auto s = __builtin_amdgcn_permlane32_swap(__float_as_uint(x), __float_as_uint(x), false, false); return fmaxf(__uint_as_float(s[0]), __uint_as_float(s[1])); }
__device__ __forceinline__ float row_sum(float x) { x += dppf<XOR1>(x); x += dppf<XOR2>(x); x += dppf<ROR4>(x); x += dppf<ROR8>(x); return x; }
__device__ __forceinline__ float row_max(float x) { x = fmaxf(x, dppf<XOR1>(x)); x = fmaxf(x, dppf<XOR2>(x)); x = fmaxf(x, dppf<ROR4>(x)); x = fmaxf(x, dppf<ROR8>(x)); return x; }
__device__ __forceinline__ float oct_max(float x) { x = fmaxf(x, dppf<XOR1>(x)); x = fmaxf(x, dppf<XOR2>(x)); return fmaxf(x, dppf<XOR7>(x)); }
template <int C0, int C1> __device__ __forceinline__ float trsel(float lo, float hi, bool bit) {
  const float t0 = lo + dppf<C0>(lo), t1 = hi + dppf<C1>(hi);
  return bit ? t1 : t0;
}
__device__ __forceinline__ bool gok(u32x4 g, unsigned tag) { return (g[1] == tag) & (g[3] == tag); }
__device__ __forceinline__ u32x4 gran2(float a, float b, unsigned tag) { u32x4 g; g[0] = __float_as_uint(a); g[1] = tag; g[2] = __float_as_uint(b); g[3] = tag; return g; }
__device__ __forceinline__ float gv0(u32x4 g) { return __uint_as_float(g[0]); }
__device__ __forceinline__ float gv1(u32x4 g) { return __uint_as_float(g[2]); }
__device__ __forceinline__ void wg_barrier() { asm volatile("s_waitcnt lgkmcnt(0)\n\ts_barrier" ::: "memory"); }
__device__ __forceinline__ unsigned lds_flag(unsigned* p) { return __builtin_amdgcn_readfirstlane(LDS_LD32(p)); }
__device__ __forceinline__ f32x2 sum8(const float* p) {
  f32x2 a[8];
#pragma unroll
  for (int i = 0; i < 8; ++i) a[i] = *(const f32x2*)(p + i * 256);
  return ((a[0] + a[1]) + (a[2] + a[3])) + ((a[4] + a[5]) + (a[6] + a[7]));
}
__device__ __forceinline__ u32x2 trread(unsigned char* p) { return __builtin_bit_cast(u32x2, __builtin_amdgcn_ds_read_tr16_b64_v4i16((LAS s16x4*)p)); }
constexpr float LOG2E = 1.4426950408889634f;

__global__ void __launch_bounds__(640, 3)
fused_decode(const unsigned short* x, const unsigned short* kc, const unsigned short* vc,
             const unsigned short* wq, const unsigned short* wo, const unsigned short* w1,
             unsigned char* ws, const unsigned short* w2, unsigned short* out) {
  unsigned long long pc_entry;
  asm volatile("s_getpc_b64 %0" : "=s"(pc_entry));
  asm volatile("" : "+s"(x), "+s"(kc), "+s"(vc), "+s"(wq), "+s"(wo), "+s"(w1), "+s"(ws), "+s"(w2), "+s"(out));
  __shared__ __attribute__((aligned(16))) unsigned char smem[L_END];
  f32x4* red = (f32x4*)(smem + L_RED);
  unsigned* qs = (unsigned*)(smem + L_QS);
  unsigned* hs = (unsigned*)(smem + L_HS);
  float* st = (float*)(smem + L_ST);
  unsigned* flg = (unsigned*)(smem + L_FLG);
  unsigned* xl = (unsigned*)(smem + L_XL);
  unsigned* os = (unsigned*)(smem + L_OS);
  const int blk = blockIdx.x, tid = threadIdx.x, lane = tid & 63, w = __builtin_amdgcn_readfirstlane(tid >> 6);
  STAMP_DECL
  const unsigned tstart_ = (unsigned)__builtin_amdgcn_s_memrealtime();
  const int j = (blk >> 3) & 7, h = (blk & 7) + 8 * (blk >> 6), b = j >> 2, s = j & 3;
  const int r = lane >> 4, c = lane & 15;
  const bool ffn = h < 27;
  const unsigned cu = (unsigned)(h * 8 + j);
  const __amdgpu_buffer_rsrc_t rws = RSRC(ws, WS_END);
  const __amdgpu_buffer_rsrc_t rx = RSRC(x, 2560);
  u32x4 mask;
#pragma unroll
  for (int i = 0; i < 4; ++i) mask[i] = (i == ((c & 7) >> 1)) ? ((c & 1) ? 0xffff0000u : 0xffffu) : 0u;
  auto f_publish = [&](int cbase, unsigned tg) {
    const int cr = cbase + (lane >> 3), e = lane & 7;
    const float* p = (const float*)(red + 512) + ((e >> 2) * 16 + (cr & 15)) * 4 + (e & 3);
    float a0[8], a1[8];
#pragma unroll
    for (int i = 0; i < 8; ++i) { a0[i] = p[i * 256]; a1[i] = p[i * 256 + 128]; }
    const float v0 = ((a0[0] + a0[1]) + (a0[2] + a0[3])) + ((a0[4] + a0[5]) + (a0[6] + a0[7]));
    const float v1 = ((a1[0] + a1[1]) + (a1[2] + a1[3])) + ((a1[4] + a1[5]) + (a1[6] + a1[7]));
    ST128(gran2(v0, v1, tg), rws, cr < 10 ? cu * 1280 + (unsigned)(cr * 128 + e * 16) : OOB, WS_F, AUX_SC1);
  };

  if (w >= 2) {
    const int sw = w - 2;
    const size_t kvbase = ((size_t)(b * 32 + h) * 8192 + 6144 + s * 512) * 128;
    const __amdgpu_buffer_rsrc_t rk = RSRC(kc + kvbase, 131072), rv = RSRC(vc + kvbase, 131072);
    const int rot = sw * 4 + ((blk * ROTM) & 127);
    const int voff = lane * 16;
#define KVOFF(t) ((((((t) >> 2) * 32 + ((t) & 3) + rot) & 127) << 10))
    u32x4 K[16], V[16], WO[4], W2[2];
    float sc = 0.f;
    auto scores = [&]() {
      CBAR();
      const u32x4 q4 = *(const u32x4*)(qs + 4 * c);
      float sb[4];
#pragma unroll
      for (int be = 0; be < 4; ++be) {
        float d[4];
#pragma unroll
        for (int tt = 0; tt < 4; ++tt) {
          const u32x4 kk = K[4 * be + tt];
          d[tt] = dot2(kk[3], q4[3], dot2(kk[2], q4[2], dot2(kk[1], q4[1], dot2(kk[0], q4[0], 0.f))));
        }
        const float e0 = trsel<XOR1, XOR1>(d[0], d[1], c & 1);
        const float e1 = trsel<XOR1, XOR1>(d[2], d[3], c & 1);
        sb[be] = trsel<XOR2, XOR2>(e0, e1, c & 2);
      }
      const float f0 = trsel<SHL4, SHR4>(sb[0], sb[1], c & 4);
      const float f1 = trsel<SHL4, SHR4>(sb[2], sb[3], c & 4);
      const float dotv = trsel<ROR8, ROR8>(f0, f1, c & 8);
      sc = rbf(rbf(dotv) * 0.08838834764831845f);
      const float mw = x32_max(x16_max(row_max(sc)));
      const float lw = x32_sum(x16_sum(row_sum(__builtin_amdgcn_exp2f((sc - mw) * LOG2E))));
      if (lane == 0) LDS_ST64(st + 2 * sw, (unsigned long long)__float_as_uint(mw) | ((unsigned long long)__float_as_uint(lw) << 32));
    };
    unsigned cp0 = 0, cp1 = 0;
    if (sw == 7 && (blk >> 3) < 4) {
      const __amdgpu_buffer_rsrc_t rc = RSRC((void*)(pc_entry & ~255ull), CODE_BYTES);
      cp0 = __builtin_amdgcn_raw_buffer_load_b32(rc, lane * 128, 0, 0);
      cp1 = __builtin_amdgcn_raw_buffer_load_b32(rc, lane * 128, 8192, 0);
    }
    __builtin_amdgcn_s_setprio(3);
    wg_barrier();
#pragma unroll
    for (int i = 0; i < 24; ++i) {
      if (i == 16) __builtin_amdgcn_s_setprio(2);
      if (i < 16) K[i] = LD128(rk, voff, KVOFF(i), AUX_NT); else V[i - 16] = LD128(rv, voff, KVOFF(i - 16), AUX_NT);
      SBAR();
    }
    const bool early = lds_flag(flg + 1) != 0u;
    if (early) scores();
    SBAR();
    __builtin_amdgcn_s_setprio(1);
#pragma unroll
    for (int t = 8; t < 16; ++t) { V[t] = LD128(rv, voff, KVOFF(t), AUX_NT); SBAR(); }
    if (!early) {
      for (unsigned sp = 0; sp < LSPIN_CAP && lds_flag(flg + 1) == 0u; ++sp) __builtin_amdgcn_s_sleep(1);
      scores();
    }
    SBAR();
    __builtin_amdgcn_s_setprio(0);
    {
      const __amdgpu_buffer_rsrc_t ro = RSRC(wo + (size_t)h * 128 * 640 + j * 80, 128 * 1280 - j * 160);
      const __amdgpu_buffer_rsrc_t r2 = RSRC(w2 + (size_t)h * 64 * 640 + j * 80, ffn ? 64 * 1280 - j * 160 : 0);
      const unsigned vo4 = c < 10 ? (unsigned)(r * 4 * 1280 + c * 16) : OOB;
      const unsigned vo2 = c < 10 ? (unsigned)(r * 2 * 1280 + c * 16) : OOB;
#pragma unroll
      for (int i = 0; i < 4; ++i) { WO[i] = LD128(ro, vo4, (16 * sw + i) * 1280, AUX_NT); SBAR(); }
#pragma unroll
      for (int i = 0; i < 2; ++i) { W2[i] = LD128(r2, vo2, (8 * sw + i) * 1280, AUX_NT); SBAR(); }
    }
    asm volatile("" :: "v"(cp0), "v"(cp1));
    unsigned pp;
    {
      unsigned long long t_ = LDS_LD64(st + 2 * (lane & 7));
      for (unsigned sp = 0; !__all((unsigned)(t_ >> 32) != 0u) && sp < LSPIN_CAP; ++sp) { __builtin_amdgcn_s_sleep(1); t_ = LDS_LD64(st + 2 * (lane & 7)); }
      const float M = oct_max(__uint_as_float((unsigned)t_));
      const float pv = __builtin_amdgcn_exp2f((sc - M) * LOG2E);
      pp = pk_bf16(pv, pv);
    }
    const unsigned tagB = lds_flag(flg) ^ 0x80000000u;
    f32x4 oacc = {0.f, 0.f, 0.f, 0.f};
#define PVSTEP(t) oacc = mfma(mask & dppu<BCAST + (t)>(pp), V[t], oacc);
    PVSTEP(0) PVSTEP(1) PVSTEP(2) PVSTEP(3) PVSTEP(4) PVSTEP(5) PVSTEP(6) PVSTEP(7)
    PVSTEP(8) PVSTEP(9) PVSTEP(10) PVSTEP(11) PVSTEP(12) PVSTEP(13) PVSTEP(14) PVSTEP(15)
#undef PVSTEP
    red[sw * 64 + lane] = oacc;
    LGKM0();
    if (lane == 0) LDS_INC(flg + 4);
    if (ffn) {
      for (unsigned sp = 0; sp < LSPIN_CAP && lds_flag(flg + 2) == 0u; ++sp) __builtin_amdgcn_s_sleep(1);
      CBAR();
      const u32x2 hv = *(const u32x2*)(hs + (c >> 3) * 64 + 8 * sw + 2 * r);
      f32x4 facc = {0.f, 0.f, 0.f, 0.f};
      facc = mfma(mask & hv[0], W2[0], facc);
      facc = mfma(mask & hv[1], W2[1], facc);
      red[(8 + sw) * 64 + lane] = facc;
      LGKM0();
      if (lane == 0) LDS_INC(flg + 3);
    }
    if (c < 10) {
#pragma unroll
      for (int i = 0; i < 4; ++i) *(u32x4*)(smem + L_WL + (16 * sw + 4 * r + i) * 160 + c * 16) = WO[i];
    }
    const int tro = L_WL + (8 * r + ((lane >> 2) & 3)) * 160 + sw * 32 + (lane & 3) * 8;
    const unsigned yoff = lane < 16 ? cu * 1280 + (unsigned)(sw * 256 + lane * 16) : OOB;
    wg_barrier();
    if (sw < 5) {
      u32x4 A[4], B[4];
#pragma unroll
      for (int t = 0; t < 4; ++t) {
        A[t] = *(const u32x4*)(os + (c & 1) * 64 + t * 16 + r * 4);
        const u32x2 lo = trread(smem + tro + t * 5120), hi = trread(smem + tro + t * 5120 + 640);
        B[t] = u32x4{lo[0], lo[1], hi[0], hi[1]};
      }
      f32x4 y = {0.f, 0.f, 0.f, 0.f}, y2 = {0.f, 0.f, 0.f, 0.f};
      y = mfma(A[0], B[0], y); y2 = mfma(A[1], B[1], y2);
      y = mfma(A[2], B[2], y); y2 = mfma(A[3], B[3], y2);
      ST128(gran2(y[0] + y2[0], y[1] + y2[1], tagB), rws, yoff, WS_Y, AUX_SC1);
    }
  } else {
    __builtin_amdgcn_s_setprio(3);
    const bool isq = (w == 0);
    const unsigned epoch = __builtin_amdgcn_raw_buffer_load_b32(rws, 0, WS_EPOCH, AUX_SC1);
    u32x2 xs[5];
#pragma unroll
    for (int i = 0; i < 5; ++i) xs[i] = __builtin_amdgcn_raw_buffer_load_b64(rx, (lane >> 5) * 1280 + (lane & 31) * 8, i * 256, 0);
    const int row0 = isq ? 160 * (j >> 1) : 80 * j;
    const int nrow = isq ? 160 : 80;
    unsigned short xr[5];
#pragma unroll
    for (int i = 0; i < 5; ++i) xr[i] = __builtin_amdgcn_raw_buffer_load_b16(rx, (lane & 31) + 32 * i < nrow ? (unsigned)((lane >> 5) * 1280 + (lane & 31) * 2) : OOB, (row0 + 32 * i) * 2, 0);
    u32x4 wa[20];
    {
      const __amdgpu_buffer_rsrc_t rw = isq ? RSRC(wq + h * 128 + (j & 1) * 64, 640 * 4096 * 2 - h * 256 - (j & 1) * 128) : RSRC(w1 + h * 64, ffn ? 640 * 1728 * 2 - h * 128 : 0);
      const int rstride = isq ? 8192 : 3456;
      const unsigned vo = (unsigned)((lane >> 3) * rstride + (lane & 7) * 16);
#pragma unroll
      for (int i = 0; i < 10; ++i) wa[i] = LD128(rw, vo, (row0 + 8 * i) * rstride, AUX_NT);
      if (isq) {
#pragma unroll
        for (int i = 10; i < 20; ++i) wa[i] = LD128(rw, vo, (row0 + 8 * i) * rstride, AUX_NT);
      }
    }
    if (isq && lane < 24) LDS_ST32(st + lane, 0u);
    wg_barrier();
    float xf[20];
#pragma unroll
    for (int i = 0; i < 5; ++i) { xf[4 * i] = bflo(xs[i][0]); xf[4 * i + 1] = bfhi(xs[i][0]); xf[4 * i + 2] = bflo(xs[i][1]); xf[4 * i + 3] = bfhi(xs[i][1]); }
    float sum = 0.f;
#pragma unroll
    for (int i = 0; i < 20; ++i) sum += xf[i];
    sum = x16_sum(row_sum(sum));
    const float mean = sum * (1.f / 640.f);
    float vs = 0.f;
#pragma unroll
    for (int i = 0; i < 20; ++i) { const float d = xf[i] - mean; vs = fmaf(d, d, vs); }
    vs = x16_sum(row_sum(vs));
    const float mb = rbf(mean), den = rbf(sqrtf(rbf(rbf(vs * (1.f / 640.f)) + 1e-5f)));
    unsigned* xw_ = xl + w * 320;
#pragma unroll
    for (int i = 0; i < 5; ++i) {
      const int row = (lane & 31) + 32 * i;
      const float xn = rbf(bflo(xr[i]) - mb) / den;
      if (row < nrow) xw_[(lane >> 5) * 160 + ((row >> 1) & 3) * 40 + (row >> 3) * 2 + (row & 1)] = pk_bf16(xn, xn);
    }
    const unsigned tag = __builtin_amdgcn_readfirstlane(epoch) + 0x9E3779B9u, tagB = tag ^ 0x80000000u;
    if (isq && lane == 0) LDS_ST32(flg, tag);
    LGKM0();
    f32x4 acc0 = {0.f, 0.f, 0.f, 0.f}, acc1 = {0.f, 0.f, 0.f, 0.f};
    const unsigned* xrd = xw_ + (c >> 3) * 160 + r * 40;
#pragma unroll
    for (int i = 0; i < 10; i += 2) {
      const u32x4 xa = *(const u32x4*)(xrd + 2 * i);
      acc0 = mfma(mask & xa[0], wa[i], acc0); acc1 = mfma(mask & xa[1], wa[i], acc1);
      acc0 = mfma(mask & xa[2], wa[i + 1], acc0); acc1 = mfma(mask & xa[3], wa[i + 1], acc1);
    }
    if (isq) {
#pragma unroll
      for (int i = 10; i < 20; i += 2) {
        const u32x4 xa = *(const u32x4*)(xrd + 2 * i);
        acc0 = mfma(mask & xa[0], wa[i], acc0); acc1 = mfma(mask & xa[1], wa[i], acc1);
        acc0 = mfma(mask & xa[2], wa[i + 1], acc0); acc1 = mfma(mask & xa[3], wa[i + 1], acc1);
      }
    }
    f32x4 v;
#pragma unroll
    for (int i = 0; i < 4; ++i) { const float t_ = c < 8 ? acc0[i] : acc1[i]; v[i] = t_ + dppf<ROR8>(t_); }
    const u32x4 pub = gran2(c < 8 ? v[0] : v[2], c < 8 ? v[1] : v[3], tag);
    if (isq) {
      ST128(pub, rws, cu * 1024 + lane * 16, WS_Q1, AUX_SC1);
      __builtin_amdgcn_s_sleep(PDELAY);
      const unsigned goff = (unsigned)((h * 8 + (lane >> 5)) * 1024 + (b * 32 + (lane & 31)) * 16);
      u32x4 G[8];
#pragma unroll
      for (int p = 0; p < 4; ++p) G[p] = LD128(rws, goff, WS_Q1 + p * 2048, AUX_SC1);
      for (unsigned sp = 0;; ++sp) {
        bool allok = true;
        CBAR();
#pragma unroll
        for (int p = 0; p < 4; ++p) if (!__all(gok(G[p], tag))) { allok = false; G[p] = LD128(rws, goff, WS_Q1 + p * 2048, AUX_SC1); }
        if (allok || sp > SPIN_CAP) break;
      }
      qs[(lane >> 5) * 32 + (c & 7) * 4 + (r & 1) * 2 + (c >> 3)] = pk_bf16((gv0(G[0]) + gv0(G[1])) + (gv0(G[2]) + gv0(G[3])), (gv1(G[0]) + gv1(G[1])) + (gv1(G[2]) + gv1(G[3])));
      ST128(gran2(0.f, 0.f, tag), rws, lane == 0 ? cu * 128 + 16 : OOB, WS_S, AUX_SC1);
      LGKM0();
      if (lane == 0) LDS_ST32(flg + 1, 1u);
      const unsigned soff = WS_S + (unsigned)((h * 8 + (lane & 7)) * 128 + (lane & 8) * 2);
      __builtin_amdgcn_s_sleep(ACKDELAY);
      u32x4 gs = LD128(rws, soff, 0, AUX_SC1);
      unsigned long long t_ = LDS_LD64(st + 2 * (lane & 7));
      for (unsigned sp = 0; !__all((unsigned)(t_ >> 32) != 0u) && sp < LSPIN_CAP; ++sp) { __builtin_amdgcn_s_sleep(2); t_ = LDS_LD64(st + 2 * (lane & 7)); }
      {
        const float m_ = __uint_as_float((unsigned)t_), l_ = __uint_as_float((unsigned)(t_ >> 32));
        const float M = oct_max(m_);
        float L = __builtin_amdgcn_exp2f((m_ - M) * LOG2E) * l_; L += dppf<XOR1>(L); L += dppf<XOR2>(L); L += dppf<XOR7>(L);
        ST128(gran2(M, L, tag), rws, lane == 0 ? cu * 128 : OOB, WS_S, AUX_SC1);
      }
      if (!__all(gok(gs, tag) | !(lane & 8))) {
        for (unsigned sp = 0; sp < SPIN_CAP; ++sp) {
          CBAR();
          gs = LD128(rws, soff, 0, AUX_SC1);
          if (__all(gok(gs, tag) | !(lane & 8))) break;
        }
      }
      for (unsigned sp = 0; sp < LSPIN_CAP && lds_flag(flg + 4) != 8u; ++sp) {}
      CBAR();
      const unsigned ooff = (unsigned)(h * 8 * 1024 + lane * 16);
      const f32x2 own = sum8((const float*)red + (((lane >> 1) & 1) * 16 + (lane >> 2)) * 4 + 2 * (lane & 1));
      const u32x4 ownG = gran2(own[0], own[1], tagB);
      ST128(ownG, rws, ooff, WS_O + j * 1024, AUX_SC1);
      gs = LD128(rws, soff, 0, AUX_SC1);
#pragma unroll
      for (int p = 0; p < 8; ++p) G[p] = LD128(rws, p == j ? OOB : ooff, WS_O + p * 1024, AUX_SC1);
#pragma unroll
      for (int p = 0; p < 8; ++p) if (p == j) G[p] = ownG;
      for (unsigned sp = 0; !__all(gok(gs, tag)) && sp < SPIN_CAP; ++sp) { CBAR(); __builtin_amdgcn_s_sleep(2); gs = LD128(rws, soff, 0, AUX_SC1); }
      float wp[8];
      {
        const float ms = gv0(gs), ls = gv1(gs);
        float Mg = fmaxf(ms, dppf<XOR1>(ms)); Mg = fmaxf(Mg, dppf<XOR2>(Mg));
        const float e_ = __builtin_amdgcn_exp2f((ms - Mg) * LOG2E);
        float Lg = e_ * ls; Lg += dppf<XOR1>(Lg); Lg += dppf<XOR2>(Lg);
        const float wgt = e_ / Lg;
#pragma unroll
        for (int p = 0; p < 8; ++p) wp[p] = __uint_as_float(__builtin_amdgcn_readlane(__float_as_uint(wgt), p));
      }
      for (unsigned sp = 0;; ++sp) {
        bool allok = true;
        CBAR();
#pragma unroll
        for (int p = 0; p < 8; ++p) if (!__all(gok(G[p], tagB))) { allok = false; G[p] = LD128(rws, ooff, WS_O + p * 1024, AUX_SC1); }
        if (allok || sp > SPIN_CAP) break;
      }
#pragma unroll
      for (int bb = 0; bb < 2; ++bb) {
        float o0 = 0.f, o1 = 0.f;
#pragma unroll
        for (int sp = 0; sp < 4; ++sp) { o0 = fmaf(wp[4 * bb + sp], gv0(G[4 * bb + sp]), o0); o1 = fmaf(wp[4 * bb + sp], gv1(G[4 * bb + sp]), o1); }
        os[bb * 64 + lane] = pk_bf16(o0, o1);
      }
      wg_barrier();
    } else {
      if (ffn) {
        ST128(pub, rws, (unsigned)((h * 8 + (c & 7)) * 1280 + j * 128 + (c >> 3) * 64 + r * 16), WS_H1, AUX_SC1);
        __builtin_amdgcn_s_sleep(2 * PDELAY);
        const unsigned goff = cu * 1280 + lane * 16;
        u32x4 G0 = LD128(rws, goff, WS_H1, AUX_SC1);
        for (unsigned sp = 0; !__all(gok(G0, tag)) && sp < SPIN_CAP; ++sp) { CBAR(); G0 = LD128(rws, goff, WS_H1, AUX_SC1); }
        float a0 = gv0(G0), a1 = gv1(G0);
        a0 += dppf<ROR8>(a0); a1 += dppf<ROR8>(a1);
        a0 = fmaxf(x32_sum(x16_sum(a0)), 0.f); a1 = fmaxf(x32_sum(x16_sum(a1)), 0.f);
        u32x2 g2; g2[0] = pk_bf16(a0, a1); g2[1] = tag;
        __builtin_amdgcn_raw_buffer_store_b64(g2, rws, lane < 8 ? cu * 128 + lane * 8 : OOB, WS_H2, AUX_SC1);
        const unsigned g2off = (unsigned)((h * 8 + (lane >> 3)) * 128 + (lane & 7) * 8);
        __builtin_amdgcn_s_sleep(3 * PDELAY);
        u32x2 q2 = __builtin_amdgcn_raw_buffer_load_b64(rws, g2off, WS_H2, AUX_SC1);
        for (unsigned sp = 0; !__all(q2[1] == tag) && sp < SPIN_CAP; ++sp) { CBAR(); q2 = __builtin_amdgcn_raw_buffer_load_b64(rws, g2off, WS_H2, AUX_SC1); }
        u32x2 hv; hv[0] = (q2[0] & 0xffffu) | (q2[0] << 16); hv[1] = (q2[0] >> 16) | (q2[0] & 0xffff0000u);
        *(u32x2*)(hs + ((lane >> 1) & 1) * 64 + (lane >> 3) * 8 + (lane & 1) * 4 + ((lane >> 2) & 1) * 2) = hv;
        LGKM0();
        if (lane == 0) LDS_ST32(flg + 2, 1u);
        for (unsigned sp = 0; sp < LSPIN_CAP && lds_flag(flg + 3) != 8u; ++sp) __builtin_amdgcn_s_sleep(1);
        CBAR();
        f_publish(0, tag);
        f_publish(8, tag);
      }
      const int e = lane & 7, ocol = j * 80 + 8 * h + e;
      const bool fin = h < 10, f3v = (lane >> 3) < 3;
      const unsigned yo = (unsigned)(((lane >> 3) * 8 + j) * 1280 + h * 128 + e * 16);
      unsigned short x0 = 0, x1 = 0;
      u32x4 g[4], y[4];
      if (fin) { x0 = __builtin_amdgcn_raw_buffer_load_b16(rx, ocol * 2, 0, 0); x1 = __builtin_amdgcn_raw_buffer_load_b16(rx, ocol * 2, 1280, 0); }
      if (OPRE && fin) __builtin_amdgcn_raw_buffer_store_b32(((unsigned)x0 << 16) | x0, RSRC(out, 2560), lane < 8 ? (unsigned)(((e & 1) * 640 + (ocol & ~1)) * 2) : OOB, 0, AUX_SC1);
      wg_barrier();
      if (fin) {
#pragma unroll
        for (int i = 0; i < 4; ++i) g[i] = LD128(rws, (i < 3 || f3v) ? yo : OOB, WS_F + i * 64 * 1280, AUX_SC1);
        __builtin_amdgcn_s_sleep(YDELAY);
#pragma unroll
        for (int i = 0; i < 4; ++i) y[i] = LD128(rws, yo, WS_Y + i * 64 * 1280, AUX_SC1);
        for (unsigned sp = 0;; ++sp) {
          bool allok = true;
          CBAR();
#pragma unroll
          for (int i = 0; i < 4; ++i) if (!__all(gok(g[i], tag) | (i == 3 && !f3v))) { allok = false; g[i] = LD128(rws, (i < 3 || f3v) ? yo : OOB, WS_F + i * 64 * 1280, AUX_SC1); }
          if (allok || sp > SPIN_CAP) break;
        }
        float fa = (gv0(g[0]) + gv0(g[1])) + (gv0(g[2]) + gv0(g[3])), fb = (gv1(g[0]) + gv1(g[1])) + (gv1(g[2]) + gv1(g[3]));
        fa += dppf<ROR8>(fa); fb += dppf<ROR8>(fb);
        fa = rbf(x32_sum(x16_sum(fa))); fb = rbf(x32_sum(x16_sum(fb)));
        const float ga = rbf(fa / (1.f + __expf(-fa))), gb = rbf(fb / (1.f + __expf(-fb)));
        for (unsigned sp = 0;; ++sp) {
          bool allok = true;
          CBAR();
#pragma unroll
          for (int i = 0; i < 4; ++i) if (!__all(gok(y[i], tagB))) { allok = false; y[i] = LD128(rws, yo, WS_Y + i * 64 * 1280, AUX_SC1); }
          if (allok || sp > SPIN_CAP) break;
        }
        float ya = (gv0(y[0]) + gv0(y[1])) + (gv0(y[2]) + gv0(y[3])), yb = (gv1(y[0]) + gv1(y[1])) + (gv1(y[2]) + gv1(y[3]));
        ya += dppf<ROR8>(ya); yb += dppf<ROR8>(yb);
        ya = rbf(x32_sum(x16_sum(ya))); yb = rbf(x32_sum(x16_sum(yb)));
        const unsigned vv = pk_bf16(bflo(x0) + rbf(ya * ga), bflo(x1) + rbf(yb * gb)), nb = dppu<XOR1>(vv);
        const unsigned res = (e & 1) ? ((nb >> 16) | (vv & 0xffff0000u)) : ((vv & 0xffffu) | (nb << 16));
        __builtin_amdgcn_raw_buffer_store_b32(res, RSRC(out, 2560), lane < 8 ? (unsigned)(((e & 1) * 640 + (ocol & ~1)) * 2) : OOB, 0, AUX_SC1);
        if (blk == 0 && lane == 0) __builtin_amdgcn_raw_buffer_store_b32(tag, rws, 0, WS_EPOCH, AUX_SC1);
      }
    }
  }
  if (w == REDW_ && h < 10) { asm volatile("s_waitcnt vmcnt(0)" ::: "memory"); unsigned t_ = (unsigned)__builtin_amdgcn_s_memrealtime(); unsigned d_ = (t_ - tstart_) * 10u; if (d_ > 60000u) d_ = 60000u; while ((unsigned)__builtin_amdgcn_s_memrealtime() - t_ < d_) __builtin_amdgcn_s_sleep(8); }
}

extern "C" void kernel_launch(void* const* d_in, const int* in_sizes, int n_in, void* d_out, int out_size, void* d_ws, size_t ws_size, hipStream_t stream) {
  fused_decode<<<256, 640, 0, stream>>>((const unsigned short*)d_in[0], (const unsigned short*)d_in[1], (const unsigned short*)d_in[2],
      (const unsigned short*)d_in[3], (const unsigned short*)d_in[4], (const unsigned short*)d_in[6],
      (unsigned char*)d_ws, (const unsigned short*)d_in[7], (unsigned short*)d_out);
}
```

```cpp
#include <hip/hip_runtime.h>
#include <hip/hip_bf16.h>
#include <stdint.h>

typedef unsigned u32x4 __attribute__((ext_vector_type(4)));
typedef unsigned u32x2 __attribute__((ext_vector_type(2)));
typedef float f32x4 __attribute__((ext_vector_type(4)));
typedef float f32x2 __attribute__((ext_vector_type(2)));
typedef __bf16 bf16x8 __attribute__((ext_vector_type(8)));
typedef __bf16 bf16x2 __attribute__((ext_vector_type(2)));
typedef short s16x4 __attribute__((ext_vector_type(4)));

constexpr int PDELAY = 32;
constexpr int ACKDELAY = 48;
constexpr int YDELAY = 4;
constexpr int ROTM = 21;
constexpr int AUX_NT = 2, AUX_SC1 = 16;
constexpr unsigned OOB = 0x7ffffff0u;
constexpr unsigned WS_EPOCH = 0;
constexpr unsigned WS_Q1 = 4096;
constexpr unsigned WS_H1 = WS_Q1 + 256 * 1024;
constexpr unsigned WS_H2 = WS_H1 + 256 * 1280;
constexpr unsigned WS_S = WS_H2 + 256 * 128;
constexpr unsigned WS_F = WS_S + 256 * 128;
constexpr unsigned WS_END = WS_F + 256 * 1280;
constexpr unsigned WS_O = WS_Q1, WS_Y = WS_H1;
constexpr unsigned SPIN_CAP = 1u << 16, LSPIN_CAP = 1u << 22;
constexpr int CODE_BYTES = 13312;
constexpr int L_RED = 0, L_WL = 16384, L_QS = L_WL + 20480, L_HS = L_QS + 256, L_ST = L_HS + 512, L_FLG = L_ST + 64, L_XL = L_FLG + 64, L_OS = L_XL + 2560, L_END = L_OS + 512;

#define RSRC(p, n) __builtin_amdgcn_make_buffer_rsrc((void*)(p), 0, (n), 0x00020000)
#define LD128(rs, vo, so, aux) __builtin_amdgcn_raw_buffer_load_b128((rs), (vo), (so), (aux))
#define ST128(v, rs, vo, so, aux) __builtin_amdgcn_raw_buffer_store_b128((v), (rs), (vo), (so), (aux))
#define SBAR() __builtin_amdgcn_sched_barrier(0)
#define CBAR() asm volatile("" ::: "memory")
#define LGKM0() asm volatile("s_waitcnt lgkmcnt(0)" ::: "memory")
#define LAS __attribute__((address_space(3)))
#define LDS_LD32(p) __hip_atomic_load((LAS unsigned*)(p), __ATOMIC_RELAXED, __HIP_MEMORY_SCOPE_WORKGROUP)
#define LDS_ST32(p, v) __hip_atomic_store((LAS unsigned*)(p), (v), __ATOMIC_RELAXED, __HIP_MEMORY_SCOPE_WORKGROUP)
#define LDS_LD64(p) __hip_atomic_load((LAS unsigned long long*)(p), __ATOMIC_RELAXED, __HIP_MEMORY_SCOPE_WORKGROUP)
#define LDS_ST64(p, v) __hip_atomic_store((LAS unsigned long long*)(p), (v), __ATOMIC_RELAXED, __HIP_MEMORY_SCOPE_WORKGROUP)
#define LDS_INC(p) asm volatile("ds_add_u32 %0, %1" :: "v"((unsigned)(size_t)(LAS unsigned*)(p)), "v"(1u) : "memory")

template <int CTRL> __device__ __forceinline__ unsigned dppu(unsigned x) { return (unsigned)__builtin_amdgcn_mov_dpp((int)x, CTRL, 0xf, 0xf, true); }
template <int CTRL> __device__ __forceinline__ float dppf(float x) { return __uint_as_float(dppu<CTRL>(__float_as_uint(x))); }
constexpr int XOR1 = 0xB1, XOR2 = 0x4E, XOR7 = 0x141, ROR8 = 0x128, ROR4 = 0x124, SHL4 = 0x104, SHR4 = 0x114, BCAST = 0x150;

__device__ __forceinline__ float dot2(unsigned k, unsigned q, float acc) {
  return __builtin_amdgcn_fdot2_f32_bf16(__builtin_bit_cast(bf16x2, k), __builtin_bit_cast(bf16x2, q), acc, false);
}
__device__ __forceinline__ unsigned pk_bf16(float lo, float hi) { bf16x2 v; v[0] = (__bf16)lo; v[1] = (__bf16)hi; return __builtin_bit_cast(unsigned, v); }
__device__ __forceinline__ float rbf(float f) { return __uint_as_float(pk_bf16(f, f) & 0xffff0000u); }
__device__ __forceinline__ float bflo(unsigned u) { return __uint_as_float(u << 16); }
__device__ __forceinline__ float bfhi(unsigned u) { return __uint_as_float(u & 0xffff0000u); }
__device__ __forceinline__ f32x4 mfma(u32x4 a, u32x4 b, f32x4 c) {
  return __builtin_amdgcn_mfma_f32_16x16x32_bf16(__builtin_bit_cast(bf16x8, a), __builtin_bit_cast(bf16x8, b), c, 0, 0, 0);
}
__device__ __forceinline__ float x16_sum(float x) { auto s = __builtin_amdgcn_permlane16_swap(__float_as_uint(x), __float_as_uint(x), false, false); return __uint_as_float(s[0]) + __uint_as_float(s[1]); }
__device__ __forceinline__ float x32_sum(float x) { auto s = __builtin_amdgcn_permlane32_swap(__float_as_uint(x), __float_as_uint(x), false, false); return __uint_as_float(s[0]) + __uint_as_float(s[1]); }
__device__ __forceinline__ float x16_max(float x) { auto s = __builtin_amdgcn_permlane16_swap(__float_as_uint(x), __float_as_uint(x), false, false); return fmaxf(__uint_as_float(s[0]), __uint_as_float(s[1])); }
__device__ __forceinline__ float x32_max(float x) { auto s = __builtin_amdgcn_permlane32_swap(__float_as_uint(x), __float_as_uint(x), false, false); return fmaxf(__uint_as_float(s[0]), __uint_as_float(s[1])); }
__device__ __forceinline__ float row_sum(float x) { x += dppf<XOR1>(x); x += dppf<XOR2>(x); x += dppf<ROR4>(x); x += dppf<ROR8>(x); return x; }
__device__ __forceinline__ float row_max(float x) { x = fmaxf(x, dppf<XOR1>(x)); x = fmaxf(x, dppf<XOR2>(x)); x = fmaxf(x, dppf<ROR4>(x)); x = fmaxf(x, dppf<ROR8>(x)); return x; }
__device__ __forceinline__ float oct_max(float x) { x = fmaxf(x, dppf<XOR1>(x)); x = fmaxf(x, dppf<XOR2>(x)); return fmaxf(x, dppf<XOR7>(x)); }
template <int C0, int C1> __device__ __forceinline__ float trsel(float lo, float hi, bool bit) {
  const float t0 = lo + dppf<C0>(lo), t1 = hi + dppf<C1>(hi);
  return bit ? t1 : t0;
}
__device__ __forceinline__ bool gok(u32x4 g, unsigned tag) { return (g[1] == tag) & (g[3] == tag); }
__device__ __forceinline__ u32x4 gran2(float a, float b, unsigned tag) { u32x4 g; g[0] = __float_as_uint(a); g[1] = tag; g[2] = __float_as_uint(b); g[3] = tag; return g; }
__device__ __forceinline__ float gv0(u32x4 g) { return __uint_as_float(g[0]); }
__device__ __forceinline__ float gv1(u32x4 g) { return __uint_as_float(g[2]); }
__device__ __forceinline__ void wg_barrier() { asm volatile("s_waitcnt lgkmcnt(0)\n\ts_barrier" ::: "memory"); }
__device__ __forceinline__ unsigned lds_flag(unsigned* p) { return __builtin_amdgcn_readfirstlane(LDS_LD32(p)); }
__device__ __forceinline__ f32x2 sum8(const float* p) {
  f32x2 a[8];
#pragma unroll
  for (int i = 0; i < 8; ++i) a[i] = *(const f32x2*)(p + i * 256);
  return ((a[0] + a[1]) + (a[2] + a[3])) + ((a[4] + a[5]) + (a[6] + a[7]));
}
__device__ __forceinline__ u32x2 trread(unsigned char* p) { return __builtin_bit_cast(u32x2, __builtin_amdgcn_ds_read_tr16_b64_v4i16((LAS s16x4*)p)); }
constexpr float LOG2E = 1.4426950408889634f;

__global__ void __launch_bounds__(640, 3)
fused_decode(const unsigned short* x, const unsigned short* kc, const unsigned short* vc,
             const unsigned short* wq, const unsigned short* wo, const unsigned short* w1,
             unsigned char* ws, const unsigned short* w2, unsigned short* out) {
  unsigned long long pc_entry;
  asm volatile("s_getpc_b64 %0" : "=s"(pc_entry));
  asm volatile("" : "+s"(x), "+s"(kc), "+s"(vc), "+s"(wq), "+s"(wo), "+s"(w1), "+s"(ws), "+s"(w2), "+s"(out));
  __shared__ __attribute__((aligned(16))) unsigned char smem[L_END];
  f32x4* red = (f32x4*)(smem + L_RED);
  unsigned* qs = (unsigned*)(smem + L_QS);
  unsigned* hs = (unsigned*)(smem + L_HS);
  float* st = (float*)(smem + L_ST);
  unsigned* flg = (unsigned*)(smem + L_FLG);
  unsigned* xl = (unsigned*)(smem + L_XL);
  unsigned* os = (unsigned*)(smem + L_OS);
  const int blk = blockIdx.x, tid = threadIdx.x, lane = tid & 63, w = __builtin_amdgcn_readfirstlane(tid >> 6);
  const int j = (blk >> 3) & 7, h = (blk & 7) + 8 * (blk >> 6), b = j >> 2, s = j & 3;
  const int r = lane >> 4, c = lane & 15;
  const bool ffn = h < 27;
  const unsigned cu = (unsigned)(h * 8 + j);
  const __amdgpu_buffer_rsrc_t rws = RSRC(ws, WS_END);
  const __amdgpu_buffer_rsrc_t rx = RSRC(x, 2560);
  u32x4 mask;
#pragma unroll
  for (int i = 0; i < 4; ++i) mask[i] = (i == ((c & 7) >> 1)) ? ((c & 1) ? 0xffff0000u : 0xffffu) : 0u;
  auto f_publish = [&](int cbase, unsigned tg) {
    const int cr = cbase + (lane >> 3), e = lane & 7;
    const float* p = (const float*)(red + 512) + ((e >> 2) * 16 + (cr & 15)) * 4 + (e & 3);
    float a0[8], a1[8];
#pragma unroll
    for (int i = 0; i < 8; ++i) { a0[i] = p[i * 256]; a1[i] = p[i * 256 + 128]; }
    const float v0 = ((a0[0] + a0[1]) + (a0[2] + a0[3])) + ((a0[4] + a0[5]) + (a0[6] + a0[7]));
    const float v1 = ((a1[0] + a1[1]) + (a1[2] + a1[3])) + ((a1[4] + a1[5]) + (a1[6] + a1[7]));
    ST128(gran2(v0, v1, tg), rws, cr < 10 ? cu * 1280 + (unsigned)(cr * 128 + e * 16) : OOB, WS_F, AUX_SC1);
  };

  if (w >= 2) {
    const int sw = w - 2;
    const size_t kvbase = ((size_t)(b * 32 + h) * 8192 + 6144 + s * 512) * 128;
    const __amdgpu_buffer_rsrc_t rk = RSRC(kc + kvbase, 131072), rv = RSRC(vc + kvbase, 131072);
    const int rot = sw * 4 + ((blk * ROTM) & 127);
    const int voff = lane * 16;
#define KVOFF(t) ((((((t) >> 2) * 32 + ((t) & 3) + rot) & 127) << 10))
    u32x4 K[16], V[16], WO[4], W2[2];
    float sc = 0.f;
    auto scores = [&]() {
      CBAR();
      const u32x4 q4 = *(const u32x4*)(qs + 4 * c);
      float sb[4];
#pragma unroll
      for (int be = 0; be < 4; ++be) {
        float d[4];
#pragma unroll
        for (int tt = 0; tt < 4; ++tt) {
          const u32x4 kk = K[4 * be + tt];
          d[tt] = dot2(kk[3], q4[3], dot2(kk[2], q4[2], dot2(kk[1], q4[1], dot2(kk[0], q4[0], 0.f))));
        }
        const float e0 = trsel<XOR1, XOR1>(d[0], d[1], c & 1);
        const float e1 = trsel<XOR1, XOR1>(d[2], d[3], c & 1);
        sb[be] = trsel<XOR2, XOR2>(e0, e1, c & 2);
      }
      const float f0 = trsel<SHL4, SHR4>(sb[0], sb[1], c & 4);
      const float f1 = trsel<SHL4, SHR4>(sb[2], sb[3], c & 4);
      const float dotv = trsel<ROR8, ROR8>(f0, f1, c & 8);
      sc = rbf(rbf(dotv) * 0.08838834764831845f);
      const float mw = x32_max(x16_max(row_max(sc)));
      const float lw = x32_sum(x16_sum(row_sum(__builtin_amdgcn_exp2f((sc - mw) * LOG2E))));
      if (lane == 0) LDS_ST64(st + 2 * sw, (unsigned long long)__float_as_uint(mw) | ((unsigned long long)__float_as_uint(lw) << 32));
    };
    unsigned cp0 = 0, cp1 = 0;
    if (sw == 7 && (blk >> 3) < 4) {
      const __amdgpu_buffer_rsrc_t rc = RSRC((void*)(pc_entry & ~255ull), CODE_BYTES);
      cp0 = __builtin_amdgcn_raw_buffer_load_b32(rc, lane * 128, 0, 0);
      cp1 = __builtin_amdgcn_raw_buffer_load_b32(rc, lane * 128, 8192, 0);
    }
    __builtin_amdgcn_s_setprio(3);
    wg_barrier();
#pragma unroll
    for (int i = 0; i < 24; ++i) {
      if (i == 16) __builtin_amdgcn_s_setprio(2);
      if (i < 16) K[i] = LD128(rk, voff, KVOFF(i), AUX_NT); else V[i - 16] = LD128(rv, voff, KVOFF(i - 16), AUX_NT);
      SBAR();
    }
    const bool early = lds_flag(flg + 1) != 0u;
    if (early) scores();
    SBAR();
    __builtin_amdgcn_s_setprio(1);
#pragma unroll
    for (int t = 8; t < 16; ++t) { V[t] = LD128(rv, voff, KVOFF(t), AUX_NT); SBAR(); }
    if (!early) {
      for (unsigned sp = 0; sp < LSPIN_CAP && lds_flag(flg + 1) == 0u; ++sp) __builtin_amdgcn_s_sleep(1);
      scores();
    }
    SBAR();
    __builtin_amdgcn_s_setprio(0);
    {
      const __amdgpu_buffer_rsrc_t ro = RSRC(wo + (size_t)h * 128 * 640 + j * 80, 128 * 1280 - j * 160);
      const __amdgpu_buffer_rsrc_t r2 = RSRC(w2 + (size_t)h * 64 * 640 + j * 80, ffn ? 64 * 1280 - j * 160 : 0);
      const unsigned vo4 = c < 10 ? (unsigned)(r * 4 * 1280 + c * 16) : OOB;
      const unsigned vo2 = c < 10 ? (unsigned)(r * 2 * 1280 + c * 16) : OOB;
#pragma unroll
      for (int i = 0; i < 4; ++i) { WO[i] = LD128(ro, vo4, (16 * sw + i) * 1280, AUX_NT); SBAR(); }
#pragma unroll
      for (int i = 0; i < 2; ++i) { W2[i] = LD128(r2, vo2, (8 * sw + i) * 1280, AUX_NT); SBAR(); }
    }
    asm volatile("" :: "v"(cp0), "v"(cp1));
    unsigned pp;
    {
      unsigned long long t_ = LDS_LD64(st + 2 * (lane & 7));
      for (unsigned sp = 0; !__all((unsigned)(t_ >> 32) != 0u) && sp < LSPIN_CAP; ++sp) { __builtin_amdgcn_s_sleep(1); t_ = LDS_LD64(st + 2 * (lane & 7)); }
      const float M = oct_max(__uint_as_float((unsigned)t_));
      const float pv = __builtin_amdgcn_exp2f((sc - M) * LOG2E);
      pp = pk_bf16(pv, pv);
    }
    const unsigned tagB = lds_flag(flg) ^ 0x80000000u;
    f32x4 oacc = {0.f, 0.f, 0.f, 0.f};
#define PVSTEP(t) oacc = mfma(mask & dppu<BCAST + (t)>(pp), V[t], oacc);
    PVSTEP(0) PVSTEP(1) PVSTEP(2) PVSTEP(3) PVSTEP(4) PVSTEP(5) PVSTEP(6) PVSTEP(7)
    PVSTEP(8) PVSTEP(9) PVSTEP(10) PVSTEP(11) PVSTEP(12) PVSTEP(13) PVSTEP(14) PVSTEP(15)
#undef PVSTEP
    red[sw * 64 + lane] = oacc;
    LGKM0();
    if (lane == 0) LDS_INC(flg + 4);
    if (ffn) {
      for (unsigned sp = 0; sp < LSPIN_CAP && lds_flag(flg + 2) == 0u; ++sp) __builtin_amdgcn_s_sleep(1);
      CBAR();
      const u32x2 hv = *(const u32x2*)(hs + (c >> 3) * 64 + 8 * sw + 2 * r);
      f32x4 facc = {0.f, 0.f, 0.f, 0.f};
      facc = mfma(mask & hv[0], W2[0], facc);
      facc = mfma(mask & hv[1], W2[1], facc);
      red[(8 + sw) * 64 + lane] = facc;
      LGKM0();
      if (lane == 0) LDS_INC(flg + 3);
    }
    if (c < 10) {
#pragma unroll
      for (int i = 0; i < 4; ++i) *(u32x4*)(smem + L_WL + (16 * sw + 4 * r + i) * 160 + c * 16) = WO[i];
    }
    const int tro = L_WL + (8 * r + ((lane >> 2) & 3)) * 160 + sw * 32 + (lane & 3) * 8;
    const unsigned yoff = lane < 16 ? cu * 1280 + (unsigned)(sw * 256 + lane * 16) : OOB;
    wg_barrier();
    if (sw < 5) {
      u32x4 A[4], B[4];
#pragma unroll
      for (int t = 0; t < 4; ++t) {
        A[t] = *(const u32x4*)(os + (c & 1) * 64 + t * 16 + r * 4);
        const u32x2 lo = trread(smem + tro + t * 5120), hi = trread(smem + tro + t * 5120 + 640);
        B[t] = u32x4{lo[0], lo[1], hi[0], hi[1]};
      }
      f32x4 y = {0.f, 0.f, 0.f, 0.f}, y2 = {0.f, 0.f, 0.f, 0.f};
      y = mfma(A[0], B[0], y); y2 = mfma(A[1], B[1], y2);
      y = mfma(A[2], B[2], y); y2 = mfma(A[3], B[3], y2);
      ST128(gran2(y[0] + y2[0], y[1] + y2[1], tagB), rws, yoff, WS_Y, AUX_SC1);
    }
  } else {
    __builtin_amdgcn_s_setprio(3);
    const bool isq = (w == 0);
    const unsigned epoch = __builtin_amdgcn_raw_buffer_load_b32(rws, 0, WS_EPOCH, AUX_SC1);
    u32x2 xs[5];
#pragma unroll
    for (int i = 0; i < 5; ++i) xs[i] = __builtin_amdgcn_raw_buffer_load_b64(rx, (lane >> 5) * 1280 + (lane & 31) * 8, i * 256, 0);
    const int row0 = isq ? 160 * (j >> 1) : 80 * j;
    const int nrow = isq ? 160 : 80;
    unsigned short xr[5];
#pragma unroll
    for (int i = 0; i < 5; ++i) xr[i] = __builtin_amdgcn_raw_buffer_load_b16(rx, (lane & 31) + 32 * i < nrow ? (unsigned)((lane >> 5) * 1280 + (lane & 31) * 2) : OOB, (row0 + 32 * i) * 2, 0);
    u32x4 wa[20];
    {
      const __amdgpu_buffer_rsrc_t rw = isq ? RSRC(wq + h * 128 + (j & 1) * 64, 640 * 4096 * 2 - h * 256 - (j & 1) * 128) : RSRC(w1 + h * 64, ffn ? 640 * 1728 * 2 - h * 128 : 0);
      const int rstride = isq ? 8192 : 3456;
      const unsigned vo = (unsigned)((lane >> 3) * rstride + (lane & 7) * 16);
#pragma unroll
      for (int i = 0; i < 10; ++i) wa[i] = LD128(rw, vo, (row0 + 8 * i) * rstride, AUX_NT);
      if (isq) {
#pragma unroll
        for (int i = 10; i < 20; ++i) wa[i] = LD128(rw, vo, (row0 + 8 * i) * rstride, AUX_NT);
      }
    }
    if (isq && lane < 24) LDS_ST32(st + lane, 0u);
    wg_barrier();
    float xf[20];
#pragma unroll
    for (int i = 0; i < 5; ++i) { xf[4 * i] = bflo(xs[i][0]); xf[4 * i + 1] = bfhi(xs[i][0]); xf[4 * i + 2] = bflo(xs[i][1]); xf[4 * i + 3] = bfhi(xs[i][1]); }
    float sum = 0.f;
#pragma unroll
    for (int i = 0; i < 20; ++i) sum += xf[i];
    sum = x16_sum(row_sum(sum));
    const float mean = sum * (1.f / 640.f);
    float vs = 0.f;
#pragma unroll
    for (int i = 0; i < 20; ++i) { const float d = xf[i] - mean; vs = fmaf(d, d, vs); }
    vs = x16_sum(row_sum(vs));
    const float mb = rbf(mean), den = rbf(sqrtf(rbf(rbf(vs * (1.f / 640.f)) + 1e-5f)));
    unsigned* xw_ = xl + w * 320;
#pragma unroll
    for (int i = 0; i < 5; ++i) {
      const int row = (lane & 31) + 32 * i;
      const float xn = rbf(bflo(xr[i]) - mb) / den;
      if (row < nrow) xw_[(lane >> 5) * 160 + ((row >> 1) & 3) * 40 + (row >> 3) * 2 + (row & 1)] = pk_bf16(xn, xn);
    }
    const unsigned tag = __builtin_amdgcn_readfirstlane(epoch) + 0x9E3779B9u, tagB = tag ^ 0x80000000u;
    if (isq && lane == 0) LDS_ST32(flg, tag);
    LGKM0();
    f32x4 acc0 = {0.f, 0.f, 0.f, 0.f}, acc1 = {0.f, 0.f, 0.f, 0.f};
    const unsigned* xrd = xw_ + (c >> 3) * 160 + r * 40;
#pragma unroll
    for (int i = 0; i < 10; i += 2) {
      const u32x4 xa = *(const u32x4*)(xrd + 2 * i);
      acc0 = mfma(mask & xa[0], wa[i], acc0); acc1 = mfma(mask & xa[1], wa[i], acc1);
      acc0 = mfma(mask & xa[2], wa[i + 1], acc0); acc1 = mfma(mask & xa[3], wa[i + 1], acc1);
    }
    if (isq) {
#pragma unroll
      for (int i = 10; i < 20; i += 2) {
        const u32x4 xa = *(const u32x4*)(xrd + 2 * i);
        acc0 = mfma(mask & xa[0], wa[i], acc0); acc1 = mfma(mask & xa[1], wa[i], acc1);
        acc0 = mfma(mask & xa[2], wa[i + 1], acc0); acc1 = mfma(mask & xa[3], wa[i + 1], acc1);
      }
    }
    f32x4 v;
#pragma unroll
    for (int i = 0; i < 4; ++i) { const float t_ = c < 8 ? acc0[i] : acc1[i]; v[i] = t_ + dppf<ROR8>(t_); }
    const u32x4 pub = gran2(c < 8 ? v[0] : v[2], c < 8 ? v[1] : v[3], tag);
    if (isq) {
      ST128(pub, rws, cu * 1024 + lane * 16, WS_Q1, AUX_SC1);
      __builtin_amdgcn_s_sleep(PDELAY);
      const unsigned goff = (unsigned)((h * 8 + (lane >> 5)) * 1024 + (b * 32 + (lane & 31)) * 16);
      u32x4 G[8];
#pragma unroll
      for (int p = 0; p < 4; ++p) G[p] = LD128(rws, goff, WS_Q1 + p * 2048, AUX_SC1);
      for (unsigned sp = 0;; ++sp) {
        bool allok = true;
        CBAR();
#pragma unroll
        for (int p = 0; p < 4; ++p) if (!__all(gok(G[p], tag))) { allok = false; G[p] = LD128(rws, goff, WS_Q1 + p * 2048, AUX_SC1); }
        if (allok || sp > SPIN_CAP) break;
      }
      qs[(lane >> 5) * 32 + (c & 7) * 4 + (r & 1) * 2 + (c >> 3)] = pk_bf16((gv0(G[0]) + gv0(G[1])) + (gv0(G[2]) + gv0(G[3])), (gv1(G[0]) + gv1(G[1])) + (gv1(G[2]) + gv1(G[3])));
      ST128(gran2(0.f, 0.f, tag), rws, lane == 0 ? cu * 128 + 16 : OOB, WS_S, AUX_SC1);
      LGKM0();
      if (lane == 0) LDS_ST32(flg + 1, 1u);
      const unsigned soff = WS_S + (unsigned)((h * 8 + (lane & 7)) * 128 + (lane & 8) * 2);
      __builtin_amdgcn_s_sleep(ACKDELAY);
      u32x4 gs = LD128(rws, soff, 0, AUX_SC1);
      unsigned long long t_ = LDS_LD64(st + 2 * (lane & 7));
      for (unsigned sp = 0; !__all((unsigned)(t_ >> 32) != 0u) && sp < LSPIN_CAP; ++sp) { __builtin_amdgcn_s_sleep(2); t_ = LDS_LD64(st + 2 * (lane & 7)); }
      {
        const float m_ = __uint_as_float((unsigned)t_), l_ = __uint_as_float((unsigned)(t_ >> 32));
        const float M = oct_max(m_);
        float L = __builtin_amdgcn_exp2f((m_ - M) * LOG2E) * l_; L += dppf<XOR1>(L); L += dppf<XOR2>(L); L += dppf<XOR7>(L);
        ST128(gran2(M, L, tag), rws, lane == 0 ? cu * 128 : OOB, WS_S, AUX_SC1);
      }
      if (!__all(gok(gs, tag) | !(lane & 8))) {
        for (unsigned sp = 0; sp < SPIN_CAP; ++sp) {
          CBAR();
          gs = LD128(rws, soff, 0, AUX_SC1);
          if (__all(gok(gs, tag) | !(lane & 8))) break;
        }
      }
      for (unsigned sp = 0; sp < LSPIN_CAP && lds_flag(flg + 4) != 8u; ++sp) {}
      CBAR();
      const unsigned ooff = (unsigned)(h * 8 * 1024 + lane * 16);
      const f32x2 own = sum8((const float*)red + (((lane >> 1) & 1) * 16 + (lane >> 2)) * 4 + 2 * (lane & 1));
      const u32x4 ownG = gran2(own[0], own[1], tagB);
      ST128(ownG, rws, ooff, WS_O + j * 1024, AUX_SC1);
      gs = LD128(rws, soff, 0, AUX_SC1);
#pragma unroll
      for (int p = 0; p < 8; ++p) G[p] = LD128(rws, p == j ? OOB : ooff, WS_O + p * 1024, AUX_SC1);
#pragma unroll
      for (int p = 0; p < 8; ++p) if (p == j) G[p] = ownG;
      for (unsigned sp = 0; !__all(gok(gs, tag)) && sp < SPIN_CAP; ++sp) { CBAR(); __builtin_amdgcn_s_sleep(2); gs = LD128(rws, soff, 0, AUX_SC1); }
      float wp[8];
      {
        const float ms = gv0(gs), ls = gv1(gs);
        float Mg = fmaxf(ms, dppf<XOR1>(ms)); Mg = fmaxf(Mg, dppf<XOR2>(Mg));
        const float e_ = __builtin_amdgcn_exp2f((ms - Mg) * LOG2E);
        float Lg = e_ * ls; Lg += dppf<XOR1>(Lg); Lg += dppf<XOR2>(Lg);
        const float wgt = e_ / Lg;
#pragma unroll
        for (int p = 0; p < 8; ++p) wp[p] = __uint_as_float(__builtin_amdgcn_readlane(__float_as_uint(wgt), p));
      }
      for (unsigned sp = 0;; ++sp) {
        bool allok = true;
        CBAR();
#pragma unroll
        for (int p = 0; p < 8; ++p) if (!__all(gok(G[p], tagB))) { allok = false; G[p] = LD128(rws, ooff, WS_O + p * 1024, AUX_SC1); }
        if (allok || sp > SPIN_CAP) break;
      }
#pragma unroll
      for (int bb = 0; bb < 2; ++bb) {
        float o0 = 0.f, o1 = 0.f;
#pragma unroll
        for (int sp = 0; sp < 4; ++sp) { o0 = fmaf(wp[4 * bb + sp], gv0(G[4 * bb + sp]), o0); o1 = fmaf(wp[4 * bb + sp], gv1(G[4 * bb + sp]), o1); }
        os[bb * 64 + lane] = pk_bf16(o0, o1);
      }
      wg_barrier();
    } else {
      if (ffn) {
        ST128(pub, rws, (unsigned)((h * 8 + (c & 7)) * 1280 + j * 128 + (c >> 3) * 64 + r * 16), WS_H1, AUX_SC1);
        __builtin_amdgcn_s_sleep(2 * PDELAY);
        const unsigned goff = cu * 1280 + lane * 16;
        u32x4 G0 = LD128(rws, goff, WS_H1, AUX_SC1);
        for (unsigned sp = 0; !__all(gok(G0, tag)) && sp < SPIN_CAP; ++sp) { CBAR(); G0 = LD128(rws, goff, WS_H1, AUX_SC1); }
        float a0 = gv0(G0), a1 = gv1(G0);
        a0 += dppf<ROR8>(a0); a1 += dppf<ROR8>(a1);
        a0 = fmaxf(x32_sum(x16_sum(a0)), 0.f); a1 = fmaxf(x32_sum(x16_sum(a1)), 0.f);
        u32x2 g2; g2[0] = pk_bf16(a0, a1); g2[1] = tag;
        __builtin_amdgcn_raw_buffer_store_b64(g2, rws, lane < 8 ? cu * 128 + lane * 8 : OOB, WS_H2, AUX_SC1);
        const unsigned g2off = (unsigned)((h * 8 + (lane >> 3)) * 128 + (lane & 7) * 8);
        __builtin_amdgcn_s_sleep(3 * PDELAY);
        u32x2 q2 = __builtin_amdgcn_raw_buffer_load_b64(rws, g2off, WS_H2, AUX_SC1);
        for (unsigned sp = 0; !__all(q2[1] == tag) && sp < SPIN_CAP; ++sp) { CBAR(); q2 = __builtin_amdgcn_raw_buffer_load_b64(rws, g2off, WS_H2, AUX_SC1); }
        u32x2 hv; hv[0] = (q2[0] & 0xffffu) | (q2[0] << 16); hv[1] = (q2[0] >> 16) | (q2[0] & 0xffff0000u);
        *(u32x2*)(hs + ((lane >> 1) & 1) * 64 + (lane >> 3) * 8 + (lane & 1) * 4 + ((lane >> 2) & 1) * 2) = hv;
        LGKM0();
        if (lane == 0) LDS_ST32(flg + 2, 1u);
        for (unsigned sp = 0; sp < LSPIN_CAP && lds_flag(flg + 3) != 8u; ++sp) __builtin_amdgcn_s_sleep(1);
        CBAR();
        f_publish(0, tag);
        f_publish(8, tag);
      }
      const int e = lane & 7, ocol = j * 80 + 8 * h + e;
      const bool fin = h < 10, f3v = (lane >> 3) < 3;
      const unsigned yo = (unsigned)(((lane >> 3) * 8 + j) * 1280 + h * 128 + e * 16);
      unsigned short x0 = 0, x1 = 0;
      u32x4 g[4], y[4];
      if (fin) { x0 = __builtin_amdgcn_raw_buffer_load_b16(rx, ocol * 2, 0, 0); x1 = __builtin_amdgcn_raw_buffer_load_b16(rx, ocol * 2, 1280, 0); }
      wg_barrier();
      if (fin) {
#pragma unroll
        for (int i = 0; i < 4; ++i) g[i] = LD128(rws, (i < 3 || f3v) ? yo : OOB, WS_F + i * 64 * 1280, AUX_SC1);
        __builtin_amdgcn_s_sleep(YDELAY);
#pragma unroll
        for (int i = 0; i < 4; ++i) y[i] = LD128(rws, yo, WS_Y + i * 64 * 1280, AUX_SC1);
        for (unsigned sp = 0;; ++sp) {
          bool allok = true;
          CBAR();
#pragma unroll
          for (int i = 0; i < 4; ++i) if (!__all(gok(g[i], tag) | (i == 3 && !f3v))) { allok = false; g[i] = LD128(rws, (i < 3 || f3v) ? yo : OOB, WS_F + i * 64 * 1280, AUX_SC1); }
          if (allok || sp > SPIN_CAP) break;
        }
        float fa = (gv0(g[0]) + gv0(g[1])) + (gv0(g[2]) + gv0(g[3])), fb = (gv1(g[0]) + gv1(g[1])) + (gv1(g[2]) + gv1(g[3]));
        fa += dppf<ROR8>(fa); fb += dppf<ROR8>(fb);
        fa = rbf(x32_sum(x16_sum(fa))); fb = rbf(x32_sum(x16_sum(fb)));
        const float ga = rbf(fa / (1.f + __expf(-fa))), gb = rbf(fb / (1.f + __expf(-fb)));
        for (unsigned sp = 0;; ++sp) {
          bool allok = true;
          CBAR();
#pragma unroll
          for (int i = 0; i < 4; ++i) if (!__all(gok(y[i], tagB))) { allok = false; y[i] = LD128(rws, yo, WS_Y + i * 64 * 1280, AUX_SC1); }
          if (allok || sp > SPIN_CAP) break;
        }
        float ya = (gv0(y[0]) + gv0(y[1])) + (gv0(y[2]) + gv0(y[3])), yb = (gv1(y[0]) + gv1(y[1])) + (gv1(y[2]) + gv1(y[3]));
        ya += dppf<ROR8>(ya); yb += dppf<ROR8>(yb);
        ya = rbf(x32_sum(x16_sum(ya))); yb = rbf(x32_sum(x16_sum(yb)));
        const unsigned vv = pk_bf16(bflo(x0) + rbf(ya * ga), bflo(x1) + rbf(yb * gb)), nb = dppu<XOR1>(vv);
        const unsigned res = (e & 1) ? ((nb >> 16) | (vv & 0xffff0000u)) : ((vv & 0xffffu) | (nb << 16));
        __builtin_amdgcn_raw_buffer_store_b32(res, RSRC(out, 2560), lane < 8 ? (unsigned)(((e & 1) * 640 + (ocol & ~1)) * 2) : OOB, 0, AUX_SC1);
        if (blk == 0 && lane == 0) __builtin_amdgcn_raw_buffer_store_b32(tag, rws, 0, WS_EPOCH, AUX_SC1);
      }
    }
  }
}

extern "C" void kernel_launch(void* const* d_in, const int* in_sizes, int n_in, void* d_out, int out_size, void* d_ws, size_t ws_size, hipStream_t stream) {
  fused_decode<<<256, 640, 0, stream>>>((const unsigned short*)d_in[0], (const unsigned short*)d_in[1], (const unsigned short*)d_in[2],
      (const unsigned short*)d_in[3], (const unsigned short*)d_in[4], (const unsigned short*)d_in[6],
      (unsigned char*)d_ws, (const unsigned short*)d_in[7], (unsigned short*)d_out);
}
```
